```python
import torch

def _softmax(x, axis=-1):
    return torch.softmax(x, dim=axis)
def _gelu(x):
    return torch.nn.functional.gelu(x, approximate="tanh")
def _silu(x):
    return torch.nn.functional.silu(x)
def _sigmoid(x):
    return torch.sigmoid(x)
def _layernorm(x, axis=-1, eps=1e-5):
    m = x.mean(dim=axis, keepdim=True)
    v = x.var(dim=axis, keepdim=True, unbiased=False)
    return (x - m) / torch.sqrt(v + eps)
def _scatter_add(tgt, idx, src, axis=0):
    return tgt.clone().index_add_(axis, idx, src)
def _segment_sum(src, seg, n):
    out = torch.zeros((n,) + src.shape[1:], dtype=src.dtype)
    return out.index_add_(0, seg, src)
def _pad(x, pw):
    flat = tuple(v for pair in reversed(pw) for v in pair)
    return torch.nn.functional.pad(x, flat)

kernel_name = 'concat_heads__gqa_block_cached__mha_block_cached__near_ridge'

def setup_inputs():

    _w = (lambda a: (lambda t: t.to(torch.bfloat16) if t.is_floating_point() else t)(torch.as_tensor(a)))
    def _A_setup():
        B, Sq, D = 8, 64, 512
        Hq, Hkv, Sk, Dh = 32, 8, 1024, 128
        assert Hq % Hkv == 0, (Hq, Hkv)
        return {
            "x": _w((lambda *s: torch.randn(*s, dtype=torch.bfloat16) * 0.8)(B, Sq, D)),
            "k_cache": _w((lambda *s: torch.randn(*s, dtype=torch.bfloat16) * 0.8)(B, Hkv, Sk, Dh)),
            "v_cache": _w((lambda *s: torch.randn(*s, dtype=torch.bfloat16) * 0.05)(B, Hkv, Sk, Dh)),
            "wq": _w((lambda *s: torch.randn(*s, dtype=torch.bfloat16) * 0.8)(D, Hq * Dh) * (D ** -0.5)),
            "wo": _w((lambda *s: torch.randn(*s, dtype=torch.bfloat16) * 0.05)(Hq * Dh, D) * ((Hq * Dh) ** -0.5)),
        }
    _A_in = _A_setup()

    def _B_setup():
        B, Sq, D = 8, 64, 512
        H, Sk, Dh = 32, 1024, 128
        return {
            "x": _w((lambda *s: torch.randn(*s, dtype=torch.bfloat16) * 0.8)(B, Sq, D)),
            "k_cache": _w((lambda *s: torch.randn(*s, dtype=torch.bfloat16) * 0.8)(B, H, Sk, Dh)),
            "v_cache": _w((lambda *s: torch.randn(*s, dtype=torch.bfloat16) * 0.05)(B, H, Sk, Dh)),
            "wq": _w((lambda *s: torch.randn(*s, dtype=torch.bfloat16) * 0.8)(D, H * Dh) * (D ** -0.5)),
            "wo": _w((lambda *s: torch.randn(*s, dtype=torch.bfloat16) * 0.05)(H * Dh, D) * ((H * Dh) ** -0.5)),
        }
    _B_in = _B_setup()

    return {
        "A_x": _A_in["x"],
        "A_k_cache": _A_in["k_cache"],
        "A_v_cache": _A_in["v_cache"],
        "A_wq": _A_in["wq"],
        "A_wo": _A_in["wo"],
        "B_x": _B_in["x"],
        "B_k_cache": _B_in["k_cache"],
        "B_v_cache": _B_in["v_cache"],
        "B_wq": _B_in["wq"],
        "B_wo": _B_in["wo"],
    }

def reference(A_x, A_k_cache, A_v_cache, A_wq, A_wo, B_x, B_k_cache, B_v_cache, B_wq, B_wo):
    def _A_ref(x, k_cache, v_cache, wq, wo):
        B, Sq, D = x.shape
        Hq, Dh = 32, 128
        Hkv = k_cache.shape[1]
        rep = Hq // Hkv
        q = (lambda x, *d: x.permute(*d))((x @ wq).reshape(B, Sq, Hq, Dh), 0, 2, 1, 3)
        k = (lambda x, n, axis: torch.repeat_interleave(x, n, dim=axis))(k_cache, rep, axis=1)
        v = (lambda x, n, axis: torch.repeat_interleave(x, n, dim=axis))(v_cache, rep, axis=1)
        scores = torch.einsum("bhqd,bhkd->bhqk", q, k) * (Dh ** -0.5)
        p = _softmax(scores, axis=-1)
        o = torch.einsum("bhqk,bhkd->bhqd", p, v)
        return (lambda x, *d: x.permute(*d))(o, 0, 2, 1, 3).reshape(B, Sq, Hq * Dh) @ wo

    def _B_ref(x, k_cache, v_cache, wq, wo):
        B, Sq, D = x.shape
        H, Dh = 32, 128
        q = (lambda x, *d: x.permute(*d))((x @ wq).reshape(B, Sq, H, Dh), 0, 2, 1, 3)
        scores = torch.einsum("bhqd,bhkd->bhqk", q, k_cache) * (Dh ** -0.5)
        p = _softmax(scores, axis=-1)
        o = torch.einsum("bhqk,bhkd->bhqd", p, v_cache)
        return (lambda x, *d: x.permute(*d))(o, 0, 2, 1, 3).reshape(B, Sq, H * Dh) @ wo

    a = _A_ref(A_x, A_k_cache, A_v_cache, A_wq, A_wo)
    b = _B_ref(B_x, B_k_cache, B_v_cache, B_wq, B_wo)
    return torch.concatenate([a, b], axis=-1)
```

```cpp
#include <hip/hip_runtime.h>
#include <hip/hip_bf16.h>

typedef float    f32x16 __attribute__((ext_vector_type(16)));
typedef float    f32x4  __attribute__((ext_vector_type(4)));
typedef float    f32x2  __attribute__((ext_vector_type(2)));
typedef __bf16   bf16x8 __attribute__((ext_vector_type(8)));
typedef __bf16   bf16x2 __attribute__((ext_vector_type(2)));
typedef short    s16x4  __attribute__((ext_vector_type(4)));
typedef unsigned u32x4  __attribute__((ext_vector_type(4)));
typedef unsigned u32x2  __attribute__((ext_vector_type(2)));
#define DI __device__ __forceinline__
#define LAS __attribute__((address_space(3)))
#define SBAR() __builtin_amdgcn_sched_barrier(0)
#define PIN(x) asm volatile("" : "+v"(x))
#define MFMA32(a, b, c) __builtin_amdgcn_mfma_f32_32x32x16_bf16(a, b, c, 0, 0, 0)
#define MFMA16(a, b, c) __builtin_amdgcn_mfma_f32_16x16x32_bf16(a, b, c, 0, 0, 0)

constexpr int SLOT = 8192, NSLOT = 5, RING = SLOT * NSLOT;
constexpr int K1_LDS = 4 * RING;
constexpr float C2 = 0.08838834764831845f * 1.4426950408889634f;
constexpr float THR = 8.0f;

DI unsigned rfl(unsigned x) { return __builtin_amdgcn_readfirstlane(x); }
DI unsigned cvtpk(float lo, float hi) { f32x2 v = {lo, hi}; bf16x2 b = __builtin_convertvector(v, bf16x2); return __builtin_bit_cast(unsigned, b); }
DI bf16x8 lds_rd128(unsigned a) { return *(const LAS bf16x8*)(unsigned long)a; }
DI f32x4 lds_rdf4(unsigned a) { return *(const LAS f32x4*)(unsigned long)a; }
DI void lds_wrf4(unsigned a, f32x4 v) { *(LAS f32x4*)(unsigned long)a = v; }
DI void lds_wru4(unsigned a, u32x4 v) { *(LAS u32x4*)(unsigned long)a = v; }
DI s16x4 lds_tr(unsigned a) { return __builtin_amdgcn_ds_read_tr16_b64_v4i16((LAS s16x4*)(unsigned long)a); }
DI bf16x8 cat8(s16x4 lo, s16x4 hi) { return __builtin_bit_cast(bf16x8, __builtin_shufflevector(lo, hi, 0, 1, 2, 3, 4, 5, 6, 7)); }
DI float swap_max(float m) { auto rr = __builtin_amdgcn_permlane32_swap(__float_as_uint(m), __float_as_uint(m), false, false); return __builtin_fmaxf(__uint_as_float(rr[0]), __uint_as_float(rr[1])); }
DI float swap_add(float m) { auto rr = __builtin_amdgcn_permlane32_swap(__float_as_uint(m), __float_as_uint(m), false, false); return __uint_as_float(rr[0]) + __uint_as_float(rr[1]); }
#define MX3(a, b, c) __builtin_fmaxf(__builtin_fmaxf((a), (b)), (c))
DI float max16(const f32x16& p) {
  float a = MX3(p[0], p[1], p[2]), b = MX3(p[3], p[4], p[5]);
  a = MX3(a, p[6], p[7]); b = MX3(b, p[8], p[9]); a = MX3(a, p[10], p[11]); b = MX3(b, p[12], p[13]); a = MX3(a, p[14], p[15]);
  return __builtin_fmaxf(a, b);
}

template <int OFF> DI void dma2(unsigned m0v, unsigned vo0, unsigned vo1, __amdgpu_buffer_rsrc_t srd, unsigned soff) {
  asm volatile("s_mov_b32 m0, %0\n\ts_nop 0\n\t"
               "buffer_load_dwordx4 %1, %3, %4 offen offset:%5 nt lds\n\t"
               "buffer_load_dwordx4 %2, %3, %4 offen offset:%6 nt lds"
               :: "s"(m0v), "v"(vo0), "v"(vo1), "s"(srd), "s"(soff), "i"(OFF), "i"(OFF + 1024) : "memory");
}
#define WAITV(N) asm volatile("s_waitcnt vmcnt(" #N ")" ::: "memory")
#define WAITVL(N) asm volatile("s_waitcnt vmcnt(" #N ") lgkmcnt(0)" ::: "memory")
#define BARRIER() asm volatile("s_waitcnt lgkmcnt(0)\n\ts_barrier" ::: "memory")

struct P1 {
  const char *xA, *kA, *vA, *wqA, *xB, *kB, *vB, *wqB;
  char* o;
};

__global__ __launch_bounds__(256, 1) void k1_attn(P1 p) {
  extern __shared__ __attribute__((aligned(16))) char lds[];
  const unsigned tid = threadIdx.x, lane = tid & 63u, wid = rfl(tid >> 6);
  const unsigned g8 = blockIdx.x & 7u, sidx = blockIdx.x >> 3;
  const unsigned h = 4u * g8 + (sidx & 3u), b = sidx >> 2;
  const bool isB = wid < 2u;
  const unsigned half = wid & 1u;
  const unsigned ldsb = (unsigned)(unsigned long)lds;
  const unsigned ring = ldsb + wid * RING, pring = ldsb + (wid ^ 1u) * RING;
  const unsigned l31 = lane & 31u, hh = lane >> 5, l15 = lane & 15u, g4 = lane >> 4;

  const size_t kvoff = isB ? ((size_t)(b * 32u + h) * 1024u + half * 512u) * 256u : ((size_t)(b * 8u + g8) * 1024u + half * 512u) * 256u;
  const char* Kp = (isB ? p.kB : p.kA) + kvoff;
  const char* Vp = (isB ? p.vB : p.vA) + kvoff;
  const __amdgpu_buffer_rsrc_t sK = __builtin_amdgcn_make_buffer_rsrc((void*)Kp, (short)0, 512 * 256, 0x00020000);
  const __amdgpu_buffer_rsrc_t sV = __builtin_amdgcn_make_buffer_rsrc((void*)Vp, (short)0, 512 * 256, 0x00020000);
  unsigned kvo[4];
#pragma unroll
  for (int i = 0; i < 4; ++i) kvo[i] = g4 * 256u + (((l15 ^ g4) ^ (4u * i)) * 16u);
  const unsigned vvo = g4 * 256u + ((l15 ^ (g4 << 2)) * 16u);
#define DMA_K(q, tile, so) do { if (q == 0) dma2<0>(ring + (so), kvo[0], kvo[1], sK, (tile) * 8192u); else if (q == 1) dma2<2048>(ring + (so), kvo[2], kvo[3], sK, (tile) * 8192u); \
    else if (q == 2) dma2<0>(ring + (so) + 4096u, kvo[0], kvo[1], sK, (tile) * 8192u + 4096u); else dma2<2048>(ring + (so) + 4096u, kvo[2], kvo[3], sK, (tile) * 8192u + 4096u); } while (0)
#define DMA_V(q, tile, so) do { if (q == 0) dma2<0>(ring + (so), vvo, vvo, sV, (tile) * 8192u); else if (q == 1) dma2<2048>(ring + (so), vvo, vvo, sV, (tile) * 8192u); \
    else if (q == 2) dma2<0>(ring + (so) + 4096u, vvo, vvo, sV, (tile) * 8192u + 4096u); else dma2<2048>(ring + (so) + 4096u, vvo, vvo, sV, (tile) * 8192u + 4096u); } while (0)
#define DMA_K_ALL(tile, so) do { DMA_K(0, tile, so); DMA_K(1, tile, so); DMA_K(2, tile, so); DMA_K(3, tile, so); } while (0)
#define DMA_V_ALL(tile, so) do { DMA_V(0, tile, so); DMA_V(1, tile, so); DMA_V(2, tile, so); DMA_V(3, tile, so); } while (0)

  bf16x8 qf[2][8];
  {
    const char* wq = (isB ? p.wqB : p.wqA) + (size_t)(256u * half + 8u * g4) * 8192u + h * 256u + l15 * 16u;
    const char* xp = (isB ? p.xB : p.xA) + (size_t)(b * 64u + l15) * 1024u + (256u * half + 8u * g4) * 2u;
    const unsigned xq0 = half * 32768u, xq2 = 32768u - xq0;
    u32x4 w[4][8]; bf16x8 xf[4][4];
    f32x4 acc[4][8];
#pragma unroll
    for (int qb = 0; qb < 4; ++qb)
#pragma unroll
      for (int e = 0; e < 8; ++e) acc[qb][e] = f32x4{0.f, 0.f, 0.f, 0.f};
#define QLOAD(s) do { _Pragma("unroll") for (int j = 0; j < 8; ++j) w[(s) & 3][j] = __builtin_nontemporal_load((const u32x4*)(wq + (size_t)(32 * (s) + j) * 8192u)); \
      _Pragma("unroll") for (int qb = 0; qb < 4; ++qb) xf[(s) & 3][qb] = *(const bf16x8*)(xp + ((qb & 2) ? xq2 : xq0) + (qb & 1) * 16384 + (s) * 64); } while (0)
    QLOAD(0); QLOAD(1); QLOAD(2); QLOAD(3);
    SBAR();
    DMA_K_ALL(0u, 0u); DMA_K_ALL(1u, 1u * SLOT); DMA_V_ALL(0u, 2u * SLOT);
    SBAR();
#pragma unroll
    for (int s = 0; s < 8; ++s) {
#pragma unroll
      for (int e = 0; e < 8; ++e) {
        u32x4 a;
#pragma unroll
        for (int jj = 0; jj < 4; ++jj)
          a[jj] = __builtin_amdgcn_perm(w[s & 3][2 * jj + 1][e >> 1], w[s & 3][2 * jj][e >> 1], (e & 1) ? 0x07060302u : 0x05040100u);
        const bf16x8 af = __builtin_bit_cast(bf16x8, a);
#pragma unroll
        for (int qb = 0; qb < 4; ++qb) acc[qb][e] = MFMA16(af, xf[s & 3][qb], acc[qb][e]);
      }
      SBAR();
      if (s < 4) { QLOAD(s + 4); }
      SBAR();
    }
#undef QLOAD
    const unsigned xo = ring + 3u * SLOT + lane * 16u, xi = pring + 3u * SLOT + lane * 16u;
#pragma unroll
    for (int q2 = 0; q2 < 2; ++q2)
#pragma unroll
      for (int e = 0; e < 8; ++e) lds_wrf4(xo + (q2 * 8 + e) * 1024u, acc[2 + q2][e]);
    BARRIER();
    f32x4 fin[2][8];
#pragma unroll
    for (int q2 = 0; q2 < 2; ++q2)
#pragma unroll
      for (int e = 0; e < 8; ++e) fin[q2][e] = lds_rdf4(xi + (q2 * 8 + e) * 1024u) + acc[q2][e];
    asm volatile("s_waitcnt lgkmcnt(0)" ::: "memory");
#pragma unroll
    for (int q2 = 0; q2 < 2; ++q2)
#pragma unroll
      for (int r = 0; r < 4; ++r) {
        const unsigned row = 16u * q2 + l15, ch = 4u * g4 + r;
        u32x4 v = {cvtpk(fin[q2][0][r], fin[q2][1][r]), cvtpk(fin[q2][2][r], fin[q2][3][r]), cvtpk(fin[q2][4][r], fin[q2][5][r]), cvtpk(fin[q2][6][r], fin[q2][7][r])};
        lds_wru4(pring + 4u * SLOT + row * 256u + ((ch ^ l15) * 16u), v);
      }
    BARRIER();
    DMA_K_ALL(2u, 3u * SLOT);
    const unsigned qlane = l31 * 256u + ((hh ^ l15) * 16u) + 4u * SLOT;
    const unsigned q0b = pring + qlane, q1b = ring + qlane;
#pragma unroll
    for (int s = 0; s < 8; ++s) { qf[0][s] = lds_rd128(q0b ^ (32u * s)); qf[1][s] = lds_rd128(q1b ^ (32u * s)); }
    BARRIER();
    DMA_V_ALL(1u, 4u * SLOT);
  }

  const unsigned kb = ring + l31 * 256u + ((hh ^ l15) * 16u);
  const unsigned vb = ring + (4u * hh + ((lane >> 2) & 3u)) * 256u + (((lane >> 2) & 3u) << 6) + (g4 & 1u) * 32u + (lane & 3u) * 8u;
  f32x16 o[4][2];
#pragma unroll
  for (int db = 0; db < 4; ++db) { o[db][0] = f32x16{}; o[db][1] = f32x16{}; }
  f32x16 sA[2], sB[2];
  bf16x8 kf[8]; s16x4 vlo[4][2], vhi[4][2]; u32x4 pw[2][2];
  float mhat[2], lsum[2] = {0.f, 0.f};
  const f32x16 zero16 = f32x16{};

  WAITV(32);
#pragma unroll
  for (int s = 0; s < 8; ++s) kf[s] = lds_rd128(kb ^ (32u * s));
  sA[0] = MFMA32(kf[0], qf[0][0], zero16); sA[1] = MFMA32(kf[0], qf[1][0], zero16);
#pragma unroll
  for (int s = 1; s < 8; ++s) { sA[0] = MFMA32(kf[s], qf[0][s], sA[0]); sA[1] = MFMA32(kf[s], qf[1][s], sA[1]); }
  WAITVL(24);
  DMA_K_ALL(3u, 0u);
#pragma unroll
  for (int s = 0; s < 8; ++s) kf[s] = lds_rd128((kb + SLOT) ^ (32u * s));
  mhat[0] = swap_max(max16(sA[0])) * C2; mhat[1] = swap_max(max16(sA[1])) * C2;

  unsigned so1 = 1u * SLOT, so2 = 2u * SLOT, so3 = 3u * SLOT;
#define ADV2(x) x = (x >= 3u * SLOT) ? x - 3u * SLOT : x + 2u * SLOT
#define EX(v, nm) __builtin_amdgcn_exp2f(__builtin_fmaf((v), C2, nm))
#define VRD(db, ks) do { const unsigned a_ = (va ^ (64u * (db))) + 4096u * (ks); vlo[db][ks] = lds_tr(a_); vhi[db][ks] = lds_tr(a_ + 2048u); } while (0)
#define SMX(C, QB, i) do { const float e0_ = EX(C[QB][2 * (i)], nm##QB), e1_ = EX(C[QB][2 * (i) + 1], nm##QB); ls##QB += e0_; lt##QB += e1_; \
    pw[QB][(i) >> 2][(i) & 3] = cvtpk(e0_, e1_); } while (0)
#define GAPA(C, N, s, QB, t) do { \
    if ((s) == 0) N[QB] = MFMA32(kf[0], qf[QB][0], zero16); else N[QB] = MFMA32(kf[s], qf[QB][s], N[QB]); \
    SMX(C, QB, s); \
    if (QB == 0) { VRD(((s) & 3), ((s) >> 2)); } \
    if (QB == 1 && ((s) & 1) == 0) { DMA_V(((s) >> 1), (t) + 2u, so1); } \
    PIN(pw[QB][(s) >> 2]); PIN(ls##QB); PIN(lt##QB); SBAR(); } while (0)
#define PHASE_A(C, N, t) do { \
    WAITVL(24); SBAR(); \
    const unsigned va = vb + so2; \
    const float nm0 = -mhat[0], nm1 = -mhat[1]; float ls0 = 0.f, lt0 = 0.f, ls1 = 0.f, lt1 = 0.f; \
    GAPA(C, N, 0, 0, t); GAPA(C, N, 0, 1, t); GAPA(C, N, 1, 0, t); GAPA(C, N, 1, 1, t); GAPA(C, N, 2, 0, t); GAPA(C, N, 2, 1, t); GAPA(C, N, 3, 0, t); GAPA(C, N, 3, 1, t); \
    GAPA(C, N, 4, 0, t); GAPA(C, N, 4, 1, t); GAPA(C, N, 5, 0, t); GAPA(C, N, 5, 1, t); GAPA(C, N, 6, 0, t); GAPA(C, N, 6, 1, t); GAPA(C, N, 7, 0, t); GAPA(C, N, 7, 1, t); \
    lsum[0] += ls0 + lt0; lsum[1] += ls1 + lt1; } while (0)
#define PVM(db, ks, QB) o[db][QB] = MFMA32(cat8(vlo[db][ks], vhi[db][ks]), __builtin_bit_cast(bf16x8, pw[QB][ks]), o[db][QB])
#define GAPB(i, t) do { \
    PVM(((i) >> 1) & 3, (i) >> 3, (i) & 1); \
    if ((i) < 8) kf[(i) & 7] = lds_rd128(ka ^ (32u * ((i) & 7))); \
    if (((i) & 3) == 1) { DMA_K(((i) >> 2), (t) + 4u, so2); } \
    SBAR(); } while (0)
#define PHASE_B(N, t) do { \
    WAITVL(24); SBAR(); \
    const unsigned ka = kb + so3; \
    GAPB(0, t); GAPB(1, t); GAPB(2, t); GAPB(3, t); GAPB(4, t); GAPB(5, t); GAPB(6, t); GAPB(7, t); \
    float mx0 = max16(N[0]); PIN(mx0); \
    GAPB(8, t); GAPB(9, t); \
    float mx1 = max16(N[1]); PIN(mx1); \
    GAPB(10, t); GAPB(11, t); \
    const float rm0 = __builtin_fmaf(swap_max(mx0), C2, -mhat[0]), rm1 = __builtin_fmaf(swap_max(mx1), C2, -mhat[1]); \
    const bool grew = __any((rm0 > THR) | (rm1 > THR)); \
    GAPB(12, t); GAPB(13, t); GAPB(14, t); GAPB(15, t); \
    if (__builtin_expect(grew, 0)) { \
      const float d0 = __builtin_fmaxf(rm0, 0.f), d1 = __builtin_fmaxf(rm1, 0.f); mhat[0] += d0; mhat[1] += d1; \
      const float f0 = __builtin_amdgcn_exp2f(-d0), f1 = __builtin_amdgcn_exp2f(-d1); lsum[0] *= f0; lsum[1] *= f1; \
      _Pragma("unroll") for (int db = 0; db < 4; ++db) { o[db][0] *= f0; o[db][1] *= f1; } \
    } \
    ADV2(so1); ADV2(so2); ADV2(so3); } while (0)

  for (unsigned t = 0; t < 14u; t += 2u) {
    PHASE_A(sA, sB, t);      PHASE_B(sB, t);
    PHASE_A(sB, sA, t + 1u); PHASE_B(sA, t + 1u);
  }
  PHASE_A(sA, sB, 14u); PHASE_B(sB, 14u);
  {
    const float nm0 = -mhat[0], nm1 = -mhat[1]; float ls0 = 0.f, lt0 = 0.f, ls1 = 0.f, lt1 = 0.f;
#pragma unroll
    for (int i = 0; i < 8; ++i) { SMX(sB, 0, i); SMX(sB, 1, i); }
    lsum[0] += ls0 + lt0; lsum[1] += ls1 + lt1;
    PIN(pw[0][0]); PIN(pw[0][1]); PIN(pw[1][0]); PIN(pw[1][1]);
    SBAR();
    WAITV(0);
    const unsigned va = vb + so2;
#pragma unroll
    for (int i = 0; i < 8; ++i) VRD(i & 3, i >> 2);
#pragma unroll
    for (int i = 0; i < 16; ++i) PVM((i >> 1) & 3, i >> 3, i & 1);
  }
  lsum[0] = swap_add(lsum[0]); lsum[1] = swap_add(lsum[1]);
  asm volatile("s_waitcnt lgkmcnt(0)" ::: "memory");
  {
    const unsigned xo = ring + lane * 16u, xi = pring + lane * 16u;
#pragma unroll
    for (int db = 0; db < 4; ++db)
#pragma unroll
      for (int r4 = 0; r4 < 4; ++r4) {
        f32x4 v;
#pragma unroll
        for (int e = 0; e < 4; ++e) v[e] = o[db][1][4 * r4 + e];
        lds_wrf4(xo + (db * 4 + r4) * 1024u, v);
      }
    { f32x4 v = {mhat[1], lsum[1], 0.f, 0.f}; lds_wrf4(xo + 16u * 1024u, v); }
    BARRIER();
    const f32x4 ml = lds_rdf4(xi + 16u * 1024u);
    const float m0 = mhat[0], l0 = lsum[0];
    const float mm = __builtin_fmaxf(m0, ml[0]);
    float a0 = __builtin_amdgcn_exp2f(m0 - mm), a1 = __builtin_amdgcn_exp2f(ml[0] - mm);
    const float inv = 1.0f / (a0 * l0 + a1 * ml[1]);
    a0 *= inv; a1 *= inv;
    char* op = p.o + ((size_t)(((isB ? 1u : 0u) * 8u + b) * 32u + h) * 2u + half) * 8192u + lane * 16u;
#pragma unroll
    for (int db = 0; db < 4; ++db)
#pragma unroll
      for (int ks = 0; ks < 2; ++ks) {
        const f32x4 x0 = lds_rdf4(xi + (db * 4 + 2 * ks) * 1024u), x1 = lds_rdf4(xi + (db * 4 + 2 * ks + 1) * 1024u);
        float y[8];
#pragma unroll
        for (int e = 0; e < 4; ++e) {
          y[e] = a0 * o[db][0][8 * ks + e] + a1 * x0[e];
          y[4 + e] = a0 * o[db][0][8 * ks + 4 + e] + a1 * x1[e];
        }
        u32x4 v = {cvtpk(y[0], y[1]), cvtpk(y[2], y[3]), cvtpk(y[4], y[5]), cvtpk(y[6], y[7])};
        *(u32x4*)(op + (db * 2 + ks) * 1024) = v;
      }
  }
}

constexpr int K2_LDS = 8 * 16384;
struct P2 { const char *o, *woA, *woB; char* out; };

template <int OFF> DI void dma2d(unsigned m0v, unsigned vo, __amdgpu_buffer_rsrc_t srd, unsigned soff0, unsigned soff1) {
  asm volatile("s_mov_b32 m0, %0\n\ts_nop 0\n\t"
               "buffer_load_dwordx4 %1, %2, %3 offen offset:%5 nt lds\n\t"
               "buffer_load_dwordx4 %1, %2, %4 offen offset:%6 nt lds"
               :: "s"(m0v), "v"(vo), "s"(srd), "s"(soff0), "s"(soff1), "i"(OFF), "i"(OFF + 1024) : "memory");
}

__global__ __launch_bounds__(512, 2) void k2_oproj(P2 p) {
  extern __shared__ __attribute__((aligned(16))) char lds[];
  const unsigned tid = threadIdx.x, lane = tid & 63u, wid = rfl(tid >> 6);
  const unsigned g8 = blockIdx.x & 7u, sidx = blockIdx.x >> 3;
  const unsigned br = g8 >> 2, nt = (g8 & 3u) * 4u + (sidx & 3u), b = sidx >> 2;
  const unsigned ldsb = (unsigned)(unsigned long)lds, wb = ldsb + wid * 16384u;
  const unsigned hh = lane >> 5;
  const char* wop = (br ? p.woB : p.woA) + (size_t)(512u * wid) * 1024u + nt * 64u;
  const __amdgpu_buffer_rsrc_t sW = __builtin_amdgcn_make_buffer_rsrc((void*)(wop - 4096), (short)0, 512 * 1024 + 4096, 0x00020000);
  const unsigned wvo = (lane >> 2) * 1024u + (lane & 3u) * 16u;
#define WDMA(hd, buf) do { \
    dma2d<0>(wb + (buf) * 8192u, wvo, sW, (hd) * 131072u + 4096u, (hd) * 131072u + 16384u + 3072u); \
    dma2d<2048>(wb + (buf) * 8192u, wvo, sW, (hd) * 131072u + 32768u + 2048u, (hd) * 131072u + 49152u + 1024u); \
    dma2d<0>(wb + (buf) * 8192u + 4096u, wvo, sW, (hd) * 131072u + 65536u + 4096u, (hd) * 131072u + 81920u + 3072u); \
    dma2d<2048>(wb + (buf) * 8192u + 4096u, wvo, sW, (hd) * 131072u + 98304u + 2048u, (hd) * 131072u + 114688u + 1024u); } while (0)
  const char* op = p.o + ((size_t)(br * 8u + b) * 32u + 4u * wid) * 16384u + lane * 16u;
  bf16x8 of[2][16];
#define OLOAD(hd, buf) do { _Pragma("unroll") for (int f = 0; f < 16; ++f) of[buf][f] = *(const bf16x8*)(op + (hd) * 16384 + f * 1024); } while (0)
  WDMA(0u, 0u); OLOAD(0, 0); WDMA(1u, 1u); OLOAD(1, 1);
  f32x16 acc[2] = {f32x16{}, f32x16{}};
  const unsigned tb = wb + (4u * hh + ((lane >> 2) & 3u)) * 64u + ((lane >> 4) & 1u) * 32u + (lane & 3u) * 8u;
#pragma unroll
  for (int hd = 0; hd < 4; ++hd) {
    const int buf = hd & 1;
    if (hd < 3) { WAITV(24); } else { WAITV(16); }
#pragma unroll
    for (int f = 0; f < 8; ++f) {
      const unsigned a = tb + buf * 8192u + f * 1024u;
      const bf16x8 wf = cat8(lds_tr(a), lds_tr(a + 512u));
      acc[0] = MFMA32(wf, of[buf][f], acc[0]);
      acc[1] = MFMA32(wf, of[buf][8 + f], acc[1]);
    }
    if (hd < 2) { asm volatile("s_waitcnt lgkmcnt(0)" ::: "memory"); SBAR(); WDMA((unsigned)hd + 2u, (unsigned)buf); OLOAD(hd + 2, buf); SBAR(); }
  }
  asm volatile("s_waitcnt lgkmcnt(0)" ::: "memory");
#pragma unroll
  for (int qb = 0; qb < 2; ++qb)
#pragma unroll
    for (int r4 = 0; r4 < 4; ++r4) {
      f32x4 v = {acc[qb][4 * r4], acc[qb][4 * r4 + 1], acc[qb][4 * r4 + 2], acc[qb][4 * r4 + 3]};
      lds_wrf4(wb + (qb * 4 + r4) * 1024u + lane * 16u, v);
    }
  BARRIER();
  f32x4 s = lds_rdf4(ldsb + wid * 1024u + lane * 16u);
#pragma unroll
  for (int w = 1; w < 8; ++w) s += lds_rdf4(ldsb + w * 16384u + wid * 1024u + lane * 16u);
  const unsigned qb = wid >> 2, r4 = wid & 3u;
  u32x2 v = {cvtpk(s[0], s[1]), cvtpk(s[2], s[3])};
  *(u32x2*)(p.out + (size_t)(b * 64u + qb * 32u + (lane & 31u)) * 2048u + (br * 512u + nt * 32u + 8u * r4 + 4u * hh) * 2u) = v;
}

extern "C" void kernel_launch(void* const* d_in, const int* in_sizes, int n_in,
                              void* d_out, int out_size, void* d_ws, size_t ws_size,
                              hipStream_t stream) {
  (void)in_sizes; (void)n_in; (void)out_size; (void)ws_size;
  (void)hipFuncSetAttribute((const void*)k1_attn, hipFuncAttributeMaxDynamicSharedMemorySize, K1_LDS);
  (void)hipFuncSetAttribute((const void*)k2_oproj, hipFuncAttributeMaxDynamicSharedMemorySize, K2_LDS);
  P1 p1;
  p1.xA = (const char*)d_in[0]; p1.kA = (const char*)d_in[1]; p1.vA = (const char*)d_in[2]; p1.wqA = (const char*)d_in[3];
  p1.xB = (const char*)d_in[5]; p1.kB = (const char*)d_in[6]; p1.vB = (const char*)d_in[7]; p1.wqB = (const char*)d_in[8];
  p1.o = (char*)d_ws;
  hipLaunchKernelGGL(k1_attn, dim3(256), dim3(256), K1_LDS, stream, p1);
  P2 p2;
  p2.o = (const char*)d_ws; p2.woA = (const char*)d_in[4]; p2.woB = (const char*)d_in[9]; p2.out = (char*)d_out;
  hipLaunchKernelGGL(k2_oproj, dim3(256), dim3(512), K2_LDS, stream, p2);
}
```

```cpp
#include <hip/hip_runtime.h>
#include <hip/hip_bf16.h>

typedef float    f32x16 __attribute__((ext_vector_type(16)));
typedef float    f32x4  __attribute__((ext_vector_type(4)));
typedef float    f32x2  __attribute__((ext_vector_type(2)));
typedef __bf16   bf16x8 __attribute__((ext_vector_type(8)));
typedef __bf16   bf16x2 __attribute__((ext_vector_type(2)));
typedef short    s16x4  __attribute__((ext_vector_type(4)));
typedef unsigned u32x4  __attribute__((ext_vector_type(4)));
typedef unsigned u32x2  __attribute__((ext_vector_type(2)));
#define DI __device__ __forceinline__
#define LAS __attribute__((address_space(3)))
#define SBAR() __builtin_amdgcn_sched_barrier(0)
#define PIN(x) asm volatile("" : "+v"(x))
#define MFMA32(a, b, c) __builtin_amdgcn_mfma_f32_32x32x16_bf16(a, b, c, 0, 0, 0)
#define MFMA16(a, b, c) __builtin_amdgcn_mfma_f32_16x16x32_bf16(a, b, c, 0, 0, 0)

constexpr int SLOT = 8192, NSLOT = 5, RING = SLOT * NSLOT;
constexpr int K1_LDS = 4 * RING;
constexpr float C2 = 0.08838834764831845f * 1.4426950408889634f;
constexpr float THR = 8.0f;

DI unsigned rfl(unsigned x) { return __builtin_amdgcn_readfirstlane(x); }
DI unsigned cvtpk(float lo, float hi) { f32x2 v = {lo, hi}; bf16x2 b = __builtin_convertvector(v, bf16x2); return __builtin_bit_cast(unsigned, b); }
DI bf16x8 lds_rd128(unsigned a) { return *(const LAS bf16x8*)(unsigned long)a; }
DI f32x4 lds_rdf4(unsigned a) { return *(const LAS f32x4*)(unsigned long)a; }
DI void lds_wrf4(unsigned a, f32x4 v) { *(LAS f32x4*)(unsigned long)a = v; }
DI void lds_wru4(unsigned a, u32x4 v) { *(LAS u32x4*)(unsigned long)a = v; }
DI s16x4 lds_tr(unsigned a) { return __builtin_amdgcn_ds_read_tr16_b64_v4i16((LAS s16x4*)(unsigned long)a); }
DI bf16x8 cat8(s16x4 lo, s16x4 hi) { return __builtin_bit_cast(bf16x8, __builtin_shufflevector(lo, hi, 0, 1, 2, 3, 4, 5, 6, 7)); }
DI float swap_max(float m) { auto rr = __builtin_amdgcn_permlane32_swap(__float_as_uint(m), __float_as_uint(m), false, false); return __builtin_fmaxf(__uint_as_float(rr[0]), __uint_as_float(rr[1])); }
DI float swap_add(float m) { auto rr = __builtin_amdgcn_permlane32_swap(__float_as_uint(m), __float_as_uint(m), false, false); return __uint_as_float(rr[0]) + __uint_as_float(rr[1]); }
#define MX3(a, b, c) __builtin_fmaxf(__builtin_fmaxf((a), (b)), (c))
DI float max16(const f32x16& p) {
  float a = MX3(p[0], p[1], p[2]), b = MX3(p[3], p[4], p[5]);
  a = MX3(a, p[6], p[7]); b = MX3(b, p[8], p[9]); a = MX3(a, p[10], p[11]); b = MX3(b, p[12], p[13]); a = MX3(a, p[14], p[15]);
  return __builtin_fmaxf(a, b);
}

template <int OFF> DI void dma2(unsigned m0v, unsigned vo0, unsigned vo1, __amdgpu_buffer_rsrc_t srd, unsigned soff) {
  asm volatile("s_mov_b32 m0, %0\n\ts_nop 0\n\t"
               "buffer_load_dwordx4 %1, %3, %4 offen offset:%5 nt lds\n\t"
               "buffer_load_dwordx4 %2, %3, %4 offen offset:%6 nt lds"
               :: "s"(m0v), "v"(vo0), "v"(vo1), "s"(srd), "s"(soff), "i"(OFF), "i"(OFF + 1024) : "memory");
}
#define WAITV(N) asm volatile("s_waitcnt vmcnt(" #N ")" ::: "memory")
#define WAITVL(N) asm volatile("s_waitcnt vmcnt(" #N ") lgkmcnt(0)" ::: "memory")
#define BARRIER() asm volatile("s_waitcnt lgkmcnt(0)\n\ts_barrier" ::: "memory")

#define STAMP(i)
#define STAMP_DECL(n)
#define STAMP_OUT(base, widx, id0, n)

struct P1 {
  const char *xA, *kA, *vA, *wqA, *xB, *kB, *vB, *wqB;
  char* o;
};

__global__ __launch_bounds__(256, 1) void k1_attn(P1 p) {
  extern __shared__ __attribute__((aligned(16))) char lds[];
  STAMP_DECL(8); STAMP(0);
  const unsigned tid = threadIdx.x, lane = tid & 63u, wid = rfl(tid >> 6);
  const unsigned g8 = blockIdx.x & 7u, sidx = blockIdx.x >> 3;
  const unsigned h = 4u * g8 + (sidx & 3u), b = sidx >> 2;
  const bool isB = wid < 2u;
  const unsigned half = wid & 1u;
  const unsigned ldsb = (unsigned)(unsigned long)lds;
  const unsigned ring = ldsb + wid * RING, pring = ldsb + (wid ^ 1u) * RING;
  const unsigned l31 = lane & 31u, hh = lane >> 5, l15 = lane & 15u, g4 = lane >> 4;

  const size_t kvoff = isB ? ((size_t)(b * 32u + h) * 1024u + half * 512u) * 256u : ((size_t)(b * 8u + g8) * 1024u + half * 512u) * 256u;
  const char* Kp = (isB ? p.kB : p.kA) + kvoff;
  const char* Vp = (isB ? p.vB : p.vA) + kvoff;
  const __amdgpu_buffer_rsrc_t sK = __builtin_amdgcn_make_buffer_rsrc((void*)Kp, (short)0, 512 * 256, 0x00020000);
  const __amdgpu_buffer_rsrc_t sV = __builtin_amdgcn_make_buffer_rsrc((void*)Vp, (short)0, 512 * 256, 0x00020000);
  unsigned kvo[4];
#pragma unroll
  for (int i = 0; i < 4; ++i) kvo[i] = g4 * 256u + (((l15 ^ g4) ^ (4u * i)) * 16u);
  const unsigned vvo = g4 * 256u + ((l15 ^ (g4 << 2)) * 16u);
#define DMA_K(q, tile, so) do { if (q == 0) dma2<0>(ring + (so), kvo[0], kvo[1], sK, (tile) * 8192u); else if (q == 1) dma2<2048>(ring + (so), kvo[2], kvo[3], sK, (tile) * 8192u); \
    else if (q == 2) dma2<0>(ring + (so) + 4096u, kvo[0], kvo[1], sK, (tile) * 8192u + 4096u); else dma2<2048>(ring + (so) + 4096u, kvo[2], kvo[3], sK, (tile) * 8192u + 4096u); } while (0)
#define DMA_V(q, tile, so) do { if (q == 0) dma2<0>(ring + (so), vvo, vvo, sV, (tile) * 8192u); else if (q == 1) dma2<2048>(ring + (so), vvo, vvo, sV, (tile) * 8192u); \
    else if (q == 2) dma2<0>(ring + (so) + 4096u, vvo, vvo, sV, (tile) * 8192u + 4096u); else dma2<2048>(ring + (so) + 4096u, vvo, vvo, sV, (tile) * 8192u + 4096u); } while (0)
#define DMA_K_ALL(tile, so) do { DMA_K(0, tile, so); DMA_K(1, tile, so); DMA_K(2, tile, so); DMA_K(3, tile, so); } while (0)
#define DMA_V_ALL(tile, so) do { DMA_V(0, tile, so); DMA_V(1, tile, so); DMA_V(2, tile, so); DMA_V(3, tile, so); } while (0)

  bf16x8 qf[2][8];
  {
    const char* wq = (isB ? p.wqB : p.wqA) + (size_t)(256u * half + 8u * g4) * 8192u + h * 256u + l15 * 16u;
    const char* xp = (isB ? p.xB : p.xA) + (size_t)(b * 64u + l15) * 1024u + (256u * half + 8u * g4) * 2u;
    const unsigned xq0 = half * 32768u, xq2 = 32768u - xq0;
    u32x4 w[4][8]; bf16x8 xf[4][4];
    f32x4 acc[4][8];
#pragma unroll
    for (int qb = 0; qb < 4; ++qb)
#pragma unroll
      for (int e = 0; e < 8; ++e) acc[qb][e] = f32x4{0.f, 0.f, 0.f, 0.f};
#define QLOAD(s) do { _Pragma("unroll") for (int j = 0; j < 8; ++j) w[(s) & 3][j] = *(const u32x4*)(wq + (size_t)(32 * (s) + j) * 8192u); \
      _Pragma("unroll") for (int qb = 0; qb < 4; ++qb) xf[(s) & 3][qb] = *(const bf16x8*)(xp + ((qb & 2) ? xq2 : xq0) + (qb & 1) * 16384 + (s) * 64); } while (0)
    QLOAD(0); QLOAD(1); QLOAD(2); QLOAD(3);
    SBAR();
    DMA_K_ALL(0u, 0u); DMA_K_ALL(1u, 1u * SLOT); DMA_V_ALL(0u, 2u * SLOT);
    SBAR();
#pragma unroll
    for (int s = 0; s < 8; ++s) {
#pragma unroll
      for (int e = 0; e < 8; ++e) {
        u32x4 a;
#pragma unroll
        for (int jj = 0; jj < 4; ++jj)
          a[jj] = __builtin_amdgcn_perm(w[s & 3][2 * jj + 1][e >> 1], w[s & 3][2 * jj][e >> 1], (e & 1) ? 0x07060302u : 0x05040100u);
        const bf16x8 af = __builtin_bit_cast(bf16x8, a);
#pragma unroll
        for (int qb = 0; qb < 4; ++qb) acc[qb][e] = MFMA16(af, xf[s & 3][qb], acc[qb][e]);
      }
      SBAR();
      if (s < 4) { QLOAD(s + 4); }
      SBAR();
      if (s == 0) { STAMP(1); }
    }
#undef QLOAD
    STAMP(2);
    const unsigned xo = ring + 3u * SLOT + lane * 16u, xi = pring + 3u * SLOT + lane * 16u;
#pragma unroll
    for (int q2 = 0; q2 < 2; ++q2)
#pragma unroll
      for (int e = 0; e < 8; ++e) lds_wrf4(xo + (q2 * 8 + e) * 1024u, acc[2 + q2][e]);
    BARRIER();
    f32x4 fin[2][8];
#pragma unroll
    for (int q2 = 0; q2 < 2; ++q2)
#pragma unroll
      for (int e = 0; e < 8; ++e) fin[q2][e] = lds_rdf4(xi + (q2 * 8 + e) * 1024u) + acc[q2][e];
    asm volatile("s_waitcnt lgkmcnt(0)" ::: "memory");
#pragma unroll
    for (int q2 = 0; q2 < 2; ++q2)
#pragma unroll
      for (int r = 0; r < 4; ++r) {
        const unsigned row = 16u * q2 + l15, ch = 4u * g4 + r;
        u32x4 v = {cvtpk(fin[q2][0][r], fin[q2][1][r]), cvtpk(fin[q2][2][r], fin[q2][3][r]), cvtpk(fin[q2][4][r], fin[q2][5][r]), cvtpk(fin[q2][6][r], fin[q2][7][r])};
        lds_wru4(pring + 4u * SLOT + row * 256u + ((ch ^ l15) * 16u), v);
      }
    BARRIER();
    DMA_K_ALL(2u, 3u * SLOT);
    const unsigned qlane = l31 * 256u + ((hh ^ l15) * 16u) + 4u * SLOT;
    const unsigned q0b = pring + qlane, q1b = ring + qlane;
#pragma unroll
    for (int s = 0; s < 8; ++s) { qf[0][s] = lds_rd128(q0b ^ (32u * s)); qf[1][s] = lds_rd128(q1b ^ (32u * s)); }
    BARRIER();
    DMA_V_ALL(1u, 4u * SLOT);
  }

  const unsigned kb = ring + l31 * 256u + ((hh ^ l15) * 16u);
  const unsigned vb = ring + (4u * hh + ((lane >> 2) & 3u)) * 256u + (((lane >> 2) & 3u) << 6) + (g4 & 1u) * 32u + (lane & 3u) * 8u;
  f32x16 o[4][2];
#pragma unroll
  for (int db = 0; db < 4; ++db) { o[db][0] = f32x16{}; o[db][1] = f32x16{}; }
  f32x16 sA[2], sB[2];
  bf16x8 kf[8]; s16x4 vlo[4][2], vhi[4][2]; u32x4 pw[2][2];
  float mhat[2], lsum[2] = {0.f, 0.f};
  const f32x16 zero16 = f32x16{};

  STAMP(3);
  WAITV(32);
#pragma unroll
  for (int s = 0; s < 8; ++s) kf[s] = lds_rd128(kb ^ (32u * s));
  sA[0] = MFMA32(kf[0], qf[0][0], zero16); sA[1] = MFMA32(kf[0], qf[1][0], zero16);
#pragma unroll
  for (int s = 1; s < 8; ++s) { sA[0] = MFMA32(kf[s], qf[0][s], sA[0]); sA[1] = MFMA32(kf[s], qf[1][s], sA[1]); }
  WAITVL(24);
  DMA_K_ALL(3u, 0u);
#pragma unroll
  for (int s = 0; s < 8; ++s) kf[s] = lds_rd128((kb + SLOT) ^ (32u * s));
  mhat[0] = swap_max(max16(sA[0])) * C2; mhat[1] = swap_max(max16(sA[1])) * C2;

  unsigned so1 = 1u * SLOT, so2 = 2u * SLOT, so3 = 3u * SLOT;
#define ADV2(x) x = (x >= 3u * SLOT) ? x - 3u * SLOT : x + 2u * SLOT
#define EX(v, nm) __builtin_amdgcn_exp2f(__builtin_fmaf((v), C2, nm))
#define VRD(db, ks) do { const unsigned a_ = (va ^ (64u * (db))) + 4096u * (ks); vlo[db][ks] = lds_tr(a_); vhi[db][ks] = lds_tr(a_ + 2048u); } while (0)
#define SMX(C, QB, i) do { const float e0_ = EX(C[QB][2 * (i)], nm##QB), e1_ = EX(C[QB][2 * (i) + 1], nm##QB); ls##QB += e0_; lt##QB += e1_; \
    pw[QB][(i) >> 2][(i) & 3] = cvtpk(e0_, e1_); } while (0)
#define GAPA(C, N, s, QB, t) do { \
    if ((s) == 0) N[QB] = MFMA32(kf[0], qf[QB][0], zero16); else N[QB] = MFMA32(kf[s], qf[QB][s], N[QB]); \
    SMX(C, QB, s); \
    if (QB == 0) { VRD(((s) & 3), ((s) >> 2)); } \
    if (QB == 1 && ((s) & 1) == 0) { DMA_V(((s) >> 1), (t) + 2u, so1); } \
    PIN(pw[QB][(s) >> 2]); PIN(ls##QB); PIN(lt##QB); SBAR(); } while (0)
#define PHASE_A(C, N, t) do { \
    WAITVL(24); SBAR(); \
    const unsigned va = vb + so2; \
    const float nm0 = -mhat[0], nm1 = -mhat[1]; float ls0 = 0.f, lt0 = 0.f, ls1 = 0.f, lt1 = 0.f; \
    GAPA(C, N, 0, 0, t); GAPA(C, N, 0, 1, t); GAPA(C, N, 1, 0, t); GAPA(C, N, 1, 1, t); GAPA(C, N, 2, 0, t); GAPA(C, N, 2, 1, t); GAPA(C, N, 3, 0, t); GAPA(C, N, 3, 1, t); \
    GAPA(C, N, 4, 0, t); GAPA(C, N, 4, 1, t); GAPA(C, N, 5, 0, t); GAPA(C, N, 5, 1, t); GAPA(C, N, 6, 0, t); GAPA(C, N, 6, 1, t); GAPA(C, N, 7, 0, t); GAPA(C, N, 7, 1, t); \
    lsum[0] += ls0 + lt0; lsum[1] += ls1 + lt1; } while (0)
#define PVM(db, ks, QB) o[db][QB] = MFMA32(cat8(vlo[db][ks], vhi[db][ks]), __builtin_bit_cast(bf16x8, pw[QB][ks]), o[db][QB])
#define GAPB(i, t) do { \
    PVM(((i) >> 1) & 3, (i) >> 3, (i) & 1); \
    if ((i) < 8) kf[(i) & 7] = lds_rd128(ka ^ (32u * ((i) & 7))); \
    if (((i) & 3) == 1) { DMA_K(((i) >> 2), (t) + 4u, so2); } \
    SBAR(); } while (0)
#define PHASE_B(N, t) do { \
    WAITVL(24); SBAR(); \
    const unsigned ka = kb + so3; \
    GAPB(0, t); GAPB(1, t); GAPB(2, t); GAPB(3, t); GAPB(4, t); GAPB(5, t); GAPB(6, t); GAPB(7, t); \
    float mx0 = max16(N[0]); PIN(mx0); \
    GAPB(8, t); GAPB(9, t); \
    float mx1 = max16(N[1]); PIN(mx1); \
    GAPB(10, t); GAPB(11, t); \
    const float rm0 = __builtin_fmaf(swap_max(mx0), C2, -mhat[0]), rm1 = __builtin_fmaf(swap_max(mx1), C2, -mhat[1]); \
    const bool grew = __any((rm0 > THR) | (rm1 > THR)); \
    GAPB(12, t); GAPB(13, t); GAPB(14, t); GAPB(15, t); \
    if (__builtin_expect(grew, 0)) { \
      const float d0 = __builtin_fmaxf(rm0, 0.f), d1 = __builtin_fmaxf(rm1, 0.f); mhat[0] += d0; mhat[1] += d1; \
      const float f0 = __builtin_amdgcn_exp2f(-d0), f1 = __builtin_amdgcn_exp2f(-d1); lsum[0] *= f0; lsum[1] *= f1; \
      _Pragma("unroll") for (int db = 0; db < 4; ++db) { o[db][0] *= f0; o[db][1] *= f1; } \
    } \
    ADV2(so1); ADV2(so2); ADV2(so3); } while (0)

  STAMP(4);
  for (unsigned t = 0; t < 14u; t += 2u) {
    PHASE_A(sA, sB, t);      PHASE_B(sB, t);
    PHASE_A(sB, sA, t + 1u); PHASE_B(sA, t + 1u);
  }
  PHASE_A(sA, sB, 14u); PHASE_B(sB, 14u);
  STAMP(5);
  {
    const float nm0 = -mhat[0], nm1 = -mhat[1]; float ls0 = 0.f, lt0 = 0.f, ls1 = 0.f, lt1 = 0.f;
#pragma unroll
    for (int i = 0; i < 8; ++i) { SMX(sB, 0, i); SMX(sB, 1, i); }
    lsum[0] += ls0 + lt0; lsum[1] += ls1 + lt1;
    PIN(pw[0][0]); PIN(pw[0][1]); PIN(pw[1][0]); PIN(pw[1][1]);
    SBAR();
    WAITV(0);
    const unsigned va = vb + so2;
#pragma unroll
    for (int i = 0; i < 8; ++i) VRD(i & 3, i >> 2);
#pragma unroll
    for (int i = 0; i < 16; ++i) PVM((i >> 1) & 3, i >> 3, i & 1);
  }
  STAMP(6);
  lsum[0] = swap_add(lsum[0]); lsum[1] = swap_add(lsum[1]);
  asm volatile("s_waitcnt lgkmcnt(0)" ::: "memory");
  {
    const unsigned xo = ring + lane * 16u, xi = pring + lane * 16u;
#pragma unroll
    for (int db = 0; db < 4; ++db)
#pragma unroll
      for (int r4 = 0; r4 < 4; ++r4) {
        f32x4 v;
#pragma unroll
        for (int e = 0; e < 4; ++e) v[e] = o[db][1][4 * r4 + e];
        lds_wrf4(xo + (db * 4 + r4) * 1024u, v);
      }
    { f32x4 v = {mhat[1], lsum[1], 0.f, 0.f}; lds_wrf4(xo + 16u * 1024u, v); }
    BARRIER();
    const f32x4 ml = lds_rdf4(xi + 16u * 1024u);
    const float m0 = mhat[0], l0 = lsum[0];
    const float mm = __builtin_fmaxf(m0, ml[0]);
    float a0 = __builtin_amdgcn_exp2f(m0 - mm), a1 = __builtin_amdgcn_exp2f(ml[0] - mm);
    const float inv = 1.0f / (a0 * l0 + a1 * ml[1]);
    a0 *= inv; a1 *= inv;
    char* op = p.o + ((size_t)(((isB ? 1u : 0u) * 8u + b) * 32u + h) * 2u + half) * 8192u + lane * 16u;
#pragma unroll
    for (int db = 0; db < 4; ++db)
#pragma unroll
      for (int ks = 0; ks < 2; ++ks) {
        const f32x4 x0 = lds_rdf4(xi + (db * 4 + 2 * ks) * 1024u), x1 = lds_rdf4(xi + (db * 4 + 2 * ks + 1) * 1024u);
        float y[8];
#pragma unroll
        for (int e = 0; e < 4; ++e) {
          y[e] = a0 * o[db][0][8 * ks + e] + a1 * x0[e];
          y[4 + e] = a0 * o[db][0][8 * ks + 4 + e] + a1 * x1[e];
        }
        u32x4 v = {cvtpk(y[0], y[1]), cvtpk(y[2], y[3]), cvtpk(y[4], y[5]), cvtpk(y[6], y[7])};
        *(u32x4*)(op + (db * 2 + ks) * 1024) = v;
      }
  }
  STAMP(7);
  STAMP_OUT(p.o, blockIdx.x * 4u + wid, isB ? 0 : 8, 8);
}

constexpr int K2_LDS = 8 * 16384;
struct P2 { const char *o, *woA, *woB; char* out; };

template <int OFF> DI void dma2d(unsigned m0v, unsigned vo, __amdgpu_buffer_rsrc_t srd, unsigned soff0, unsigned soff1) {
  asm volatile("s_mov_b32 m0, %0\n\ts_nop 0\n\t"
               "buffer_load_dwordx4 %1, %2, %3 offen offset:%5 lds\n\t"
               "buffer_load_dwordx4 %1, %2, %4 offen offset:%6 lds"
               :: "s"(m0v), "v"(vo), "s"(srd), "s"(soff0), "s"(soff1), "i"(OFF), "i"(OFF + 1024) : "memory");
}

__global__ __launch_bounds__(512, 2) void k2_oproj(P2 p) {
  extern __shared__ __attribute__((aligned(16))) char lds[];
  STAMP_DECL(4); STAMP(0);
  const unsigned tid = threadIdx.x, lane = tid & 63u, wid = rfl(tid >> 6);
  const unsigned g8 = blockIdx.x & 7u, sidx = blockIdx.x >> 3;
  const unsigned br = g8 >> 2, nt = (g8 & 3u) * 4u + (sidx & 3u), b = sidx >> 2;
  const unsigned ldsb = (unsigned)(unsigned long)lds, wb = ldsb + wid * 16384u;
  const unsigned hh = lane >> 5;
  const char* wop = (br ? p.woB : p.woA) + (size_t)(512u * wid) * 1024u + nt * 64u;
  const __amdgpu_buffer_rsrc_t sW = __builtin_amdgcn_make_buffer_rsrc((void*)(wop - 4096), (short)0, 512 * 1024 + 4096, 0x00020000);
  const unsigned wvo = (lane >> 2) * 1024u + (lane & 3u) * 16u;
#define WDMA(hd, buf) do { \
    dma2d<0>(wb + (buf) * 8192u, wvo, sW, (hd) * 131072u + 4096u, (hd) * 131072u + 16384u + 3072u); \
    dma2d<2048>(wb + (buf) * 8192u, wvo, sW, (hd) * 131072u + 32768u + 2048u, (hd) * 131072u + 49152u + 1024u); \
    dma2d<0>(wb + (buf) * 8192u + 4096u, wvo, sW, (hd) * 131072u + 65536u + 4096u, (hd) * 131072u + 81920u + 3072u); \
    dma2d<2048>(wb + (buf) * 8192u + 4096u, wvo, sW, (hd) * 131072u + 98304u + 2048u, (hd) * 131072u + 114688u + 1024u); } while (0)
  const char* op = p.o + ((size_t)(br * 8u + b) * 32u + 4u * wid) * 16384u + lane * 16u;
  bf16x8 of[2][16];
#define OLOAD(hd, buf) do { _Pragma("unroll") for (int f = 0; f < 16; ++f) of[buf][f] = *(const bf16x8*)(op + (hd) * 16384 + f * 1024); } while (0)
  WDMA(0u, 0u); OLOAD(0, 0); WDMA(1u, 1u); OLOAD(1, 1);
  f32x16 acc[2] = {f32x16{}, f32x16{}};
  const unsigned tb = wb + (4u * hh + ((lane >> 2) & 3u)) * 64u + ((lane >> 4) & 1u) * 32u + (lane & 3u) * 8u;
#pragma unroll
  for (int hd = 0; hd < 4; ++hd) {
    const int buf = hd & 1;
    if (hd < 3) { WAITV(24); } else { WAITV(16); }
    if (hd == 0) { STAMP(1); }
#pragma unroll
    for (int f = 0; f < 8; ++f) {
      const unsigned a = tb + buf * 8192u + f * 1024u;
      const bf16x8 wf = cat8(lds_tr(a), lds_tr(a + 512u));
      acc[0] = MFMA32(wf, of[buf][f], acc[0]);
      acc[1] = MFMA32(wf, of[buf][8 + f], acc[1]);
    }
    if (hd < 2) { asm volatile("s_waitcnt lgkmcnt(0)" ::: "memory"); SBAR(); WDMA((unsigned)hd + 2u, (unsigned)buf); OLOAD(hd + 2, buf); SBAR(); }
  }
  asm volatile("s_waitcnt lgkmcnt(0)" ::: "memory");
  STAMP(2);
#pragma unroll
  for (int qb = 0; qb < 2; ++qb)
#pragma unroll
    for (int r4 = 0; r4 < 4; ++r4) {
      f32x4 v = {acc[qb][4 * r4], acc[qb][4 * r4 + 1], acc[qb][4 * r4 + 2], acc[qb][4 * r4 + 3]};
      lds_wrf4(wb + (qb * 4 + r4) * 1024u + lane * 16u, v);
    }
  BARRIER();
  f32x4 s = lds_rdf4(ldsb + wid * 1024u + lane * 16u);
#pragma unroll
  for (int w = 1; w < 8; ++w) s += lds_rdf4(ldsb + w * 16384u + wid * 1024u + lane * 16u);
  const unsigned qb = wid >> 2, r4 = wid & 3u;
  u32x2 v = {cvtpk(s[0], s[1]), cvtpk(s[2], s[3])};
  *(u32x2*)(p.out + (size_t)(b * 64u + qb * 32u + (lane & 31u)) * 2048u + (br * 512u + nt * 32u + 8u * r4 + 4u * hh) * 2u) = v;
  STAMP(3);
  STAMP_OUT(p.o, 1024u + blockIdx.x * 8u + wid, 16, 4);
}

extern "C" void kernel_launch(void* const* d_in, const int* in_sizes, int n_in,
                              void* d_out, int out_size, void* d_ws, size_t ws_size,
                              hipStream_t stream) {
  (void)in_sizes; (void)n_in; (void)out_size; (void)ws_size;
  (void)hipFuncSetAttribute((const void*)k1_attn, hipFuncAttributeMaxDynamicSharedMemorySize, K1_LDS);
  (void)hipFuncSetAttribute((const void*)k2_oproj, hipFuncAttributeMaxDynamicSharedMemorySize, K2_LDS);
  P1 p1;
  p1.xA = (const char*)d_in[0]; p1.kA = (const char*)d_in[1]; p1.vA = (const char*)d_in[2]; p1.wqA = (const char*)d_in[3];
  p1.xB = (const char*)d_in[5]; p1.kB = (const char*)d_in[6]; p1.vB = (const char*)d_in[7]; p1.wqB = (const char*)d_in[8];
  p1.o = (char*)d_ws;
  hipLaunchKernelGGL(k1_attn, dim3(256), dim3(256), K1_LDS, stream, p1);
  P2 p2;
  p2.o = (const char*)d_ws; p2.woA = (const char*)d_in[4]; p2.woB = (const char*)d_in[9]; p2.out = (char*)d_out;
  hipLaunchKernelGGL(k2_oproj, dim3(256), dim3(512), K2_LDS, stream, p2);
}
```

```cpp
#include <hip/hip_runtime.h>
#include <hip/hip_bf16.h>

typedef float    f32x16 __attribute__((ext_vector_type(16)));
typedef float    f32x4  __attribute__((ext_vector_type(4)));
typedef float    f32x2  __attribute__((ext_vector_type(2)));
typedef __bf16   bf16x8 __attribute__((ext_vector_type(8)));
typedef __bf16   bf16x2 __attribute__((ext_vector_type(2)));
typedef short    s16x4  __attribute__((ext_vector_type(4)));
typedef unsigned u32x4  __attribute__((ext_vector_type(4)));
typedef unsigned u32x2  __attribute__((ext_vector_type(2)));
typedef unsigned long long u64;
typedef __amdgpu_buffer_rsrc_t srd_t;
#define DI __device__ __forceinline__
#define LAS __attribute__((address_space(3)))
#define GAS __attribute__((address_space(1)))
#define SBAR() __builtin_amdgcn_sched_barrier(0)
#define PIN(x) asm volatile("" : "+v"(x))
#define MFMA32(a, b, c) __builtin_amdgcn_mfma_f32_32x32x16_bf16(a, b, c, 0, 0, 0)
#define MFMA16(a, b, c) __builtin_amdgcn_mfma_f32_16x16x32_bf16(a, b, c, 0, 0, 0)
#define RLX_AGENT __ATOMIC_RELAXED, __HIP_MEMORY_SCOPE_AGENT
#define MKSRD(ptr, n) __builtin_amdgcn_make_buffer_rsrc((void*)(ptr), (short)0, (n), 0x00020000)

#define NSB_ 4
#define NSA_ 4
constexpr unsigned SLOT = 8192u, NSB = NSB_, NSA = NSA_;
constexpr unsigned LDS_A = 8u * SLOT, LDS_SCR = 18u * SLOT;
static_assert(NSB >= 3 && NSB <= 4 && NSA >= 3 && NSA <= 4, "ring sizes");
constexpr int K1_LDS = 163840;
constexpr float C2 = 0.08838834764831845f * 1.4426950408889634f;
constexpr float THR = 8.0f;
constexpr size_t WS_O = 0;
constexpr size_t WS_PART = 8u << 20;
constexpr size_t WS_FLAG = 16u << 20;
constexpr size_t WS_FLAG2 = WS_FLAG + 65536;
constexpr size_t WS_EPOCH = WS_FLAG2 + 65536;

DI unsigned rfl(unsigned x) { return __builtin_amdgcn_readfirstlane(x); }
DI unsigned cvtpk(float lo, float hi) { f32x2 v = {lo, hi}; bf16x2 b = __builtin_convertvector(v, bf16x2); return __builtin_bit_cast(unsigned, b); }
DI bf16x8 lds_rd128(unsigned a) { return *(const LAS bf16x8*)(unsigned long)a; }
DI f32x4 lds_rdf4(unsigned a) { return *(const LAS f32x4*)(unsigned long)a; }
DI void lds_wrf4(unsigned a, f32x4 v) { *(LAS f32x4*)(unsigned long)a = v; }
DI void lds_wru4(unsigned a, u32x4 v) { *(LAS u32x4*)(unsigned long)a = v; }
DI s16x4 lds_tr(unsigned a) { return __builtin_amdgcn_ds_read_tr16_b64_v4i16((LAS s16x4*)(unsigned long)a); }
DI bf16x8 cat8(s16x4 lo, s16x4 hi) { return __builtin_bit_cast(bf16x8, __builtin_shufflevector(lo, hi, 0, 1, 2, 3, 4, 5, 6, 7)); }
DI float max3f(float a, float b, float c) { float r; asm("v_max3_f32 %0, %1, %2, %3" : "=v"(r) : "v"(a), "v"(b), "v"(c)); return r; }
DI float max2f(float a, float b) { float r; asm("v_max_f32_e32 %0, %1, %2" : "=v"(r) : "v"(a), "v"(b)); return r; }
DI float fadd_s(float a, float b) { return a + b; }
DI float swap_max(float m) { auto rr = __builtin_amdgcn_permlane32_swap(__float_as_uint(m), __float_as_uint(m), false, false); return max2f(__uint_as_float(rr[0]), __uint_as_float(rr[1])); }
DI float swap_add(float m) { auto rr = __builtin_amdgcn_permlane32_swap(__float_as_uint(m), __float_as_uint(m), false, false); return __uint_as_float(rr[0]) + __uint_as_float(rr[1]); }
DI float max8lo(const f32x16& p) { float a = max3f(p[0], p[1], p[2]), b = max3f(p[3], p[4], p[5]); return max3f(max3f(a, p[6], p[7]), b, b); }
DI float max8hi(float m, const f32x16& p) { float a = max3f(m, p[8], p[9]), b = max3f(p[10], p[11], p[12]); a = max3f(a, p[13], p[14]); return max3f(a, b, p[15]); }
DI void scale_acc(f32x16& x, float f) {
#pragma unroll
  for (int r = 0; r < 16; ++r) { float e = x[r], t;
    asm volatile("v_accvgpr_read_b32 %1, %0\n\tv_mul_f32 %1, %1, %2\n\tv_accvgpr_write_b32 %0, %1" : "+a"(e), "=&v"(t) : "v"(f));
    x[r] = e; }
}
DI void sub_tile(f32x16& x, float d) {
#pragma unroll
  for (int r = 0; r < 16; ++r) { float e = x[r]; asm volatile("v_sub_f32_e32 %0, %0, %1" : "+v"(e) : "v"(d)); x[r] = e; }
}
DI u64 mix64(u64 z) { z += 0x9E3779B97F4A7C15ull; z = (z ^ (z >> 30)) * 0xBF58476D1CE4E5B9ull; z = (z ^ (z >> 27)) * 0x94D049BB133111EBull; return z ^ (z >> 31); }

#define SETM0(x) asm volatile("s_mov_b32 m0, %0" :: "s"(x) : "memory")
#define SETM0N(x) asm volatile("s_mov_b32 m0, %0\n\ts_nop 0" :: "s"(x) : "memory")
template <int OFF> DI void dma2(unsigned vo0, unsigned vo1, srd_t srd, unsigned soff) {
  asm volatile("buffer_load_dwordx4 %0, %2, %3 offen offset:%4 nt lds\n\t"
               "buffer_load_dwordx4 %1, %2, %3 offen offset:%5 nt lds"
               :: "v"(vo0), "v"(vo1), "s"(srd), "s"(soff), "i"(OFF), "i"(OFF + 1024) : "memory");
}
DI void dma4(unsigned m0v, const unsigned (&vo)[4], srd_t srd, unsigned soff) { SETM0N(m0v); dma2<0>(vo[0], vo[1], srd, soff); dma2<2048>(vo[2], vo[3], srd, soff); }
#define WAITV(N) asm volatile("s_waitcnt vmcnt(" #N ")" ::: "memory")
#define WAITL() do { __builtin_amdgcn_s_waitcnt(0xC07F); asm volatile("" ::: "memory"); } while (0)
#define BARRIER() do { WAITL(); asm volatile("s_barrier" ::: "memory"); } while (0)

DI void lds_signal(unsigned addr, unsigned v) { asm volatile("" ::: "memory"); WAITL(); __hip_atomic_store((LAS unsigned*)(unsigned long)addr, v, __ATOMIC_RELAXED, __HIP_MEMORY_SCOPE_WORKGROUP); }
DI void lds_wait(unsigned addr, unsigned v) {
  while (rfl(__hip_atomic_load((LAS unsigned*)(unsigned long)addr, __ATOMIC_RELAXED, __HIP_MEMORY_SCOPE_WORKGROUP)) < v) __builtin_amdgcn_s_sleep(1);
  asm volatile("" ::: "memory");
}
DI void poll_flags(GAS u64* f, u64 tag) {
#pragma unroll 1
  for (unsigned spins = 0; spins < (1u << 21); ++spins) {
    const bool ok = (__hip_atomic_load(f, RLX_AGENT) == tag);
    if (__all(ok)) break;
    __builtin_amdgcn_s_sleep(1);
  }
  asm volatile("" ::: "memory");
}

#define STAMP(i)
#define STAMP_DECL(n)
#define STAMP_OUT(base, widx, id0, n)
#define KVREC (512 * 256)

#define QWD dma4d
struct P1 {
  const char *xA, *kA, *vA, *wqA, *woA, *xB, *kB, *vB, *wqB, *woB;
  char* ws;
  char* out;
};

template <int N> DI void waitv() { asm volatile("s_waitcnt vmcnt(%0)" :: "i"(N > 63 ? 63 : N) : "memory"); }
DI void dma4d(unsigned m0v, const unsigned (&vo)[4], srd_t srd, unsigned soff) {
  asm volatile("s_mov_b32 m0, %0\n\ts_nop 0\n\t"
               "buffer_load_dwordx4 %1, %5, %6 offen offset:0 lds\n\t"
               "buffer_load_dwordx4 %2, %5, %6 offen offset:1024 lds\n\t"
               "buffer_load_dwordx4 %3, %5, %6 offen offset:2048 lds\n\t"
               "buffer_load_dwordx4 %4, %5, %6 offen offset:3072 lds"
               :: "s"(m0v), "v"(vo[0]), "v"(vo[1]), "v"(vo[2]), "v"(vo[3]), "s"(srd), "s"(soff) : "memory");
}
DI u32x4 pack8(const f32x16& x, int s, float c) {
  return u32x4{cvtpk(x[8 * s] * c, x[8 * s + 1] * c), cvtpk(x[8 * s + 2] * c, x[8 * s + 3] * c), cvtpk(x[8 * s + 4] * c, x[8 * s + 5] * c), cvtpk(x[8 * s + 6] * c, x[8 * s + 7] * c)};
}
template <int OFF> DI void oload4(bf16x8* x, unsigned vo, srd_t srd, unsigned soff) {
  asm volatile("buffer_load_dwordx4 %0, %4, %5, %6 offen offset:0 sc1\n\t"
               "buffer_load_dwordx4 %1, %4, %5, %6 offen offset:1024 sc1\n\t"
               "buffer_load_dwordx4 %2, %4, %5, %6 offen offset:2048 sc1\n\t"
               "buffer_load_dwordx4 %3, %4, %5, %6 offen offset:3072 sc1"
               : "=&v"(x[0]), "=&v"(x[1]), "=&v"(x[2]), "=&v"(x[3]) : "v"(vo), "s"(srd), "s"(soff) : "memory");
}
template <int N> DI void wait_o(bf16x8 (&x)[16]) {
  asm volatile("s_waitcnt vmcnt(%16)" : "+v"(x[0]), "+v"(x[1]), "+v"(x[2]), "+v"(x[3]), "+v"(x[4]), "+v"(x[5]), "+v"(x[6]), "+v"(x[7]),
               "+v"(x[8]), "+v"(x[9]), "+v"(x[10]), "+v"(x[11]), "+v"(x[12]), "+v"(x[13]), "+v"(x[14]), "+v"(x[15]) : "i"(N) : "memory");
}

__global__ __launch_bounds__(256, 1) void k1_attn(P1 p) {
  extern __shared__ __attribute__((aligned(16))) char lds[];
  STAMP_DECL(16); STAMP(0);
  const unsigned tid = threadIdx.x, lane = tid & 63u, wid = rfl(tid >> 6);
  const unsigned g8 = blockIdx.x & 7u, sidx = blockIdx.x >> 3;
  const unsigned h = 4u * g8 + (sidx & 3u), b = sidx >> 2;
  const bool isB = wid < 2u;
  const unsigned half = wid & 1u;
  const unsigned ldsb = (unsigned)(unsigned long)lds;
  const unsigned rsz = (isB ? NSB : NSA) * SLOT;
  const unsigned rstride = (isB ? 4u : 5u) * SLOT;
  const unsigned ring = ldsb + (isB ? 0u : LDS_A) + half * rstride, pring = ldsb + (isB ? 0u : LDS_A) + (half ^ 1u) * rstride;
  const unsigned l31 = lane & 31u, hh = lane >> 5, l15 = lane & 15u, g4 = lane >> 4;
  const unsigned sig = ldsb + 17u * SLOT;
  const unsigned mysig = sig + wid * 4u, psig = sig + (wid ^ 1u) * 4u;

  u64 epoch_v;
  asm volatile("global_load_dwordx2 %0, %1, off sc1" : "=v"(epoch_v) : "v"(p.ws + WS_EPOCH) : "memory");
  const size_t kvoff = isB ? ((size_t)(b * 32u + h) * 1024u + half * 512u) * 256u : ((size_t)(b * 8u + g8) * 1024u + half * 512u) * 256u;
  const srd_t sK = MKSRD((isB ? p.kB : p.kA) + kvoff, KVREC), sV = MKSRD((isB ? p.vB : p.vA) + kvoff, KVREC);
  unsigned kvo[4];
#pragma unroll
  for (int i = 0; i < 4; ++i) kvo[i] = g4 * 256u + (((l15 ^ g4) ^ (4u * i)) * 16u);
  const unsigned vvo = g4 * 256u + ((l15 ^ (g4 << 2)) * 16u);
  const unsigned vv4[4] = {vvo, vvo, vvo, vvo};
#define DMA_ALL(vo, srd, soff, so) do { dma4(ring + (so), vo, srd, (soff)); dma4(ring + (so) + 4096u, vo, srd, (soff) + 4096u); } while (0)

  bf16x8 qf[2][8];
  {
    const unsigned rot = (b + sidx) & 3u;
    const srd_t swB = MKSRD(p.wqB + h * 256u, 4 << 20), sxB = MKSRD(p.xB + (size_t)b * 65536u, 65536);
    const srd_t swA = MKSRD(p.wqA + h * 256u, 4 << 20), sxA = MKSRD(p.xA + (size_t)b * 65536u, 65536);
    unsigned wvo[4], xvo[4];
#pragma unroll
    for (int i = 0; i < 4; ++i) { wvo[i] = g4 * 8192u + ((l15 ^ (g4 << 2)) * 16u) + i * (32768u - 1024u); xvo[i] = g4 * 1024u + (((l15 ^ g4) ^ (4u * i)) * 16u) + i * (4096u - 1024u); }
#define QBUF(s) ((((s) + 1) % 3) * 49152u)
#define QLD1(s, j) do { const unsigned cv_ = (((s) >> 1) + rot) & 3u; \
      if ((j) == 0) dma4d(ldsb + QBUF(s) + 32768u + wid * 4096u, xvo, ((s) & 1) ? sxA : sxB, wid * 16384u + cv_ * 256u); \
      else QWD(ldsb + QBUF(s) + wid * 8192u + ((j) - 1) * 4096u, wvo, ((s) & 1) ? swA : swB, (cv_ * 128u + wid * 32u + ((j) - 1) * 16u) * 8192u); } while (0)
#define QLD(s) do { QLD1(s, 0); QLD1(s, 1); QLD1(s, 2); } while (0)
    const unsigned p4 = lane & 3u, q4 = (lane >> 2) & 3u;
    const unsigned wa0 = (ldsb + (8u * hh + q4) * 256u + (q4 << 6) + (g4 & 1u) * 32u + (((p4 & 1u) << 1) | (p4 >> 1)) * 8u) ^ (128u * (wid >> 1)), wa1 = wa0 ^ 64u;
    const unsigned xa = ldsb + 32768u + half * SLOT + l31 * 256u + ((hh ^ l15) * 16u);
    const f32x16 z16 = {};
    f32x16 acc[2][2];
    bf16x8 xf[2][8], wf0[2][8], wf1[2][8];
#define QMM(s, ks) do { const bool z_ = ((s) < 2 && (ks) == 0); \
      acc[(s) & 1][0] = MFMA32(wf0[(s) & 1][ks], xf[(s) & 1][ks], z_ ? z16 : acc[(s) & 1][0]); \
      acc[(s) & 1][1] = MFMA32(wf1[(s) & 1][ks], xf[(s) & 1][ks], z_ ? z16 : acc[(s) & 1][1]); } while (0)
#define QCMP(s, HOOK) do { const unsigned w0_ = wa0 + QBUF(s), w1_ = wa1 + QBUF(s), x_ = xa + QBUF(s); SBAR(); \
      _Pragma("unroll") for (int ks = 0; ks < 8; ++ks) { \
        xf[(s) & 1][ks] = lds_rd128(x_ ^ (32u * ks)); wf0[(s) & 1][ks] = cat8(lds_tr(w0_ + ks * 4096u), lds_tr(w0_ + ks * 4096u + 1024u)); \
        wf1[(s) & 1][ks] = cat8(lds_tr(w1_ + ks * 4096u), lds_tr(w1_ + ks * 4096u + 1024u)); \
        if ((s) > 0) QMM((s) - 1, ks); SBAR(); \
        if (ks == 0) { HOOK(0); } if (ks == 2) { HOOK(1); } if (ks == 4) { HOOK(2); } if (ks == 6) { HOOK(3); } SBAR(); } } while (0)
    const unsigned qo = ldsb + LDS_SCR + half * SLOT + (wid >> 1) * 4096u + lane * 16u;
#define QOUT(i) do { lds_wru4(qo, pack8(acc[i][0], 0, C2)); lds_wru4(qo + 1024u, pack8(acc[i][0], 1, C2)); lds_wru4(qo + 2048u, pack8(acc[i][1], 0, C2)); lds_wru4(qo + 3072u, pack8(acc[i][1], 1, C2)); } while (0)
    const unsigned qi0 = ldsb + LDS_SCR + half * SLOT + lane * 16u, qi1 = qi0 ^ SLOT;
#define QIN() do { _Pragma("unroll") for (int s = 0; s < 8; ++s) { qf[0][s] = lds_rd128(qi0 + s * 1024u); qf[1][s] = lds_rd128(qi1 + s * 1024u); } } while (0)
#define H2(j) if ((j) < 3) QLD1(2, (j) % 3)
#define H3(j) if ((j) < 3) QLD1(3, (j) % 3)
#define H4(j) if ((j) < 3) QLD1(4, (j) % 3)
#define H5(j) if ((j) < 3) QLD1(5, (j) % 3)
#define H6(j) if ((j) < 3) QLD1(6, (j) % 3)
#define H7(j) if ((j) < 3) QLD1(7, (j) % 3)
#define HK01(j) dma4(ring + (j) * 4096u, kvo, sK, (j) * 4096u)
#define H8(j) if (isB) HK01(j)
#define H9(j) if (isB) { if ((j) < 2) dma4(ring + 2u * SLOT + (j) * 4096u, vv4, sV, (j) * 4096u); else if (NSB > 3) dma4(ring + 2u * SLOT + (j) * 4096u, kvo, sK, 8192u + (j) * 4096u); } else if (half == 0u) HK01(j)
    QLD(0); QLD(1);
    waitv<12>(); BARRIER(); QCMP(0, H2);
    STAMP(1);
    waitv<12>(); BARRIER(); QCMP(1, H3);
    waitv<12>(); BARRIER(); QCMP(2, H4);
    waitv<12>(); BARRIER(); QCMP(3, H5);
    waitv<12>(); BARRIER(); QCMP(4, H6);
    waitv<12>(); BARRIER(); QCMP(5, H7);
    waitv<12>(); BARRIER(); QCMP(6, H8);
    if (isB) { waitv<16>(); } else { waitv<0>(); }
    BARRIER();
    QCMP(7, H9);
    STAMP(2);
    QMM(7, 0); QMM(7, 1); SBAR();
    QOUT(0);
    BARRIER();
    if (isB) { QIN(); } else if (half == 1u) { DMA_ALL(kvo, sK, 0u, 0u); DMA_ALL(kvo, sK, 8192u, SLOT); }
    QMM(7, 2); QMM(7, 3); QMM(7, 4); QMM(7, 5); QMM(7, 6); QMM(7, 7);
    __hip_atomic_store((LAS unsigned*)(unsigned long)mysig, 0u, __ATOMIC_RELAXED, __HIP_MEMORY_SCOPE_WORKGROUP);
    {
      const unsigned qa = ldsb + (half ? 16u : 12u) * SLOT + (wid >> 1) * 4096u + lane * 16u;
      lds_wru4(qa, pack8(acc[1][0], 0, C2)); lds_wru4(qa + 1024u, pack8(acc[1][0], 1, C2)); lds_wru4(qa + 2048u, pack8(acc[1][1], 0, C2)); lds_wru4(qa + 3072u, pack8(acc[1][1], 1, C2));
    }
    BARRIER();
    if (!isB) {
      const unsigned a0 = ldsb + (half ? 16u : 12u) * SLOT + lane * 16u, a1 = ldsb + (half ? 12u : 16u) * SLOT + lane * 16u;
#pragma unroll
      for (int s = 0; s < 8; ++s) { qf[0][s] = lds_rd128(a0 + s * 1024u); qf[1][s] = lds_rd128(a1 + s * 1024u); }
      DMA_ALL(vv4, sV, 0u, 2u * SLOT);
      if (NSA > 3) { lds_signal(mysig, 1u); lds_wait(psig, 1u); DMA_ALL(kvo, sK, 2u * 8192u, 3u * SLOT); }
    }
  }
  PIN(epoch_v);
  const u64 tag = mix64(((u64)rfl((unsigned)(epoch_v >> 32)) << 32) | rfl((unsigned)epoch_v));

  const bool aK = isB ? (NSB % 2 == 0) : (NSA % 2 == 0);
  const srd_t sPA = aK ? sK : sV, sPB = aK ? sV : sK;
  unsigned voA[4], voB[4];
#pragma unroll
  for (int i = 0; i < 4; ++i) { voA[i] = aK ? kvo[i] : vvo; voB[i] = aK ? vvo : kvo[i]; }
  constexpr unsigned TA_B = NSB % 2 ? (NSB - 1) / 2 : NSB / 2 + 1, TB_B = NSB % 2 ? (NSB + 3) / 2 : NSB / 2;
  constexpr unsigned TA_A = NSA % 2 ? (NSA - 1) / 2 : NSA / 2 + 1, TB_A = NSA % 2 ? (NSA + 3) / 2 : NSA / 2;
  unsigned tA = (isB ? TA_B : TA_A) * 8192u, tB = (isB ? TB_B : TB_A) * 8192u;
  const unsigned kb = ring + l31 * 256u + ((hh ^ l15) * 16u);
  const unsigned vb = ring + (4u * hh + ((lane >> 2) & 3u)) * 256u + (((lane >> 2) & 3u) << 6) + (g4 & 1u) * 32u + (lane & 3u) * 8u;
  f32x16 o[4][2];
#pragma unroll
  for (int db = 0; db < 4; ++db) { o[db][0] = f32x16{}; o[db][1] = f32x16{}; }
  f32x16 sA[2], sB[2], negm[2];
  bf16x8 kf[8]; s16x4 vlo[4][2], vhi[4][2]; u32x4 pw[2][2];
  float f0 = 1.f, f1 = 1.f, ls0 = 0.f, lt0 = 0.f, ls1 = 0.f, lt1 = 0.f;
#define QK0(D, a, bq) asm volatile("v_mfma_f32_32x32x16_bf16 %0, %1, %2, 0" : "=&v"(D) : "v"(a), "a"(bq))
#define QKC(D, a, bq, C) asm volatile("v_mfma_f32_32x32x16_bf16 %0, %1, %2, %3" : "=&v"(D) : "v"(a), "a"(bq), "v"(C))
#define QKA(D, a, bq) asm volatile("v_mfma_f32_32x32x16_bf16 %0, %1, %2, %0" : "+v"(D) : "v"(a), "a"(bq))
#define WAITN(k) do { if (NSA >= NSB) { waitv<8 * (NSA - (k))>(); if (NSA != NSB && isB) waitv<8 * (NSB - (k))>(); } else { waitv<8 * (NSB - (k))>(); if (!isB) waitv<8 * (NSA - (k))>(); } } while (0)
#define WAITR() do { WAITN(2); WAITL(); } while (0)
#define EXP2(x) __builtin_amdgcn_exp2f(x)

  STAMP(3);
  WAITN(1);
#pragma unroll
  for (int s = 0; s < 8; ++s) kf[s] = lds_rd128(kb ^ (32u * s));
  QK0(sA[0], kf[0], qf[0][0]); QK0(sA[1], kf[0], qf[1][0]);
#pragma unroll
  for (int s = 1; s < 8; ++s) { QKA(sA[0], kf[s], qf[0][s]); QKA(sA[1], kf[s], qf[1][s]); }
  WAITR();
  DMA_ALL(voB, sPB, tB - 8192u, 0u);
#pragma unroll
  for (int s = 0; s < 8; ++s) kf[s] = lds_rd128((kb + SLOT) ^ (32u * s));
  asm volatile("s_nop 7\n\ts_nop 7" : "+v"(sA[0]), "+v"(sA[1]));
#pragma unroll
  for (int qb = 0; qb < 2; ++qb) {
    const float m0 = swap_max(max8hi(max8lo(sA[qb]), sA[qb]));
#pragma unroll
    for (int r = 0; r < 16; ++r) { sA[qb][r] -= m0; negm[qb][r] = -m0; }
    PIN(negm[qb]);
#pragma unroll
    for (int r = 0; r < 8; ++r) sA[qb][r] = EXP2(sA[qb][r]);
  }
#pragma unroll
  for (int r = 0; r < 8; r += 2) { ls0 += sA[0][r]; lt0 += sA[0][r + 1]; ls1 += sA[1][r]; lt1 += sA[1][r + 1]; }

  unsigned so1 = 1u * SLOT, so2 = 2u * SLOT, so3 = (rsz == 3u * SLOT) ? 0u : 3u * SLOT;
  const unsigned wrap = rsz - 2u * SLOT;
#define ADV2(x) x = (x >= wrap) ? x - wrap : x + 2u * SLOT
#define VRD(db, ks) do { const unsigned a_ = (va ^ (64u * (db))) + 4096u * (ks); vlo[db][ks] = lds_tr(a_); vhi[db][ks] = lds_tr(a_ + 2048u); } while (0)
#define GAPA(C, N, s, QB) do { \
    if ((s) == 0) QKC(N[QB], kf[0], qf[QB][0], negm[QB]); else QKA(N[QB], kf[s], qf[QB][s]); \
    if ((s) < 4) { C[QB][8 + 2 * (s)] = EXP2(C[QB][8 + 2 * (s)]); C[QB][9 + 2 * (s)] = EXP2(C[QB][9 + 2 * (s)]); pw[QB][0][(s) & 3] = cvtpk(C[QB][2 * (s)], C[QB][2 * (s) + 1]); PIN(C[QB]); PIN(pw[QB][0]); } \
    else { ls##QB = fadd_s(ls##QB, C[QB][2 * (s)]); lt##QB = fadd_s(lt##QB, C[QB][2 * (s) + 1]); pw[QB][1][(s) & 3] = cvtpk(C[QB][2 * (s)], C[QB][2 * (s) + 1]); PIN(pw[QB][1]); PIN(ls##QB); PIN(lt##QB); } \
    if (QB == 0) { VRD(((s) & 3), ((s) >> 2)); } \
    if (QB == 0 && (s) == 4) SETM0(ring + so1); \
    if (QB == 0 && (s) == 6) SETM0(ring + so1 + 4096u); \
    if (QB == 1 && (s) == 4) dma2<0>(voA[0], voA[1], sPA, tA); \
    if (QB == 1 && (s) == 5) dma2<2048>(voA[2], voA[3], sPA, tA); \
    if (QB == 1 && (s) == 6) dma2<0>(voA[0], voA[1], sPA, tA + 4096u); \
    if (QB == 1 && (s) == 7) dma2<2048>(voA[2], voA[3], sPA, tA + 4096u); \
    SBAR(); } while (0)
#define PHASE_A(C, N) do { \
    WAITR(); SBAR(); \
    const unsigned va = vb + so2; \
    GAPA(C, N, 0, 0); GAPA(C, N, 0, 1); GAPA(C, N, 1, 0); GAPA(C, N, 1, 1); GAPA(C, N, 2, 0); GAPA(C, N, 2, 1); GAPA(C, N, 3, 0); GAPA(C, N, 3, 1); \
    GAPA(C, N, 4, 0); GAPA(C, N, 4, 1); GAPA(C, N, 5, 0); GAPA(C, N, 5, 1); GAPA(C, N, 6, 0); GAPA(C, N, 6, 1); GAPA(C, N, 7, 0); GAPA(C, N, 7, 1); \
    tA += 8192u; } while (0)
#define PVM(i) o[((i) >> 1) & 3][(i) & 1] = MFMA32(cat8(vlo[((i) >> 1) & 3][(i) >> 3], vhi[((i) >> 1) & 3][(i) >> 3]), __builtin_bit_cast(bf16x8, pw[(i) & 1][(i) >> 3]), o[((i) >> 1) & 3][(i) & 1])
#define KRD(i) kf[i] = lds_rd128(ka ^ (32u * (i)))
#define GAPE(N, j) do { PVM(8 + (j)); N[0][j] = EXP2(N[0][j]); N[1][j] = EXP2(N[1][j]); \
    if ((j) > 0) { if ((j) & 1) { ls0 = fadd_s(ls0, N[0][((j) + 15) & 15]); ls1 = fadd_s(ls1, N[1][((j) + 15) & 15]); } else { lt0 = fadd_s(lt0, N[0][((j) + 15) & 15]); lt1 = fadd_s(lt1, N[1][((j) + 15) & 15]); } } \
    PIN(N[0]); PIN(N[1]); PIN(ls0); PIN(ls1); PIN(lt0); PIN(lt1); SBAR(); } while (0)
#define PHASE_B(N) do { \
    SETM0(ring + so2); WAITR(); SBAR(); \
    const unsigned ka = kb + so3; \
    PVM(0); KRD(0); KRD(1); dma2<0>(voB[0], voB[1], sPB, tB); SBAR(); \
    PVM(1); KRD(2); KRD(3); dma2<2048>(voB[2], voB[3], sPB, tB); SETM0(ring + so2 + 4096u); SBAR(); \
    PVM(2); KRD(4); float ma0 = max3f(N[0][0], N[0][1], N[0][2]), ma1 = max3f(N[1][0], N[1][1], N[1][2]), mb0 = max3f(N[0][3], N[0][4], N[0][5]), mb1 = max3f(N[1][3], N[1][4], N[1][5]); SBAR(); \
    PVM(3); KRD(5); ma0 = max3f(ma0, N[0][6], N[0][7]); ma1 = max3f(ma1, N[1][6], N[1][7]); mb0 = max3f(mb0, N[0][8], N[0][9]); mb1 = max3f(mb1, N[1][8], N[1][9]); SBAR(); \
    PVM(4); KRD(6); ma0 = max3f(ma0, N[0][10], N[0][11]); ma1 = max3f(ma1, N[1][10], N[1][11]); mb0 = max3f(mb0, N[0][12], N[0][13]); mb1 = max3f(mb1, N[1][12], N[1][13]); SBAR(); \
    PVM(5); ma0 = max3f(ma0, N[0][14], N[0][15]); ma1 = max3f(ma1, N[1][14], N[1][15]); const float mbb = max2f(mb0, mb1); KRD(7); SBAR(); \
    PVM(6); const float mall = max3f(ma0, ma1, mbb); dma2<0>(voB[0], voB[1], sPB, tB + 4096u); const bool grew = __any(mall > THR); SBAR(); \
    PVM(7); dma2<2048>(voB[2], voB[3], sPB, tB + 4096u); SBAR(); \
    if (__builtin_expect(grew, 0)) {     \
      const float d0 = max2f(swap_max(max2f(ma0, mb0)), 0.f), d1 = max2f(swap_max(max2f(ma1, mb1)), 0.f); \
      f0 = EXP2(-d0); f1 = EXP2(-d1); ls0 *= f0; lt0 *= f0; ls1 *= f1; lt1 *= f1; \
      sub_tile(N[0], d0); sub_tile(N[1], d1); sub_tile(negm[0], d0); sub_tile(negm[1], d1); \
    } \
    SBAR(); \
    GAPE(N, 0); GAPE(N, 1); GAPE(N, 2); GAPE(N, 3); GAPE(N, 4); GAPE(N, 5); GAPE(N, 6); GAPE(N, 7); \
    lt0 = fadd_s(lt0, N[0][7]); lt1 = fadd_s(lt1, N[1][7]); \
    if (__builtin_expect(grew, 0)) { \
      asm volatile("s_nop 7\n\ts_nop 7" ::: "memory"); \
      _Pragma("unroll") for (int db = 0; db < 4; ++db) { scale_acc(o[db][0], f0); scale_acc(o[db][1], f1); } \
    } \
    ADV2(so1); ADV2(so2); ADV2(so3); tB += 8192u; } while (0)

  STAMP(4);
  for (unsigned t = 0; t < 14u; t += 2u) {
    PHASE_A(sA, sB); PHASE_B(sB);
    PHASE_A(sB, sA); PHASE_B(sA);
  }
  PHASE_A(sA, sB); PHASE_B(sB);
  STAMP(5);
  const unsigned br = isB ? 1u : 0u;
  const unsigned kq = g8 >> 1, ob = 4u * (g8 & 1u) + (sidx >> 3), nt = sidx & 7u, hd0 = 8u * kq + 4u * half, orot = (nt + ob) & 3u;
  const srd_t sW = MKSRD((isB ? p.woB : p.woA) + (size_t)hd0 * 131072u + nt * 128u, 512 * 1024 - 896);
  unsigned wvo[4];
#pragma unroll
  for (int j = 0; j < 4; ++j) wvo[j] = (lane >> 3) * 1024u + (((lane & 7u) ^ ((g4 & 1u) << 2)) * 16u) + j * 7168u;
#define WDMA4(i, j) dma4(ring + (((i) & 1) ^ 1) * 16384u + (j) * 4096u, wvo, sW, (((i) + orot) & 3u) * 131072u + (j) * 32768u)
#define WDMA(i) do { WDMA4(i, 0); WDMA4(i, 1); WDMA4(i, 2); WDMA4(i, 3); } while (0)
  {
#pragma unroll
    for (int r = 8; r < 16; ++r) { sB[0][r] = EXP2(sB[0][r]); sB[1][r] = EXP2(sB[1][r]); ls0 += sB[0][r]; ls1 += sB[1][r]; }
#pragma unroll
    for (int i = 0; i < 8; ++i) { pw[0][i >> 2][i & 3] = cvtpk(sB[0][2 * i], sB[0][2 * i + 1]); pw[1][i >> 2][i & 3] = cvtpk(sB[1][2 * i], sB[1][2 * i + 1]); }
    PIN(pw[0][0]); PIN(pw[0][1]); PIN(pw[1][0]); PIN(pw[1][1]);
    SBAR();
    WAITV(0);
    const unsigned va = vb + so2;
#pragma unroll
    for (int i = 0; i < 8; ++i) VRD(i & 3, i >> 2);
    WAITL(); SBAR();
    PVM(0); PVM(1); PVM(2); PVM(3); WDMA4(0, 0); SBAR(); PVM(4); PVM(5); PVM(6); PVM(7); WDMA4(0, 1); SBAR();
    PVM(8); PVM(9); PVM(10); PVM(11); WDMA4(0, 2); SBAR(); PVM(12); PVM(13); PVM(14); PVM(15); WDMA4(0, 3); SBAR();
  }
  STAMP(6);
  const srd_t sO = MKSRD(p.ws + WS_O, 16 << 20);
  GAS u64* flags = (GAS u64*)(p.ws + WS_FLAG);
  {
    const float l0 = swap_add(ls0 + lt0), l1 = swap_add(ls1 + lt1);
    const float mhat[2] = {-negm[0][0], -negm[1][0]};
    const unsigned xo = ring + lane * 16u, xi = pring + lane * 16u;
    const unsigned mo = sig + 1024u + wid * 512u + lane * 8u, mi = sig + 1024u + (wid ^ 1u) * 512u + lane * 8u;
#pragma unroll
    for (int db = 0; db < 4; ++db)
#pragma unroll
      for (int r4 = 0; r4 < 4; ++r4) lds_wrf4(xo + (db * 4 + r4) * 1024u, f32x4{o[db][1][4 * r4], o[db][1][4 * r4 + 1], o[db][1][4 * r4 + 2], o[db][1][4 * r4 + 3]});
    *(LAS f32x2*)(unsigned long)mo = f32x2{mhat[1], l1};
    lds_signal(mysig, 15u); lds_wait(psig, 15u);
    const f32x2 ml = *(const LAS f32x2*)(unsigned long)mi;
    const float mm = __builtin_fmaxf(mhat[0], ml[0]);
    float a0 = EXP2(mhat[0] - mm), a1 = EXP2(ml[0] - mm);
    const float inv = 1.0f / (a0 * l0 + a1 * ml[1]);
    a0 *= inv; a1 *= inv;
    const unsigned oidx = ((br * 8u + b) * 32u + h) * 2u + half;
#pragma unroll
    for (int db = 0; db < 4; ++db)
#pragma unroll
      for (int ks = 0; ks < 2; ++ks) {
        const f32x4 x0 = lds_rdf4(xi + (db * 4 + 2 * ks) * 1024u), x1 = lds_rdf4(xi + (db * 4 + 2 * ks + 1) * 1024u);
        float y[8];
#pragma unroll
        for (int e = 0; e < 4; ++e) {
          y[e] = a0 * o[db][0][8 * ks + e] + a1 * x0[e];
          y[4 + e] = a0 * o[db][0][8 * ks + 4 + e] + a1 * x1[e];
        }
        u32x4 v = {cvtpk(y[0], y[1]), cvtpk(y[2], y[3]), cvtpk(y[4], y[5]), cvtpk(y[6], y[7])};
        __builtin_amdgcn_raw_buffer_store_b128(v, sO, oidx * 8192u + lane * 16u + (db * 2 + ks) * 1024u, 0, 16);
      }
    lds_signal(mysig, 16u);
    WAITV(0);
    if (lane == 0) __hip_atomic_store(flags + (size_t)oidx * 8u, tag, RLX_AGENT);
  }
  STAMP(7);
  {
    const unsigned q2 = (lane >> 2) & 3u;
    const unsigned tl = ring + (4u * hh + q2) * 128u + (g4 & 1u) * 32u + (lane & 3u) * 8u;
    const unsigned tb0 = tl + (((kq ^ (q2 >> 1)) & 1u) * 64u), tb1 = tb0 ^ 64u;
#define WFRAG(tb, i, f) cat8(lds_tr((tb) + (((i) & 1) ^ 1) * 16384u + (f) * 2048u), lds_tr((tb) + (((i) & 1) ^ 1) * 16384u + (f) * 2048u + 1024u))
    bf16x8 wr[2][2][8];
#pragma unroll
    for (int f = 0; f < 8; ++f) { wr[0][0][f] = WFRAG(tb0, 0, f); wr[0][1][f] = WFRAG(tb1, 0, f); }
    lds_wait(psig, 16u);
    WDMA(1);
    WAITL(); WDMA(2);
    waitv<16>();
#pragma unroll
    for (int f = 0; f < 8; ++f) { wr[1][0][f] = WFRAG(tb0, 1, f); wr[1][1][f] = WFRAG(tb1, 1, f); }
    WAITL(); WDMA(3);
#pragma unroll
    for (int f = 0; f < 8; ++f) asm volatile("" : "+a"(wr[0][0][f]), "+a"(wr[0][1][f]), "+a"(wr[1][0][f]), "+a"(wr[1][1][f]));
    STAMP(14);
    const unsigned fidx = ((br * 8u + ob) * 32u + hd0) * 2u;
    poll_flags(flags + (size_t)(fidx + (lane & 7u)) * 8u, tag);
    STAMP(8);
    bf16x8 of[3][16];
    const unsigned ovo = lane * 16u, obase = fidx * 8192u, q0 = half * 8192u, q1 = 8192u - q0;
#define OLD(i) do { const unsigned s_ = obase + (((i) + orot) & 3u) * 16384u; \
    oload4<0>(&of[(i) % 3][0], ovo, sO, s_ + q0); oload4<0>(&of[(i) % 3][8], ovo, sO, s_ + q1); oload4<0>(&of[(i) % 3][4], ovo, sO, s_ + q0 + 4096u); oload4<0>(&of[(i) % 3][12], ovo, sO, s_ + q1 + 4096u); } while (0)
    f32x16 acc[2][2];
#define OSTEP(i, N) do { wait_o<N>(of[(i) % 3]); \
    _Pragma("unroll") for (int f = 0; f < 8; ++f) { \
      const bf16x8 w0 = (i) < 2 ? wr[(i) & 1][0][f] : WFRAG(tb0, i, f), w1 = (i) < 2 ? wr[(i) & 1][1][f] : WFRAG(tb1, i, f); \
      if ((i) == 0 && f == 0) { const f32x16 z_ = {}; acc[0][0] = MFMA32(w0, of[0][0], z_); acc[0][1] = MFMA32(w1, of[0][0], z_); acc[1][0] = MFMA32(w0, of[0][8], z_); acc[1][1] = MFMA32(w1, of[0][8], z_); } \
      else { acc[0][0] = MFMA32(w0, of[(i) % 3][f], acc[0][0]); acc[0][1] = MFMA32(w1, of[(i) % 3][f], acc[0][1]); acc[1][0] = MFMA32(w0, of[(i) % 3][8 + f], acc[1][0]); acc[1][1] = MFMA32(w1, of[(i) % 3][8 + f], acc[1][1]); } \
      if ((i) == 0 && f == 1) { SBAR(); OLD(2); SBAR(); } } \
    SBAR(); } while (0)
    OLD(0); OLD(1);
    OSTEP(0, 16); OLD(3); SBAR();
    OSTEP(1, 32);
    OSTEP(2, 16);
    OSTEP(3, 0);
    STAMP(9);
    WAITL();
    const unsigned xo = ring + lane * 16u, xi = pring + lane * 16u;
#pragma unroll
    for (int nb = 0; nb < 2; ++nb)
#pragma unroll
      for (int r4 = 0; r4 < 4; ++r4) lds_wrf4(xo + (nb * 4 + r4) * 1024u, f32x4{acc[1][nb][4 * r4], acc[1][nb][4 * r4 + 1], acc[1][nb][4 * r4 + 2], acc[1][nb][4 * r4 + 3]});
    lds_signal(mysig, 17u); lds_wait(psig, 17u);
    f32x4 y[2][4];
#pragma unroll
    for (int nb = 0; nb < 2; ++nb)
#pragma unroll
      for (int r4 = 0; r4 < 4; ++r4) y[nb][r4] = lds_rdf4(xi + (nb * 4 + r4) * 1024u) + f32x4{acc[0][nb][4 * r4], acc[0][nb][4 * r4 + 1], acc[0][nb][4 * r4 + 2], acc[0][nb][4 * r4 + 3]};
    const unsigned tidx = (br * 8u + ob) * 8u + nt;
    const bool owner = (half == (kq >> 1));
    const unsigned pbase = (unsigned)WS_PART + tidx * 65536u + kq * 4096u + lane * 16u;
    GAS u64* flags2 = (GAS u64*)(p.ws + WS_FLAG2);
#pragma unroll
    for (int r4 = 0; r4 < 4; ++r4) __builtin_amdgcn_raw_buffer_store_b128(__builtin_bit_cast(u32x4, y[1][r4]), sO, pbase + (2u * half + ((kq & 1u) ^ 1u)) * 16384u + r4 * 1024u, 0, 16);
    if (!owner) {
#pragma unroll
      for (int r4 = 0; r4 < 4; ++r4) __builtin_amdgcn_raw_buffer_store_b128(__builtin_bit_cast(u32x4, y[0][r4]), sO, pbase + (2u * half + (kq & 1u)) * 16384u + r4 * 1024u, 0, 16);
    }
    WAITV(0);
    if (lane == 0) __hip_atomic_store(flags2 + (size_t)((tidx * 2u + half) * 4u + kq) * 8u, tag, RLX_AGENT);
    STAMP(10);
    if (owner) {
      poll_flags(flags2 + (size_t)((tidx * 2u + half) * 4u + (lane & 3u)) * 8u, tag);
      STAMP(11);
      const unsigned rbase = (unsigned)WS_PART + tidx * 65536u + kq * 16384u + lane * 16u;
#pragma unroll
      for (int j = 1; j < 4; ++j)
#pragma unroll
        for (int r4 = 0; r4 < 4; ++r4) y[0][r4] += __builtin_bit_cast(f32x4, __builtin_amdgcn_raw_buffer_load_b128(sO, rbase + (kq ^ j) * 4096u + r4 * 1024u, 0, 16));
      char* op = p.out + (size_t)(ob * 64u + half * 32u + l31) * 2048u + (br * 512u + nt * 64u + (kq & 1u) * 32u + 4u * hh) * 2u;
#pragma unroll
      for (int r4 = 0; r4 < 4; ++r4) *(u32x2*)(op + 16 * r4) = u32x2{cvtpk(y[0][r4][0], y[0][r4][1]), cvtpk(y[0][r4][2], y[0][r4][3])};
    } else if (blockIdx.x == 0 && isB) {
#pragma unroll 1
      for (unsigned spins = 0; spins < (1u << 21); ++spins) {
        bool ok = true;
#pragma unroll
        for (int k = 0; k < 16; ++k) ok &= (__hip_atomic_load(flags + (size_t)(k * 64 + lane) * 8u, RLX_AGENT) == tag);
        if (__all(ok)) break;
        __builtin_amdgcn_s_sleep(8);
      }
      if (lane == 0) __hip_atomic_store((GAS u64*)(p.ws + WS_EPOCH), tag, RLX_AGENT);
    }
  }
  STAMP(12);
  STAMP_OUT(p.ws, blockIdx.x * 4u + wid, isB ? 0 : 16, 16);
}

extern "C" void kernel_launch(void* const* d_in, const int* in_sizes, int n_in,
                              void* d_out, int out_size, void* d_ws, size_t ws_size,
                              hipStream_t stream) {
  (void)in_sizes; (void)n_in; (void)out_size; (void)ws_size;
  (void)hipFuncSetAttribute((const void*)k1_attn, hipFuncAttributeMaxDynamicSharedMemorySize, K1_LDS);
  P1 p1;
  p1.xA = (const char*)d_in[0]; p1.kA = (const char*)d_in[1]; p1.vA = (const char*)d_in[2]; p1.wqA = (const char*)d_in[3]; p1.woA = (const char*)d_in[4];
  p1.xB = (const char*)d_in[5]; p1.kB = (const char*)d_in[6]; p1.vB = (const char*)d_in[7]; p1.wqB = (const char*)d_in[8]; p1.woB = (const char*)d_in[9];
  p1.ws = (char*)d_ws; p1.out = (char*)d_out;
  hipLaunchKernelGGL(k1_attn, dim3(256), dim3(256), K1_LDS, stream, p1);
}
```

```cpp
#include <hip/hip_runtime.h>
#include <hip/hip_bf16.h>

typedef float    f32x16 __attribute__((ext_vector_type(16)));
typedef float    f32x4  __attribute__((ext_vector_type(4)));
typedef float    f32x2  __attribute__((ext_vector_type(2)));
typedef __bf16   bf16x8 __attribute__((ext_vector_type(8)));
typedef __bf16   bf16x2 __attribute__((ext_vector_type(2)));
typedef short    s16x4  __attribute__((ext_vector_type(4)));
typedef unsigned u32x4  __attribute__((ext_vector_type(4)));
typedef unsigned u32x2  __attribute__((ext_vector_type(2)));
typedef unsigned long long u64;
typedef __amdgpu_buffer_rsrc_t srd_t;
#define DI __device__ __forceinline__
#define LAS __attribute__((address_space(3)))
#define GAS __attribute__((address_space(1)))
#define SBAR() __builtin_amdgcn_sched_barrier(0)
#define PIN(x) asm volatile("" : "+v"(x))
#define MFMA32(a, b, c) __builtin_amdgcn_mfma_f32_32x32x16_bf16(a, b, c, 0, 0, 0)
#define MFMA16(a, b, c) __builtin_amdgcn_mfma_f32_16x16x32_bf16(a, b, c, 0, 0, 0)
#define RLX_AGENT __ATOMIC_RELAXED, __HIP_MEMORY_SCOPE_AGENT
#define MKSRD(ptr, n) __builtin_amdgcn_make_buffer_rsrc((void*)(ptr), (short)0, (n), 0x00020000)

constexpr unsigned SLOT = 8192u;
constexpr int K1_LDS = 163840;
constexpr float C2 = 0.08838834764831845f * 1.4426950408889634f;
constexpr float THR = 8.0f;
constexpr size_t WS_O = 0;
constexpr size_t WS_PART = 8u << 20;
constexpr size_t WS_FLAG = 16u << 20;
constexpr size_t WS_FLAG2 = WS_FLAG + 65536;
constexpr size_t WS_FLAG3 = WS_FLAG2 + 65536;
constexpr size_t WS_EPOCH = WS_FLAG3 + 8192;

DI unsigned rfl(unsigned x) { return __builtin_amdgcn_readfirstlane(x); }
DI unsigned cvtpk(float lo, float hi) { f32x2 v = {lo, hi}; bf16x2 b = __builtin_convertvector(v, bf16x2); return __builtin_bit_cast(unsigned, b); }
DI bf16x8 lds_rd128(unsigned a) { return *(const LAS bf16x8*)(unsigned long)a; }
DI f32x4 lds_rdf4(unsigned a) { return *(const LAS f32x4*)(unsigned long)a; }
DI void lds_wrf4(unsigned a, f32x4 v) { *(LAS f32x4*)(unsigned long)a = v; }
DI void lds_wru4(unsigned a, u32x4 v) { *(LAS u32x4*)(unsigned long)a = v; }
DI s16x4 lds_tr(unsigned a) { return __builtin_amdgcn_ds_read_tr16_b64_v4i16((LAS s16x4*)(unsigned long)a); }
DI bf16x8 cat8(s16x4 lo, s16x4 hi) { return __builtin_bit_cast(bf16x8, __builtin_shufflevector(lo, hi, 0, 1, 2, 3, 4, 5, 6, 7)); }
DI float max3f(float a, float b, float c) { float r; asm("v_max3_f32 %0, %1, %2, %3" : "=v"(r) : "v"(a), "v"(b), "v"(c)); return r; }
DI float max2f(float a, float b) { float r; asm("v_max_f32_e32 %0, %1, %2" : "=v"(r) : "v"(a), "v"(b)); return r; }
DI float swap_max(float m) { auto rr = __builtin_amdgcn_permlane32_swap(__float_as_uint(m), __float_as_uint(m), false, false); return max2f(__uint_as_float(rr[0]), __uint_as_float(rr[1])); }
DI float swap_add(float m) { auto rr = __builtin_amdgcn_permlane32_swap(__float_as_uint(m), __float_as_uint(m), false, false); return __uint_as_float(rr[0]) + __uint_as_float(rr[1]); }
DI float max8lo(const f32x16& p) { float a = max3f(p[0], p[1], p[2]), b = max3f(p[3], p[4], p[5]); return max3f(max3f(a, p[6], p[7]), b, b); }
DI float max8hi(float m, const f32x16& p) { float a = max3f(m, p[8], p[9]), b = max3f(p[10], p[11], p[12]); a = max3f(a, p[13], p[14]); return max3f(a, b, p[15]); }
DI void scale_acc(f32x16& x, float f) {
#pragma unroll
  for (int r = 0; r < 16; ++r) { float e = x[r], t;
    asm volatile("v_accvgpr_read_b32 %1, %0\n\tv_mul_f32 %1, %1, %2\n\tv_accvgpr_write_b32 %0, %1" : "+a"(e), "=&v"(t) : "v"(f));
    x[r] = e; }
}
DI void sub_tile(f32x16& x, float d) {
#pragma unroll
  for (int r = 0; r < 16; ++r) { float e = x[r]; asm volatile("v_sub_f32_e32 %0, %0, %1" : "+v"(e) : "v"(d)); x[r] = e; }
}
DI u64 mix64(u64 z) { z += 0x9E3779B97F4A7C15ull; z = (z ^ (z >> 30)) * 0xBF58476D1CE4E5B9ull; z = (z ^ (z >> 27)) * 0x94D049BB133111EBull; return z ^ (z >> 31); }

#define SETM0(x) asm volatile("s_mov_b32 m0, %0" :: "s"(x) : "memory")
#define SETM0N(x) asm volatile("s_mov_b32 m0, %0\n\ts_nop 0" :: "s"(x) : "memory")
template <int OFF, bool NT> DI void dma2(unsigned vo0, unsigned vo1, srd_t srd, unsigned soff) {
  if constexpr (NT)
    asm volatile("buffer_load_dwordx4 %0, %2, %3 offen offset:%4 nt lds\n\tbuffer_load_dwordx4 %1, %2, %3 offen offset:%5 nt lds"
                 :: "v"(vo0), "v"(vo1), "s"(srd), "s"(soff), "i"(OFF), "i"(OFF + 1024) : "memory");
  else
    asm volatile("buffer_load_dwordx4 %0, %2, %3 offen offset:%4 lds\n\tbuffer_load_dwordx4 %1, %2, %3 offen offset:%5 lds"
                 :: "v"(vo0), "v"(vo1), "s"(srd), "s"(soff), "i"(OFF), "i"(OFF + 1024) : "memory");
}
template <int OFF, bool NT> DI void dma1(unsigned vo, srd_t srd, unsigned soff) {
  if constexpr (NT) asm volatile("buffer_load_dwordx4 %0, %1, %2 offen offset:%3 nt lds" :: "v"(vo), "s"(srd), "s"(soff), "i"(OFF) : "memory");
  else asm volatile("buffer_load_dwordx4 %0, %1, %2 offen offset:%3 lds" :: "v"(vo), "s"(srd), "s"(soff), "i"(OFF) : "memory");
}
template <bool NT> DI void dma4(unsigned m0v, const unsigned (&vo)[4], srd_t srd, unsigned soff) { SETM0N(m0v); dma2<0, NT>(vo[0], vo[1], srd, soff); dma2<2048, NT>(vo[2], vo[3], srd, soff); }
template <int N> DI void waitv() { asm volatile("s_waitcnt vmcnt(%0)" :: "i"(N > 63 ? 63 : N) : "memory"); }
#define WAITL() do { __builtin_amdgcn_s_waitcnt(0xC07F); asm volatile("" ::: "memory"); } while (0)
#define BARRIER() do { asm volatile("" ::: "memory"); WAITL(); asm volatile("s_barrier" ::: "memory"); } while (0)

template <int SLP> DI void poll_flags(GAS u64* f, u64 tag) {
#pragma unroll 1
  for (unsigned spins = 0; spins < (1u << 21); ++spins) {
    const bool ok = (__hip_atomic_load(f, RLX_AGENT) == tag);
    if (__all(ok)) break;
    __builtin_amdgcn_s_sleep(SLP);
  }
  asm volatile("" ::: "memory");
}

#define STAMP(i)
#define STAMP_DECL(n)
#define STAMP_OUT(base, widx, id0, n)
#define KVREC (512 * 256)

#define NT_KB 1
#define NT_VB 1
#define NT_KA 1
#define NT_VA 1
#define NT_WO 0
#define PSLP 4
#define TSIG 12u
#define TCH_END 0
#define TCH_B 0
#define TCH_WO 0
#define TCH_KV 2
struct P1 {
  const char *xA, *kA, *vA, *wqA, *woA, *xB, *kB, *vB, *wqB, *woB;
  char* ws;
  char* out;
};

DI u32x4 pack8(const f32x16& x, int s, float c) {
  return u32x4{cvtpk(x[8 * s] * c, x[8 * s + 1] * c), cvtpk(x[8 * s + 2] * c, x[8 * s + 3] * c), cvtpk(x[8 * s + 4] * c, x[8 * s + 5] * c), cvtpk(x[8 * s + 6] * c, x[8 * s + 7] * c)};
}
DI void oload4(bf16x8* x, unsigned vo, srd_t srd, unsigned soff) {
  asm volatile("buffer_load_dwordx4 %0, %4, %5, %6 offen offset:0 sc1\n\t"
               "buffer_load_dwordx4 %1, %4, %5, %6 offen offset:1024 sc1\n\t"
               "buffer_load_dwordx4 %2, %4, %5, %6 offen offset:2048 sc1\n\t"
               "buffer_load_dwordx4 %3, %4, %5, %6 offen offset:3072 sc1"
               : "=&v"(x[0]), "=&v"(x[1]), "=&v"(x[2]), "=&v"(x[3]) : "v"(vo), "s"(srd), "s"(soff) : "memory");
}
template <int N> DI void wait_o8(bf16x8* x, bf16x8* y) {
  asm volatile("s_waitcnt vmcnt(%8)" : "+v"(x[0]), "+v"(x[1]), "+v"(x[2]), "+v"(x[3]), "+v"(y[0]), "+v"(y[1]), "+v"(y[2]), "+v"(y[3]) : "i"(N) : "memory");
}

constexpr unsigned REG = 5u * SLOT;

template <bool BB> DI void body(const P1& p, const unsigned ldsb) {
  STAMP_DECL(16); STAMP(0);
  constexpr unsigned NS = BB ? 4u : 8u;
  constexpr int PE = BB ? 8 : 4;
  constexpr bool NTK = BB ? NT_KB : NT_KA, NTV = BB ? NT_VB : NT_VA;
  const unsigned tid = threadIdx.x, lane = tid & 63u, wid = rfl(tid >> 6);
  const unsigned g8 = blockIdx.x & 7u, idx = (blockIdx.x >> 3) & 15u;
  const unsigned half = wid & 1u, item = wid >> 1;
  const unsigned h0 = BB ? 4u * g8 + (idx & 3u) : 4u * g8 + 2u * (idx & 1u), b0 = BB ? 2u * (idx >> 2) : (idx >> 1);
  const unsigned h = BB ? h0 : h0 + item, b = BB ? b0 + item : b0;
  const unsigned l31 = lane & 31u, hh = lane >> 5, l15 = lane & 15u, g4 = lane >> 4;
  const unsigned reg = ldsb + wid * REG, preg = ldsb + (wid ^ 1u) * REG;
  const unsigned ring = BB ? reg : ldsb + half * (2u * REG);
  const unsigned dsub = BB ? 0u : item * 4096u;

  u64 epoch_v;
  asm volatile("global_load_dwordx2 %0, %1, off sc1" : "=v"(epoch_v) : "v"(p.ws + WS_EPOCH) : "memory");
  const size_t kvoff = BB ? ((size_t)(b * 32u + h) * 1024u + half * 512u) * 256u : ((size_t)(b * 8u + g8) * 1024u + half * 512u) * 256u;
  const srd_t sK = MKSRD((BB ? p.kB : p.kA) + kvoff, KVREC), sV = MKSRD((BB ? p.vB : p.vA) + kvoff, KVREC);
  unsigned kvo[4];
#pragma unroll
  for (int i = 0; i < 4; ++i) kvo[i] = g4 * 256u + (((l15 ^ g4) ^ (4u * i)) * 16u);
  const unsigned vvo = g4 * 256u + ((l15 ^ (g4 << 2)) * 16u);
  const unsigned vv4[4] = {vvo, vvo, vvo, vvo};
#define ELEM_H(n, j) do { if ((n) < 2 || ((n) & 1)) dma4<NTK>(ring + ((n) % NS) * SLOT + (j), kvo, sK, ((n) < 2 ? (n) : ((n) + 1) / 2) * 8192u + (j)); \
                          else dma4<NTV>(ring + ((n) % NS) * SLOT + (j), vv4, sV, ((n) - 2) / 2 * 8192u + (j)); } while (0)
#define ELEM(n) do { if (BB) { ELEM_H(n, 0u); ELEM_H(n, 4096u); } else ELEM_H(n, dsub); } while (0)

  bf16x8 qf[2][8];
  f32x16 o[4][2];
  {
    constexpr int QE = BB ? 8 : 10;
    const unsigned rot = BB ? 2u * ((idx >> 2) + idx) : idx + (idx >> 3);
    const srd_t sw = MKSRD((BB ? p.wqB : p.wqA) + h0 * 256u, 4 << 20), sx = MKSRD((BB ? p.xB : p.xA) + (size_t)b0 * 65536u, BB ? 131072 : 65536);
    unsigned wvo[4], xvo[4];
#pragma unroll
    for (int i = 0; i < 4; ++i) {
      wvo[i] = g4 * 8192u + ((l15 ^ (g4 << 2)) * 16u) + i * (32768u - 1024u);
      xvo[i] = (lane >> 3) * 1024u + (((lane & 7u) ^ ((4u * i + g4) & 7u)) * 16u) + i * (8192u - 1024u);
    }
#define WOFF(s) ((BB ? 1u + ((s) + 3) % 4 : ((s) % 4 == 0 ? 0u : (s) % 4 == 1 ? 8u : (s) % 4 == 2 ? 3u : 6u)) * SLOT)
#define XOFF(s) ((BB ? 1u + ((s) + 3) % 4 : ((s) % 4 == 0 ? 2u : (s) % 4 == 1 ? 12u : (s) % 4 == 2 ? 5u : 15u)) * SLOT)
    const unsigned wdst = ldsb + (BB ? item * REG + half * 4096u : wid * 4096u), xdst = ldsb + (BB ? (2u + item) * REG + half * 4096u : wid * 2048u);
#define QLD1(s, j) do { const unsigned cv_ = ((s) + rot) & 7u; \
      if ((j) == 0) dma4<false>(wdst + WOFF(s), wvo, sw, (cv_ * 64u + wid * 16u) * 8192u); \
      if ((j) == 1) { if (BB) dma4<false>(xdst + XOFF(s), xvo, sx, wid * 32768u + cv_ * 128u); \
                      else dma4<false>(wdst + WOFF(s) + 2u * REG, wvo, sw, (cv_ * 64u + wid * 16u) * 8192u + 256u); } \
      if ((j) == 2 && !BB) { SETM0N(xdst + XOFF(s)); dma2<0, false>(xvo[0], xvo[1], sx, wid * 16384u + cv_ * 128u); } } while (0)
#define QLD(s) do { QLD1(s, 0); QLD1(s, 1); QLD1(s, 2); } while (0)
    const unsigned p4 = lane & 3u, q4 = (lane >> 2) & 3u;
    const unsigned wa0 = ((ldsb + (8u * hh + q4) * 256u + (q4 << 6) + (g4 & 1u) * 32u + (((p4 & 1u) << 1) | (p4 >> 1)) * 8u) ^ (128u * half)) + (BB ? 0u : item * (2u * REG)), wa1 = wa0 ^ 64u;
    const unsigned xa = ldsb + (BB ? (2u + item) * REG : 0u) + l31 * 128u + ((hh ^ ((l31 >> 1) & 7u)) * 16u);
    const f32x16 z16 = {};
    f32x16 acc[2][2];
    bf16x8 xf[2][2][4], wf[2][2][4];
#define QM1(s, ks, g) acc[(g) >> 1][(g) & 1] = MFMA32(wf[(s) & 1][(g) & 1][ks], xf[(s) & 1][(g) >> 1][ks], ((s) == 0 && (ks) == 0) ? z16 : acc[(g) >> 1][(g) & 1])
#define QMM(s, ks) do { QM1(s, ks, 0); QM1(s, ks, 1); QM1(s, ks, 2); QM1(s, ks, 3); } while (0)
#define WKS(ks) (BB ? ((ks) >> 1) * REG + ((ks) & 1) * 4096u : (ks) * 4096u)
#define DG(m0v, vo, srd, soff, NT, cnt, g) do { if ((g) < 0) SETM0(m0v); else if ((g) < (cnt)) dma1<((g) & 3) * 1024, NT>(vo[(g) & 3], srd, soff); } while (0)
#define QG(s, j, g) do { const unsigned cv_ = ((s) + rot) & 7u; \
      if ((j) == 0) DG(wdst + WOFF(s), wvo, sw, (cv_ * 64u + wid * 16u) * 8192u, false, 4, g); \
      if ((j) == 1) { if (BB) DG(xdst + XOFF(s), xvo, sx, wid * 32768u + cv_ * 128u, false, 4, g); \
                      else DG(wdst + WOFF(s) + 2u * REG, wvo, sw, (cv_ * 64u + wid * 16u) * 8192u + 256u, false, 4, g); } \
      if ((j) == 2 && !BB) DG(xdst + XOFF(s), xvo, sx, wid * 16384u + cv_ * 128u, false, 2, g); } while (0)
#define EG(n, j, g) do { if ((n) < 2 || ((n) & 1)) DG(ring + ((n) % NS) * SLOT + (j), kvo, sK, ((n) < 2 ? (n) : ((n) + 1) / 2) * 8192u + (j), NTK, 4, g); \
                         else DG(ring + ((n) % NS) * SLOT + (j), vv4, sV, ((n) - 2) / 2 * 8192u + (j), NTV, 4, g); } while (0)
#define QOP(HOOK, NOP, G, v) do { if ((v) < (NOP) && ((v) * 16) / (NOP) == (G)) { HOOK((v) >> 2, (v) & 3); if (((v) & 3) == 3 && (v) + 1 < (NOP)) { HOOK(((v) >> 2) + 1, -1); } } } while (0)
#define QGAP(s, ks, g, HOOK, NOP) do { if ((s) > 0) QM1((s) - 1, ks, g); else asm volatile("s_nop 0"); \
        if ((g) == 0) { xf[(s) & 1][0][ks] = lds_rd128(x_ ^ (32u * (ks))); xf[(s) & 1][1][ks] = lds_rd128((x_ ^ (32u * (ks))) + 4096u); } \
        if ((g) == 1) wf[(s) & 1][0][ks] = cat8(lds_tr(w0_ + WKS(ks)), lds_tr(w0_ + WKS(ks) + 1024u)); \
        if ((g) == 2) wf[(s) & 1][1][ks] = cat8(lds_tr(w1_ + WKS(ks)), lds_tr(w1_ + WKS(ks) + 1024u)); \
        SBAR(); \
        QOP(HOOK, NOP, 4 * (ks) + (g), 0); QOP(HOOK, NOP, 4 * (ks) + (g), 1); QOP(HOOK, NOP, 4 * (ks) + (g), 2); QOP(HOOK, NOP, 4 * (ks) + (g), 3); \
        QOP(HOOK, NOP, 4 * (ks) + (g), 4); QOP(HOOK, NOP, 4 * (ks) + (g), 5); QOP(HOOK, NOP, 4 * (ks) + (g), 6); QOP(HOOK, NOP, 4 * (ks) + (g), 7); \
        QOP(HOOK, NOP, 4 * (ks) + (g), 8); QOP(HOOK, NOP, 4 * (ks) + (g), 9); QOP(HOOK, NOP, 4 * (ks) + (g), 10); QOP(HOOK, NOP, 4 * (ks) + (g), 11); \
        QOP(HOOK, NOP, 4 * (ks) + (g), 12); QOP(HOOK, NOP, 4 * (ks) + (g), 13); QOP(HOOK, NOP, 4 * (ks) + (g), 14); QOP(HOOK, NOP, 4 * (ks) + (g), 15); \
        SBAR(); } while (0)
#define QKS(s, ks, HOOK, NOP) do { QGAP(s, ks, 0, HOOK, NOP); QGAP(s, ks, 1, HOOK, NOP); QGAP(s, ks, 2, HOOK, NOP); QGAP(s, ks, 3, HOOK, NOP); } while (0)
#define QCMP(s, HOOK, NOP) do { const unsigned w0_ = wa0 + WOFF(s), w1_ = wa1 + WOFF(s), x_ = xa + XOFF(s); HOOK(0, -1); SBAR(); \
      QKS(s, 0, HOOK, NOP); QKS(s, 1, HOOK, NOP); QKS(s, 2, HOOK, NOP); QKS(s, 3, HOOK, NOP); } while (0)
#define HQ0(j, g) QG(3, j, g)
#define HQ1(j, g) QG(4, j, g)
#define HQ2(j, g) QG(5, j, g)
#define HQ3(j, g) QG(6, j, g)
#define HQ4(j, g) QG(7, j, g)
#define HQ5(j, g) do { if (BB) { if ((j) == 0) EG(0, 0u, g); if ((j) == 1) EG(0, 4096u, g); } else { if ((j) == 0) EG(0, dsub, g); if ((j) == 1) EG(1, dsub, g); } } while (0)
#define HQ6(j, g) do { if (BB) { if ((j) == 0) EG(1, 0u, g); if ((j) == 1) EG(1, 4096u, g); } else { if ((j) == 0) EG(2, dsub, g); } } while (0)
#define HQ7(j, g) do { if (BB) { if ((j) == 0) EG(2, 0u, g); if ((j) == 1) EG(2, 4096u, g); } else { if ((j) == 0) EG(3, dsub, g); if ((j) == 1) EG(4, dsub, g); } } while (0)
    unsigned tch = 0u;
#define TOUCH(srd, soff, stride) asm volatile("buffer_load_dword %0, %1, %2, %3 offen" : "+v"(tch) : "v"(lane * (stride)), "s"(srd), "s"(soff) : "memory")
#define TB(i) do { if (BB && TCH_B) TOUCH(((i) & 2) ? sV : sK, ((i) & 2) ? ((i) & 1) * 4096u : 8192u + ((i) >> 2) * 8192u + ((i) & 1) * 4096u, 64u); } while (0)
    constexpr int NTCH = BB ? 0 : TCH_WO + TCH_KV, TE = (BB && TCH_B) ? 1 : 0;
    QLD(0); QLD(1);
#pragma unroll
    for (int db = 0; db < 4; ++db) { o[db][0] = f32x16{}; o[db][1] = f32x16{}; asm volatile("" : "+a"(o[db][0]), "+a"(o[db][1])); }
    QLD(2);
    if (!BB) {
      const srd_t st = MKSRD((half ? p.woB : p.woA) + (size_t)(g8 >> 1) * 1048576u + (TCH_WO == 4 ? (idx * 2u + item) * 32768u : ((g8 & 1u) * 32u + idx * 2u + item) * 16384u), 32768);
#pragma unroll
      for (int i = 0; i < TCH_WO; ++i) TOUCH(st, i * 8192u, 128u);
#pragma unroll
      for (int i = 0; i < TCH_KV; ++i) TOUCH(item ? sV : sK, (2u * i + (idx & 1u)) * 8192u, 128u);
    }
    TB(0);
    waitv<2 * QE + NTCH + TE>(); BARRIER(); QCMP(0, HQ0, QE); TB(1);
    STAMP(1);
    waitv<2 * QE + NTCH + 2 * TE>(); BARRIER(); QCMP(1, HQ1, QE); TB(2);
    waitv<2 * QE + NTCH + 3 * TE>(); BARRIER(); QCMP(2, HQ2, QE); TB(3);
    waitv<2 * QE + 3 * TE>(); BARRIER(); QCMP(3, HQ3, QE); TB(4);
    waitv<2 * QE + 3 * TE>(); BARRIER(); QCMP(4, HQ4, QE);
    waitv<2 * QE + 2 * TE>(); BARRIER(); QCMP(5, HQ5, 8);
    waitv<QE + 8 + TE>(); BARRIER(); QCMP(6, HQ6, (BB ? 8 : 4));
    waitv<BB ? 16 : 12>(); BARRIER(); QCMP(7, HQ7, 8);
    if (NTCH + TE > 0) PIN(tch);
    STAMP(2);
    const unsigned sp = BB ? reg + 4u * SLOT : ldsb + item * (2u * REG) + 8u * SLOT + half * SLOT, psp = BB ? preg + 4u * SLOT : sp ^ SLOT;
#define QOUT(g) do { lds_wru4(sp + (g) * 2048u + lane * 16u, pack8(acc[(g) >> 1][(g) & 1], 0, C2)); lds_wru4(sp + (g) * 2048u + 1024u + lane * 16u, pack8(acc[(g) >> 1][(g) & 1], 1, C2)); } while (0)
#define QLAST(g) do { QM1(7, 0, g); QM1(7, 1, g); QM1(7, 2, g); QM1(7, 3, g); } while (0)
    QLAST(0); SBAR(); QLAST(1); SBAR(); QLAST(2); QOUT(0); SBAR(); QLAST(3); QOUT(1); SBAR(); QOUT(2); QOUT(3);
    BARRIER();
    const unsigned lo = (half ? psp : sp) + lane * 16u, hi = (half ? sp : psp) + lane * 16u, q0 = half * 4096u, q1 = 4096u - q0;
#pragma unroll
    for (int j = 0; j < 4; ++j) {
      qf[0][j] = lds_rd128(lo + q0 + j * 1024u); qf[0][4 + j] = lds_rd128(hi + q0 + j * 1024u);
      qf[1][j] = lds_rd128(lo + q1 + j * 1024u); qf[1][4 + j] = lds_rd128(hi + q1 + j * 1024u);
    }
  }
  PIN(epoch_v);
  const u64 tag = mix64(((u64)rfl((unsigned)(epoch_v >> 32)) << 32) | rfl((unsigned)epoch_v));

  unsigned tA = (NS / 2u + 1u) * 8192u + dsub, tB = (NS / 2u) * 8192u + dsub;
  const unsigned dring = ring + dsub;
  const unsigned kb = ring + l31 * 256u + ((hh ^ l15) * 16u);
  const unsigned vb = ring + (4u * hh + ((lane >> 2) & 3u)) * 256u + (((lane >> 2) & 3u) << 6) + (g4 & 1u) * 32u + (lane & 3u) * 8u;
  f32x16 sA[2], sB[2], negm[2];
  bf16x8 kf[8]; s16x4 vlo[4][2], vhi[4][2]; u32x4 pw[2][2];
  float f0 = 1.f, f1 = 1.f, ls0 = 0.f, lt0 = 0.f, ls1 = 0.f, lt1 = 0.f;
#define QK0(D, a, bq) asm volatile("v_mfma_f32_32x32x16_bf16 %0, %1, %2, 0" : "=&v"(D) : "v"(a), "a"(bq))
#define QKC(D, a, bq, C) asm volatile("v_mfma_f32_32x32x16_bf16 %0, %1, %2, %3" : "=&v"(D) : "v"(a), "a"(bq), "v"(C))
#define QKA(D, a, bq) asm volatile("v_mfma_f32_32x32x16_bf16 %0, %1, %2, %0" : "+v"(D) : "v"(a), "a"(bq))
#define WAITE(k) do { waitv<PE * (k)>(); if (BB) { WAITL(); } else { BARRIER(); } } while (0)
#define WAITR() WAITE(NS - 2)
#define EXP2(x) __builtin_amdgcn_exp2f(x)

  STAMP(3);
  WAITE(BB ? 2 : 4);
#pragma unroll
  for (int s = 0; s < 8; ++s) kf[s] = lds_rd128(kb ^ (32u * s));
  QK0(sA[0], kf[0], qf[0][0]); QK0(sA[1], kf[0], qf[1][0]);
#pragma unroll
  for (int s = 1; s < 8; ++s) { QKA(sA[0], kf[s], qf[0][s]); QKA(sA[1], kf[s], qf[1][s]);
    if (s == 2) { if (BB) ELEM(3); else ELEM(5); } if (s == 4 && !BB) ELEM(6); if (s == 6 && !BB) ELEM(7); }
  WAITR();
  if (BB) asm volatile("s_barrier" ::: "memory");
  ELEM(NS);
#pragma unroll
  for (int s = 0; s < 8; ++s) kf[s] = lds_rd128((kb + SLOT) ^ (32u * s));
  asm volatile("s_nop 7\n\ts_nop 7" : "+v"(sA[0]), "+v"(sA[1]));
#pragma unroll
  for (int qb = 0; qb < 2; ++qb) {
    const float m0 = swap_max(max8hi(max8lo(sA[qb]), sA[qb]));
#pragma unroll
    for (int r = 0; r < 16; ++r) { sA[qb][r] -= m0; negm[qb][r] = -m0; }
    PIN(negm[qb]);
#pragma unroll
    for (int r = 0; r < 8; ++r) sA[qb][r] = EXP2(sA[qb][r]);
  }
#pragma unroll
  for (int r = 0; r < 8; r += 2) { ls0 += sA[0][r]; lt0 += sA[0][r + 1]; ls1 += sA[1][r]; lt1 += sA[1][r + 1]; }

  unsigned so1 = 1u * SLOT, so2 = 2u * SLOT, so3 = 3u * SLOT;
#define ADV2(x) x = (x + 2u * SLOT) & (NS * SLOT - 1u)
#define VRD(db, ks) do { const unsigned a_ = (va ^ (64u * (db))) + 4096u * (ks); vlo[db][ks] = lds_tr(a_); vhi[db][ks] = lds_tr(a_ + 2048u); } while (0)
#define GAPA(C, N, s, QB, LAST) do { \
    if ((s) == 0) QKC(N[QB], kf[0], qf[QB][0], negm[QB]); else QKA(N[QB], kf[s], qf[QB][s]); \
    if ((s) < 4) { C[QB][8 + 2 * (s)] = EXP2(C[QB][8 + 2 * (s)]); C[QB][9 + 2 * (s)] = EXP2(C[QB][9 + 2 * (s)]); pw[QB][0][(s) & 3] = cvtpk(C[QB][2 * (s)], C[QB][2 * (s) + 1]); PIN(C[QB]); PIN(pw[QB][0]); } \
    else { ls##QB += C[QB][2 * (s)]; lt##QB += C[QB][2 * (s) + 1]; pw[QB][1][(s) & 3] = cvtpk(C[QB][2 * (s)], C[QB][2 * (s) + 1]); PIN(pw[QB][1]); PIN(ls##QB); PIN(lt##QB); } \
    if ((s) < 4) { VRD((2 * (s) + QB) & 3, ((s) >> 1) & 1); } \
    if (BB && QB == 0 && (s) == 6) SETM0(dring + so1 + 4096u); \
    if (LAST) { if (BB) { \
      if (QB == 1 && (s) == 4) dma2<0, NT_WO>(wvo[0], wvo[1], sW, wso); \
      if (QB == 1 && (s) == 5) dma2<2048, NT_WO>(wvo[2], wvo[3], sW, wso); \
      if (QB == 1 && (s) == 6) dma2<0, NT_WO>(wvo[0], wvo[1], sW, wso + 32768u); \
      if (QB == 1 && (s) == 7) dma2<2048, NT_WO>(wvo[2], wvo[3], sW, wso + 32768u); } \
    } else { \
      if (QB == 1 && (s) == 4) dma2<0, NTK>(kvo[0], kvo[1], sK, tA); \
      if (QB == 1 && (s) == 5) dma2<2048, NTK>(kvo[2], kvo[3], sK, tA); \
      if (BB && QB == 1 && (s) == 6) dma2<0, NTK>(kvo[0], kvo[1], sK, tA + 4096u); \
      if (BB && QB == 1 && (s) == 7) dma2<2048, NTK>(kvo[2], kvo[3], sK, tA + 4096u); } \
    SBAR(); } while (0)
#define PHASE_A(C, N, L) do { \
    SETM0(dring + so1); WAITR(); SBAR(); \
    const unsigned va = vb + so2; \
    GAPA(C, N, 0, 0, L); GAPA(C, N, 0, 1, L); GAPA(C, N, 1, 0, L); GAPA(C, N, 1, 1, L); GAPA(C, N, 2, 0, L); GAPA(C, N, 2, 1, L); GAPA(C, N, 3, 0, L); GAPA(C, N, 3, 1, L); \
    GAPA(C, N, 4, 0, L); GAPA(C, N, 4, 1, L); GAPA(C, N, 5, 0, L); GAPA(C, N, 5, 1, L); GAPA(C, N, 6, 0, L); GAPA(C, N, 6, 1, L); GAPA(C, N, 7, 0, L); GAPA(C, N, 7, 1, L); \
    tA += 8192u; } while (0)
#define PVM(i) o[((i) >> 1) & 3][(i) & 1] = MFMA32(cat8(vlo[((i) >> 1) & 3][(i) >> 3], vhi[((i) >> 1) & 3][(i) >> 3]), __builtin_bit_cast(bf16x8, pw[(i) & 1][(i) >> 3]), o[((i) >> 1) & 3][(i) & 1])
#define KRD(i) kf[i] = lds_rd128(ka ^ (32u * (i)))
#define GAPE(N, j) do { PVM(8 + (j)); N[0][j] = EXP2(N[0][j]); N[1][j] = EXP2(N[1][j]); \
    if ((j) > 0) { if ((j) & 1) { ls0 += N[0][((j) + 15) & 15]; ls1 += N[1][((j) + 15) & 15]; } else { lt0 += N[0][((j) + 15) & 15]; lt1 += N[1][((j) + 15) & 15]; } } \
    PIN(N[0]); PIN(N[1]); PIN(ls0); PIN(ls1); PIN(lt0); PIN(lt1); SBAR(); } while (0)
#define DMAB(OFF, i0, i1, hi, LAST) do { if (LAST) { if (BB) dma2<OFF, NT_WO>(wvo[i0], wvo[i1], sW, wso + 65536u + (hi) * 32768u); } \
    else if (BB || !(hi)) dma2<OFF, NTV>(vvo, vvo, sV, tB + (hi) * 4096u); } while (0)
#define PHASE_B(N, LAST) do { \
    SETM0(dring + so2); if (LAST) { WAITL(); } else { WAITR(); } SBAR(); \
    const unsigned ka = kb + so3; \
    PVM(0); KRD(0); KRD(1); DMAB(0, 0, 1, 0, LAST); SBAR(); \
    PVM(1); KRD(2); KRD(3); DMAB(2048, 2, 3, 0, LAST); if (BB) SETM0(dring + so2 + 4096u); SBAR(); \
    PVM(2); KRD(4); float ma0 = max3f(N[0][0], N[0][1], N[0][2]), ma1 = max3f(N[1][0], N[1][1], N[1][2]), mb0 = max3f(N[0][3], N[0][4], N[0][5]), mb1 = max3f(N[1][3], N[1][4], N[1][5]); SBAR(); \
    PVM(3); KRD(5); ma0 = max3f(ma0, N[0][6], N[0][7]); ma1 = max3f(ma1, N[1][6], N[1][7]); mb0 = max3f(mb0, N[0][8], N[0][9]); mb1 = max3f(mb1, N[1][8], N[1][9]); SBAR(); \
    PVM(4); KRD(6); ma0 = max3f(ma0, N[0][10], N[0][11]); ma1 = max3f(ma1, N[1][10], N[1][11]); mb0 = max3f(mb0, N[0][12], N[0][13]); mb1 = max3f(mb1, N[1][12], N[1][13]); SBAR(); \
    PVM(5); ma0 = max3f(ma0, N[0][14], N[0][15]); ma1 = max3f(ma1, N[1][14], N[1][15]); const float mbb = max2f(mb0, mb1); KRD(7); SBAR(); \
    PVM(6); const float mall = max3f(ma0, ma1, mbb); DMAB(0, 0, 1, 1, LAST); const bool grew = __any(mall > THR); SBAR(); \
    PVM(7); DMAB(2048, 2, 3, 1, LAST); SBAR(); \
    if (__builtin_expect(grew, 0)) {     \
      const float d0 = max2f(swap_max(max2f(ma0, mb0)), 0.f), d1 = max2f(swap_max(max2f(ma1, mb1)), 0.f); \
      f0 = EXP2(-d0); f1 = EXP2(-d1); ls0 *= f0; lt0 *= f0; ls1 *= f1; lt1 *= f1; \
      sub_tile(N[0], d0); sub_tile(N[1], d1); sub_tile(negm[0], d0); sub_tile(negm[1], d1); \
    } \
    SBAR(); \
    GAPE(N, 0); GAPE(N, 1); GAPE(N, 2); GAPE(N, 3); GAPE(N, 4); GAPE(N, 5); GAPE(N, 6); GAPE(N, 7); \
    lt0 += N[0][7]; lt1 += N[1][7]; \
    if (__builtin_expect(grew, 0)) { \
      asm volatile("s_nop 7\n\ts_nop 7" ::: "memory"); \
      _Pragma("unroll") for (int db = 0; db < 4; ++db) { scale_acc(o[db][0], f0); scale_acc(o[db][1], f1); } \
    } \
    ADV2(so1); ADV2(so2); ADV2(so3); tB += 8192u; } while (0)

  const unsigned kq = g8 >> 1, ob = 4u * (g8 & 1u) + 2u * (idx >> 3) + item, nt = idx & 7u, wrot = 4u * (idx >> 3) + 2u * (g8 & 1u);
  unsigned wvo[4];
  const srd_t sW = MKSRD((BB ? p.woB : p.woA) + (size_t)kq * 1048576u + nt * 128u, 1048576 - 896);
  const unsigned wso = ((wid + wrot) & 7u) * 131072u;
#define WDMA(hi) do { _Pragma("unroll") for (int j_ = 0; j_ < 4; ++j_) dma4<NT_WO>(reg + (1u + 2u * (hi)) * SLOT + j_ * 4096u, wvo, sW, (wso ^ ((hi) * 524288u)) + j_ * 32768u); } while (0)
  STAMP(4);
  for (unsigned t = 0; t < 14u; t += 2u) {
    if (BB && t == TSIG && tid == 0u) __hip_atomic_store((GAS u64*)(p.ws + WS_FLAG3) + (size_t)blockIdx.x * 8u, tag, RLX_AGENT);
    if (t == 4u) { STAMP(12); } if (t == 8u) { STAMP(13); } if (t == 12u) { STAMP(14); }
    PHASE_A(sA, sB, false); PHASE_B(sB, false);
    PHASE_A(sB, sA, false); PHASE_B(sA, false);
  }
#pragma unroll
  for (int j = 0; j < 4; ++j) wvo[j] = (lane >> 3) * 1024u + (((lane & 7u) ^ ((g4 & 1u) << 2)) * 16u) + j * 7168u;
  if (BB && TSIG == 14u && tid == 0u) __hip_atomic_store((GAS u64*)(p.ws + WS_FLAG3) + (size_t)blockIdx.x * 8u, tag, RLX_AGENT);
  PHASE_A(sA, sB, true); PHASE_B(sB, true);
  STAMP(5);
  {
#pragma unroll
    for (int r = 8; r < 16; ++r) { sB[0][r] = EXP2(sB[0][r]); sB[1][r] = EXP2(sB[1][r]); ls0 += sB[0][r]; ls1 += sB[1][r]; }
#pragma unroll
    for (int i = 0; i < 8; ++i) { pw[0][i >> 2][i & 3] = cvtpk(sB[0][2 * i], sB[0][2 * i + 1]); pw[1][i >> 2][i & 3] = cvtpk(sB[1][2 * i], sB[1][2 * i + 1]); }
    PIN(pw[0][0]); PIN(pw[0][1]); PIN(pw[1][0]); PIN(pw[1][1]);
    SBAR();
    WAITE(BB ? 2 : 0);
    const unsigned va = vb + so2;
#pragma unroll
    for (int i = 0; i < 8; ++i) VRD(i & 3, i >> 2);
    PVM(1); PVM(3); PVM(5); PVM(7); PVM(9); PVM(11); PVM(13); PVM(15);
    if (BB) { WAITL(); } else { BARRIER(); }
    SBAR();
    PVM(0); PVM(2); PVM(4); PVM(6); PVM(8); PVM(10); PVM(12); PVM(14);
  }
  STAMP(6);
  const srd_t sO = MKSRD(p.ws + WS_O, 16 << 20);
  GAS u64* flags = (GAS u64*)(p.ws + WS_FLAG);
  {
    const float l0 = swap_add(ls0 + lt0), l1 = swap_add(ls1 + lt1);
    const float mhat[2] = {-negm[0][0], -negm[1][0]};
    const unsigned xo = reg + 3u * SLOT + lane * 16u, xi = preg + 3u * SLOT + lane * 16u;
    const unsigned mo = reg + lane * 8u, mi = preg + lane * 8u;
#pragma unroll
    for (int db = 0; db < 4; ++db)
#pragma unroll
      for (int r4 = 0; r4 < 4; ++r4) lds_wrf4(xo + (db * 4 + r4) * 1024u, f32x4{o[db][1][4 * r4], o[db][1][4 * r4 + 1], o[db][1][4 * r4 + 2], o[db][1][4 * r4 + 3]});
    *(LAS f32x2*)(unsigned long)mo = f32x2{mhat[1], l1};
    BARRIER();
    const f32x2 ml = *(const LAS f32x2*)(unsigned long)mi;
    const float mm = __builtin_fmaxf(mhat[0], ml[0]);
    float a0 = EXP2(mhat[0] - mm), a1 = EXP2(ml[0] - mm);
    const float inv = 1.0f / (a0 * l0 + a1 * ml[1]);
    a0 *= inv; a1 *= inv;
    const unsigned oidx = (((BB ? 1u : 0u) * 8u + b) * 32u + h) * 2u + half;
#pragma unroll
    for (int db = 0; db < 4; ++db)
#pragma unroll
      for (int ks = 0; ks < 2; ++ks) {
        const f32x4 x0 = lds_rdf4(xi + (db * 4 + 2 * ks) * 1024u), x1 = lds_rdf4(xi + (db * 4 + 2 * ks + 1) * 1024u);
        float y[8];
#pragma unroll
        for (int e = 0; e < 4; ++e) {
          y[e] = a0 * o[db][0][8 * ks + e] + a1 * x0[e];
          y[4 + e] = a0 * o[db][0][8 * ks + 4 + e] + a1 * x1[e];
        }
        u32x4 v = {cvtpk(y[0], y[1]), cvtpk(y[2], y[3]), cvtpk(y[4], y[5]), cvtpk(y[6], y[7])};
        __builtin_amdgcn_raw_buffer_store_b128(v, sO, oidx * 8192u + lane * 16u + (db * 2 + ks) * 1024u, 0, 16);
      }
    waitv<0>();
    if (lane == 0) __hip_atomic_store(flags + (size_t)oidx * 8u, tag, RLX_AGENT);
  }
  STAMP(7);
  {
    constexpr unsigned br = BB ? 1u : 0u;
    if (!BB && TCH_END > 0) {
      const unsigned hb = 4u * g8 + (idx & 3u), bb2 = 2u * (idx >> 2) + item;
      const size_t off = ((size_t)(bb2 * 32u + hb) * 1024u + half * 512u) * 256u;
      const srd_t tK = MKSRD(p.kB + off, 131072), tV = MKSRD(p.vB + off, 131072);
      unsigned tch = 0u;
#pragma unroll
      for (int i = 16 - TCH_END; i < 16; ++i) { TOUCH(tK, i * 8192u, 128u); TOUCH(tV, i * 8192u, 128u); }
      waitv<0>(); PIN(tch);
    }
    if (!BB && TSIG < 16u && wid == 0u) poll_flags<32>((GAS u64*)(p.ws + WS_FLAG3) + (size_t)(blockIdx.x - 128u) * 8u, tag);
    BARRIER();
    if (!BB) WDMA(0);
    WDMA(1);
    const unsigned q2 = (lane >> 2) & 3u;
    const unsigned tl = ldsb + half * (2u * REG) + SLOT + (4u * hh + q2) * 128u + (g4 & 1u) * 32u + (lane & 3u) * 8u;
    const unsigned tb0 = tl + (((kq ^ (q2 >> 1)) & 1u) * 64u), tb1 = tb0 ^ 64u;
    const bool owner = (half == (kq >> 1));
    GAS u64* flags2 = (GAS u64*)(p.ws + WS_FLAG2);
#define HSL(i) (((i) & 1) + 4 * ((i) >> 1))
#define WFRAG(tb, i, f) cat8(lds_tr((tb) + ((i) & 1) * REG + ((i) >> 1) * 16384u + (f) * 2048u), lds_tr((tb) + ((i) & 1) * REG + ((i) >> 1) * 16384u + (f) * 2048u + 1024u))
    const unsigned fidx = ((br * 8u + ob) * 32u + 8u * kq) * 2u;
    const unsigned hrot = 2u * half + wrot;
    poll_flags<PSLP>(flags + (size_t)(fidx + ((((lane >> 1) & 1u) + (lane & 4u) + hrot) & 7u) * 2u + (lane & 1u)) * 8u, tag);
    STAMP(8);
    bf16x8 of[3][16];
    const unsigned ovo = lane * 16u, obase = fidx * 8192u, q0 = half * 8192u, q1 = 8192u - q0;
#define OLDH(i, c) do { const unsigned s_ = obase + ((HSL(i) + hrot) & 7u) * 16384u + (c) * 4096u; oload4(&of[(i) % 3][4 * (c)], ovo, sO, s_ + q0); oload4(&of[(i) % 3][8 + 4 * (c)], ovo, sO, s_ + q1); } while (0)
    f32x16 acc[2][2];
#define OHALF(i, c, N) do { wait_o8<N>(&of[(i) % 3][4 * (c)], &of[(i) % 3][8 + 4 * (c)]); \
    _Pragma("unroll") for (int f = 4 * (c); f < 4 * (c) + 4; ++f) { \
      const bf16x8 w0 = wn[f & 3][0], w1 = wn[f & 3][1]; \
      if (2 * (i) + (c) < 7) { wn[f & 3][0] = WFRAG(tb0, (2 * (i) + (c) + 1) >> 1, (f + 4) & 7); wn[f & 3][1] = WFRAG(tb1, (2 * (i) + (c) + 1) >> 1, (f + 4) & 7); } \
      if ((i) == 0 && f == 0) { const f32x16 z_ = {}; acc[0][0] = MFMA32(w0, of[0][0], z_); acc[0][1] = MFMA32(w1, of[0][0], z_); acc[1][0] = MFMA32(w0, of[0][8], z_); acc[1][1] = MFMA32(w1, of[0][8], z_); } \
      else { acc[0][0] = MFMA32(w0, of[(i) % 3][f], acc[0][0]); acc[0][1] = MFMA32(w1, of[(i) % 3][f], acc[0][1]); acc[1][0] = MFMA32(w0, of[(i) % 3][8 + f], acc[1][0]); acc[1][1] = MFMA32(w1, of[(i) % 3][8 + f], acc[1][1]); } } \
    SBAR(); } while (0)
    OLDH(0, 0); OLDH(0, 1); OLDH(1, 0);
    asm volatile("s_barrier" ::: "memory");
    bf16x8 wn[4][2];
#pragma unroll
    for (int f = 0; f < 4; ++f) { wn[f][0] = WFRAG(tb0, 0, f); wn[f][1] = WFRAG(tb1, 0, f); }
    OHALF(0, 0, 16); OLDH(1, 1); SBAR();
    OHALF(0, 1, 16); OLDH(2, 0); SBAR();
    OHALF(1, 0, 16); OLDH(2, 1); SBAR();
    OHALF(1, 1, 16); OLDH(3, 0); SBAR();
    OHALF(2, 0, 16); OLDH(3, 1); SBAR();
    OHALF(2, 1, 16);
    OHALF(3, 0, 8);
    OHALF(3, 1, 0);
    STAMP(9);
    const unsigned xo = reg + lane * 16u, xi = preg + lane * 16u;
#pragma unroll
    for (int nb = 0; nb < 2; ++nb)
#pragma unroll
      for (int r4 = 0; r4 < 4; ++r4) lds_wrf4(xo + (nb * 4 + r4) * 1024u, f32x4{acc[1][nb][4 * r4], acc[1][nb][4 * r4 + 1], acc[1][nb][4 * r4 + 2], acc[1][nb][4 * r4 + 3]});
    BARRIER();
    const unsigned tidx = (br * 8u + ob) * 8u + nt;
    const unsigned pbase = (unsigned)WS_PART + tidx * 65536u + kq * 4096u + lane * 16u;
    f32x4 y0[4];
#pragma unroll
    for (int r4 = 0; r4 < 4; ++r4) {
      const f32x4 y1 = lds_rdf4(xi + (4 + r4) * 1024u) + f32x4{acc[0][1][4 * r4], acc[0][1][4 * r4 + 1], acc[0][1][4 * r4 + 2], acc[0][1][4 * r4 + 3]};
      __builtin_amdgcn_raw_buffer_store_b128(__builtin_bit_cast(u32x4, y1), sO, pbase + (2u * half + ((kq & 1u) ^ 1u)) * 16384u + r4 * 1024u, 0, 16);
    }
#pragma unroll
    for (int r4 = 0; r4 < 4; ++r4) y0[r4] = lds_rdf4(xi + r4 * 1024u) + f32x4{acc[0][0][4 * r4], acc[0][0][4 * r4 + 1], acc[0][0][4 * r4 + 2], acc[0][0][4 * r4 + 3]};
    if (!owner) {
#pragma unroll
      for (int r4 = 0; r4 < 4; ++r4) __builtin_amdgcn_raw_buffer_store_b128(__builtin_bit_cast(u32x4, y0[r4]), sO, pbase + (2u * half + (kq & 1u)) * 16384u + r4 * 1024u, 0, 16);
    }
    waitv<0>();
    if (lane == 0) __hip_atomic_store(flags2 + (size_t)((tidx * 2u + half) * 4u + kq) * 8u, tag, RLX_AGENT);
    STAMP(10);
    if (owner) {
      poll_flags<PSLP>(flags2 + (size_t)((tidx * 2u + half) * 4u + (lane & 3u)) * 8u, tag);
      STAMP(11);
      const unsigned rbase = (unsigned)WS_PART + tidx * 65536u + kq * 16384u + lane * 16u;
#pragma unroll
      for (int j = 1; j < 4; ++j)
#pragma unroll
        for (int r4 = 0; r4 < 4; ++r4) y0[r4] += __builtin_bit_cast(f32x4, __builtin_amdgcn_raw_buffer_load_b128(sO, rbase + (kq ^ j) * 4096u + r4 * 1024u, 0, 16));
      char* op = p.out + (size_t)(ob * 64u + half * 32u + l31) * 2048u + (br * 512u + nt * 64u + (kq & 1u) * 32u + 4u * hh) * 2u;
#pragma unroll
      for (int r4 = 0; r4 < 4; ++r4) *(u32x2*)(op + 16 * r4) = u32x2{cvtpk(y0[r4][0], y0[r4][1]), cvtpk(y0[r4][2], y0[r4][3])};
    } else if (!BB && blockIdx.x == 128u && item == 0u) {
#pragma unroll 1
      for (unsigned spins = 0; spins < (1u << 21); ++spins) {
        bool ok = true;
#pragma unroll
        for (int k = 0; k < 16; ++k) ok &= (__hip_atomic_load(flags + (size_t)(k * 64 + lane) * 8u, RLX_AGENT) == tag);
        if (__all(ok)) break;
        __builtin_amdgcn_s_sleep(16);
      }
      if (lane == 0) __hip_atomic_store((GAS u64*)(p.ws + WS_EPOCH), tag, RLX_AGENT);
    }
  }
  STAMP(15);
  STAMP_OUT(p.ws, blockIdx.x * 4u + wid, BB ? 0 : 16, 16);
}

__global__ __launch_bounds__(256, 1) void k1_attn(P1 p) {
  extern __shared__ __attribute__((aligned(16))) char lds[];
  const unsigned ldsb = (unsigned)(unsigned long)lds;
  if (blockIdx.x < 128u) body<true>(p, ldsb); else body<false>(p, ldsb);
}

extern "C" void kernel_launch(void* const* d_in, const int* in_sizes, int n_in,
                              void* d_out, int out_size, void* d_ws, size_t ws_size,
                              hipStream_t stream) {
  (void)in_sizes; (void)n_in; (void)out_size; (void)ws_size;
  (void)hipFuncSetAttribute((const void*)k1_attn, hipFuncAttributeMaxDynamicSharedMemorySize, K1_LDS);
  P1 p1;
  p1.xA = (const char*)d_in[0]; p1.kA = (const char*)d_in[1]; p1.vA = (const char*)d_in[2]; p1.wqA = (const char*)d_in[3]; p1.woA = (const char*)d_in[4];
  p1.xB = (const char*)d_in[5]; p1.kB = (const char*)d_in[6]; p1.vB = (const char*)d_in[7]; p1.wqB = (const char*)d_in[8]; p1.woB = (const char*)d_in[9];
  p1.ws = (char*)d_ws; p1.out = (char*)d_out;
  hipLaunchKernelGGL(k1_attn, dim3(256), dim3(256), K1_LDS, stream, p1);
}
```

```cpp
#include <hip/hip_runtime.h>
#include <hip/hip_bf16.h>

typedef float    f32x16 __attribute__((ext_vector_type(16)));
typedef float    f32x4  __attribute__((ext_vector_type(4)));
typedef float    f32x2  __attribute__((ext_vector_type(2)));
typedef __bf16   bf16x8 __attribute__((ext_vector_type(8)));
typedef __bf16   bf16x2 __attribute__((ext_vector_type(2)));
typedef short    s16x4  __attribute__((ext_vector_type(4)));
typedef unsigned u32x4  __attribute__((ext_vector_type(4)));
typedef unsigned u32x2  __attribute__((ext_vector_type(2)));
typedef unsigned long long u64;
typedef __amdgpu_buffer_rsrc_t srd_t;
#define DI __device__ __forceinline__
#define LAS __attribute__((address_space(3)))
#define GAS __attribute__((address_space(1)))
#define SBAR() __builtin_amdgcn_sched_barrier(0)
#define PIN(x) asm volatile("" : "+v"(x))
#define MFMA32(a, b, c) __builtin_amdgcn_mfma_f32_32x32x16_bf16(a, b, c, 0, 0, 0)
#define MFMA16(a, b, c) __builtin_amdgcn_mfma_f32_16x16x32_bf16(a, b, c, 0, 0, 0)
#define RLX_AGENT __ATOMIC_RELAXED, __HIP_MEMORY_SCOPE_AGENT
#define MKSRD(ptr, n) __builtin_amdgcn_make_buffer_rsrc((void*)(ptr), (short)0, (n), 0x00020000)

constexpr unsigned SLOT = 8192u;
constexpr int K1_LDS = 163840;
constexpr float C2 = 0.08838834764831845f * 1.4426950408889634f;
constexpr float THR = 8.0f;
constexpr size_t WS_O = 0;
constexpr size_t WS_PART = 8u << 20;
constexpr size_t WS_FLAG = 16u << 20;
constexpr size_t WS_FLAG2 = WS_FLAG + 65536;
constexpr size_t WS_FLAG3 = WS_FLAG2 + 65536;
constexpr size_t WS_EPOCH = WS_FLAG3 + 8192;

DI unsigned rfl(unsigned x) { return __builtin_amdgcn_readfirstlane(x); }
DI unsigned cvtpk(float lo, float hi) { f32x2 v = {lo, hi}; bf16x2 b = __builtin_convertvector(v, bf16x2); return __builtin_bit_cast(unsigned, b); }
DI bf16x8 lds_rd128(unsigned a) { return *(const LAS bf16x8*)(unsigned long)a; }
DI f32x4 lds_rdf4(unsigned a) { return *(const LAS f32x4*)(unsigned long)a; }
DI void lds_wrf4(unsigned a, f32x4 v) { *(LAS f32x4*)(unsigned long)a = v; }
DI void lds_wru4(unsigned a, u32x4 v) { *(LAS u32x4*)(unsigned long)a = v; }
DI s16x4 lds_tr(unsigned a) { return __builtin_amdgcn_ds_read_tr16_b64_v4i16((LAS s16x4*)(unsigned long)a); }
DI bf16x8 cat8(s16x4 lo, s16x4 hi) { return __builtin_bit_cast(bf16x8, __builtin_shufflevector(lo, hi, 0, 1, 2, 3, 4, 5, 6, 7)); }
DI float max3f(float a, float b, float c) { float r; asm("v_max3_f32 %0, %1, %2, %3" : "=v"(r) : "v"(a), "v"(b), "v"(c)); return r; }
DI float max2f(float a, float b) { float r; asm("v_max_f32_e32 %0, %1, %2" : "=v"(r) : "v"(a), "v"(b)); return r; }
DI float swap_max(float m) { auto rr = __builtin_amdgcn_permlane32_swap(__float_as_uint(m), __float_as_uint(m), false, false); return max2f(__uint_as_float(rr[0]), __uint_as_float(rr[1])); }
DI float swap_add(float m) { auto rr = __builtin_amdgcn_permlane32_swap(__float_as_uint(m), __float_as_uint(m), false, false); return __uint_as_float(rr[0]) + __uint_as_float(rr[1]); }
DI float max8lo(const f32x16& p) { float a = max3f(p[0], p[1], p[2]), b = max3f(p[3], p[4], p[5]); return max3f(max3f(a, p[6], p[7]), b, b); }
DI float max8hi(float m, const f32x16& p) { float a = max3f(m, p[8], p[9]), b = max3f(p[10], p[11], p[12]); a = max3f(a, p[13], p[14]); return max3f(a, b, p[15]); }
DI void scale_acc(f32x16& x, float f) {
#pragma unroll
  for (int r = 0; r < 16; ++r) { float e = x[r], t;
    asm volatile("v_accvgpr_read_b32 %1, %0\n\tv_mul_f32 %1, %1, %2\n\tv_accvgpr_write_b32 %0, %1" : "+a"(e), "=&v"(t) : "v"(f));
    x[r] = e; }
}
DI void sub_tile(f32x16& x, float d) {
#pragma unroll
  for (int r = 0; r < 16; ++r) { float e = x[r]; asm volatile("v_sub_f32_e32 %0, %0, %1" : "+v"(e) : "v"(d)); x[r] = e; }
}
DI u64 mix64(u64 z) { z += 0x9E3779B97F4A7C15ull; z = (z ^ (z >> 30)) * 0xBF58476D1CE4E5B9ull; z = (z ^ (z >> 27)) * 0x94D049BB133111EBull; return z ^ (z >> 31); }

#define SETM0(x) asm volatile("s_mov_b32 m0, %0" :: "s"(x) : "memory")
#define SETM0N(x) asm volatile("s_mov_b32 m0, %0\n\ts_nop 0" :: "s"(x) : "memory")
template <int OFF, bool NT> DI void dma2(unsigned vo0, unsigned vo1, srd_t srd, unsigned soff) {
  if constexpr (NT)
    asm volatile("buffer_load_dwordx4 %0, %2, %3 offen offset:%4 nt lds\n\tbuffer_load_dwordx4 %1, %2, %3 offen offset:%5 nt lds"
                 :: "v"(vo0), "v"(vo1), "s"(srd), "s"(soff), "i"(OFF), "i"(OFF + 1024) : "memory");
  else
    asm volatile("buffer_load_dwordx4 %0, %2, %3 offen offset:%4 lds\n\tbuffer_load_dwordx4 %1, %2, %3 offen offset:%5 lds"
                 :: "v"(vo0), "v"(vo1), "s"(srd), "s"(soff), "i"(OFF), "i"(OFF + 1024) : "memory");
}
template <int OFF, bool NT> DI void dma1(unsigned vo, srd_t srd, unsigned soff) {
  if constexpr (NT) asm volatile("buffer_load_dwordx4 %0, %1, %2 offen offset:%3 nt lds" :: "v"(vo), "s"(srd), "s"(soff), "i"(OFF) : "memory");
  else asm volatile("buffer_load_dwordx4 %0, %1, %2 offen offset:%3 lds" :: "v"(vo), "s"(srd), "s"(soff), "i"(OFF) : "memory");
}
template <bool NT> DI void dma4(unsigned m0v, const unsigned (&vo)[4], srd_t srd, unsigned soff) { SETM0N(m0v); dma2<0, NT>(vo[0], vo[1], srd, soff); dma2<2048, NT>(vo[2], vo[3], srd, soff); }
template <int N> DI void waitv() { asm volatile("s_waitcnt vmcnt(%0)" :: "i"(N > 63 ? 63 : N) : "memory"); }
#define WAITL() do { __builtin_amdgcn_s_waitcnt(0xC07F); asm volatile("" ::: "memory"); } while (0)
#define BARRIER() do { asm volatile("" ::: "memory"); WAITL(); asm volatile("s_barrier" ::: "memory"); } while (0)

template <int SLP> DI void poll_flags(GAS u64* f, u64 tag) {
#pragma unroll 1
  for (unsigned spins = 0; spins < (1u << 21); ++spins) {
    const bool ok = (__hip_atomic_load(f, RLX_AGENT) == tag);
    if (__all(ok)) break;
    __builtin_amdgcn_s_sleep(SLP);
  }
  asm volatile("" ::: "memory");
}

#define STAMP(i)
#define STAMP_DECL(n)
#define STAMP_OUT(base, widx, id0, n)
#define KVREC (512 * 256)

#define NT_KB 1
#define NT_VB 1
#define NT_KA 1
#define NT_VA 1
#define NT_WO 0
#define KVROT 0
#define PSLP 4
#define TSIG 12u
#define ASLP 10
#define ADELAY 0
#define TCH_END 0
#define TCH_B 0
#define TCH_WO 0
#define TCH_KV 2
struct P1 {
  const char *xA, *kA, *vA, *wqA, *woA, *xB, *kB, *vB, *wqB, *woB;
  char* ws;
  char* out;
};

DI u32x4 pack8(const f32x16& x, int s, float c) {
  return u32x4{cvtpk(x[8 * s] * c, x[8 * s + 1] * c), cvtpk(x[8 * s + 2] * c, x[8 * s + 3] * c), cvtpk(x[8 * s + 4] * c, x[8 * s + 5] * c), cvtpk(x[8 * s + 6] * c, x[8 * s + 7] * c)};
}
DI void oload4(bf16x8* x, unsigned vo, srd_t srd, unsigned soff) {
  asm volatile("buffer_load_dwordx4 %0, %4, %5, %6 offen offset:0 sc1\n\t"
               "buffer_load_dwordx4 %1, %4, %5, %6 offen offset:1024 sc1\n\t"
               "buffer_load_dwordx4 %2, %4, %5, %6 offen offset:2048 sc1\n\t"
               "buffer_load_dwordx4 %3, %4, %5, %6 offen offset:3072 sc1"
               : "=&v"(x[0]), "=&v"(x[1]), "=&v"(x[2]), "=&v"(x[3]) : "v"(vo), "s"(srd), "s"(soff) : "memory");
}
template <int N> DI void wait_o8(bf16x8* x, bf16x8* y) {
  asm volatile("s_waitcnt vmcnt(%8)" : "+v"(x[0]), "+v"(x[1]), "+v"(x[2]), "+v"(x[3]), "+v"(y[0]), "+v"(y[1]), "+v"(y[2]), "+v"(y[3]) : "i"(N) : "memory");
}

constexpr unsigned REG = 5u * SLOT;

template <bool BB> DI void body(const P1& p, const unsigned ldsb) {
  STAMP_DECL(16); STAMP(0);
  constexpr unsigned NS = BB ? 4u : 8u;
  constexpr int PE = BB ? 8 : 4;
  constexpr bool NTK = BB ? NT_KB : NT_KA, NTV = BB ? NT_VB : NT_VA;
  const unsigned tid = threadIdx.x, lane = tid & 63u, wid = rfl(tid >> 6);
  const unsigned g8 = blockIdx.x & 7u, idx = (blockIdx.x >> 3) & 15u;
  const unsigned half = wid & 1u, item = wid >> 1;
  const unsigned h0 = BB ? 4u * g8 + (idx & 3u) : 4u * g8 + 2u * (idx & 1u), b0 = BB ? 2u * (idx >> 2) : (idx >> 1);
  const unsigned h = BB ? h0 : h0 + item, b = BB ? b0 + item : b0;
  const unsigned l31 = lane & 31u, hh = lane >> 5, l15 = lane & 15u, g4 = lane >> 4;
  const unsigned reg = ldsb + wid * REG, preg = ldsb + (wid ^ 1u) * REG;
  const unsigned ring = BB ? reg : ldsb + half * (2u * REG);
  const unsigned dsub = BB ? 0u : item * 4096u;

  u64 epoch_v;
  asm volatile("global_load_dwordx2 %0, %1, off sc1" : "=v"(epoch_v) : "v"(p.ws + WS_EPOCH) : "memory");
  const size_t kvoff = BB ? ((size_t)(b * 32u + h) * 1024u + half * 512u) * 256u : ((size_t)(b * 8u + g8) * 1024u + half * 512u) * 256u;
  const srd_t sK = MKSRD((BB ? p.kB : p.kA) + kvoff, KVREC), sV = MKSRD((BB ? p.vB : p.vA) + kvoff, KVREC);
  unsigned kvo[4];
#pragma unroll
  for (int i = 0; i < 4; ++i) kvo[i] = g4 * 256u + (((l15 ^ g4) ^ (4u * i)) * 16u);
  const unsigned vvo = g4 * 256u + ((l15 ^ (g4 << 2)) * 16u);
  const unsigned vv4[4] = {vvo, vvo, vvo, vvo};
  const unsigned rr = KVROT ? ((BB ? blockIdx.x * 4u + wid + (blockIdx.x >> 2) : g8 * 5u + b * 2u + half) & 15u) * 8192u : 0u;
#define RO(x) ((((x) + rr) & 0x1ffffu) | ((x) & 0x20000u))
#define ELEM_H(n, j) do { if ((n) < 2 || ((n) & 1)) dma4<NTK>(ring + ((n) % NS) * SLOT + (j), kvo, sK, RO(((n) < 2 ? (n) : ((n) + 1) / 2) * 8192u + (j))); \
                          else dma4<NTV>(ring + ((n) % NS) * SLOT + (j), vv4, sV, RO(((n) - 2) / 2 * 8192u + (j))); } while (0)
#define ELEM(n) do { if (BB) { ELEM_H(n, 0u); ELEM_H(n, 4096u); } else ELEM_H(n, dsub); } while (0)

  bf16x8 qf[2][8];
  f32x16 o[4][2];
  {
    constexpr int QE = BB ? 8 : 10;
    const unsigned rot = BB ? 2u * ((idx >> 2) + idx) : idx + (idx >> 3);
    const srd_t sw = MKSRD((BB ? p.wqB : p.wqA) + h0 * 256u, 4 << 20), sx = MKSRD((BB ? p.xB : p.xA) + (size_t)b0 * 65536u, BB ? 131072 : 65536);
    unsigned wvo[4], xvo[4];
#pragma unroll
    for (int i = 0; i < 4; ++i) {
      wvo[i] = g4 * 8192u + ((l15 ^ (g4 << 2)) * 16u) + i * (32768u - 1024u);
      xvo[i] = (lane >> 3) * 1024u + (((lane & 7u) ^ ((4u * i + g4) & 7u)) * 16u) + i * (8192u - 1024u);
    }
#define WOFF(s) ((BB ? 1u + ((s) + 3) % 4 : ((s) % 4 == 0 ? 0u : (s) % 4 == 1 ? 8u : (s) % 4 == 2 ? 3u : 6u)) * SLOT)
#define XOFF(s) ((BB ? 1u + ((s) + 3) % 4 : ((s) % 4 == 0 ? 2u : (s) % 4 == 1 ? 12u : (s) % 4 == 2 ? 5u : 15u)) * SLOT)
    const unsigned wdst = ldsb + (BB ? item * REG + half * 4096u : wid * 4096u), xdst = ldsb + (BB ? (2u + item) * REG + half * 4096u : wid * 2048u);
#define QLD1(s, j) do { const unsigned cv_ = ((s) + rot) & 7u; \
      if ((j) == 0) dma4<false>(wdst + WOFF(s), wvo, sw, (cv_ * 64u + wid * 16u) * 8192u); \
      if ((j) == 1) { if (BB) dma4<false>(xdst + XOFF(s), xvo, sx, wid * 32768u + cv_ * 128u); \
                      else dma4<false>(wdst + WOFF(s) + 2u * REG, wvo, sw, (cv_ * 64u + wid * 16u) * 8192u + 256u); } \
      if ((j) == 2 && !BB) { SETM0N(xdst + XOFF(s)); dma2<0, false>(xvo[0], xvo[1], sx, wid * 16384u + cv_ * 128u); } } while (0)
#define QLD(s) do { QLD1(s, 0); QLD1(s, 1); QLD1(s, 2); } while (0)
    const unsigned p4 = lane & 3u, q4 = (lane >> 2) & 3u;
    const unsigned wa0 = ((ldsb + (8u * hh + q4) * 256u + (q4 << 6) + (g4 & 1u) * 32u + (((p4 & 1u) << 1) | (p4 >> 1)) * 8u) ^ (128u * half)) + (BB ? 0u : item * (2u * REG)), wa1 = wa0 ^ 64u;
    const unsigned xa = ldsb + (BB ? (2u + item) * REG : 0u) + l31 * 128u + ((hh ^ ((l31 >> 1) & 7u)) * 16u);
    const f32x16 z16 = {};
    f32x16 acc[2][2];
    bf16x8 xf[2][2][4], wf[2][2][4];
#define QM1(s, ks, g) acc[(g) >> 1][(g) & 1] = MFMA32(wf[(s) & 1][(g) & 1][ks], xf[(s) & 1][(g) >> 1][ks], ((s) == 0 && (ks) == 0) ? z16 : acc[(g) >> 1][(g) & 1])
#define QMM(s, ks) do { QM1(s, ks, 0); QM1(s, ks, 1); QM1(s, ks, 2); QM1(s, ks, 3); } while (0)
#define WKS(ks) (BB ? ((ks) >> 1) * REG + ((ks) & 1) * 4096u : (ks) * 4096u)
#define DG(m0v, vo, srd, soff, NT, cnt, g) do { if ((g) < 0) SETM0(m0v); else if ((g) < (cnt)) dma1<((g) & 3) * 1024, NT>(vo[(g) & 3], srd, soff); } while (0)
#define QG(s, j, g) do { const unsigned cv_ = ((s) + rot) & 7u; \
      if ((j) == 0) DG(wdst + WOFF(s), wvo, sw, (cv_ * 64u + wid * 16u) * 8192u, false, 4, g); \
      if ((j) == 1) { if (BB) DG(xdst + XOFF(s), xvo, sx, wid * 32768u + cv_ * 128u, false, 4, g); \
                      else DG(wdst + WOFF(s) + 2u * REG, wvo, sw, (cv_ * 64u + wid * 16u) * 8192u + 256u, false, 4, g); } \
      if ((j) == 2 && !BB) DG(xdst + XOFF(s), xvo, sx, wid * 16384u + cv_ * 128u, false, 2, g); } while (0)
#define EG(n, j, g) do { if ((n) < 2 || ((n) & 1)) DG(ring + ((n) % NS) * SLOT + (j), kvo, sK, RO(((n) < 2 ? (n) : ((n) + 1) / 2) * 8192u + (j)), NTK, 4, g); \
                         else DG(ring + ((n) % NS) * SLOT + (j), vv4, sV, RO(((n) - 2) / 2 * 8192u + (j)), NTV, 4, g); } while (0)
#define QOP(HOOK, NOP, G, v) do { if ((v) < (NOP) && ((v) * 16) / (NOP) == (G)) { HOOK((v) >> 2, (v) & 3); if (((v) & 3) == 3 && (v) + 1 < (NOP)) { HOOK(((v) >> 2) + 1, -1); } } } while (0)
#define QGAP(s, ks, g, HOOK, NOP) do { if ((s) > 0) QM1((s) - 1, ks, g); else asm volatile("s_nop 0"); \
        if ((g) == 0) { xf[(s) & 1][0][ks] = lds_rd128(x_ ^ (32u * (ks))); xf[(s) & 1][1][ks] = lds_rd128((x_ ^ (32u * (ks))) + 4096u); } \
        if ((g) == 1) wf[(s) & 1][0][ks] = cat8(lds_tr(w0_ + WKS(ks)), lds_tr(w0_ + WKS(ks) + 1024u)); \
        if ((g) == 2) wf[(s) & 1][1][ks] = cat8(lds_tr(w1_ + WKS(ks)), lds_tr(w1_ + WKS(ks) + 1024u)); \
        SBAR(); \
        QOP(HOOK, NOP, 4 * (ks) + (g), 0); QOP(HOOK, NOP, 4 * (ks) + (g), 1); QOP(HOOK, NOP, 4 * (ks) + (g), 2); QOP(HOOK, NOP, 4 * (ks) + (g), 3); \
        QOP(HOOK, NOP, 4 * (ks) + (g), 4); QOP(HOOK, NOP, 4 * (ks) + (g), 5); QOP(HOOK, NOP, 4 * (ks) + (g), 6); QOP(HOOK, NOP, 4 * (ks) + (g), 7); \
        QOP(HOOK, NOP, 4 * (ks) + (g), 8); QOP(HOOK, NOP, 4 * (ks) + (g), 9); QOP(HOOK, NOP, 4 * (ks) + (g), 10); QOP(HOOK, NOP, 4 * (ks) + (g), 11); \
        QOP(HOOK, NOP, 4 * (ks) + (g), 12); QOP(HOOK, NOP, 4 * (ks) + (g), 13); QOP(HOOK, NOP, 4 * (ks) + (g), 14); QOP(HOOK, NOP, 4 * (ks) + (g), 15); \
        SBAR(); } while (0)
#define QKS(s, ks, HOOK, NOP) do { QGAP(s, ks, 0, HOOK, NOP); QGAP(s, ks, 1, HOOK, NOP); QGAP(s, ks, 2, HOOK, NOP); QGAP(s, ks, 3, HOOK, NOP); } while (0)
#define QCMP(s, HOOK, NOP) do { const unsigned w0_ = wa0 + WOFF(s), w1_ = wa1 + WOFF(s), x_ = xa + XOFF(s); HOOK(0, -1); SBAR(); \
      QKS(s, 0, HOOK, NOP); QKS(s, 1, HOOK, NOP); QKS(s, 2, HOOK, NOP); QKS(s, 3, HOOK, NOP); } while (0)
#define HQ0(j, g) QG(3, j, g)
#define HQ1(j, g) QG(4, j, g)
#define HQ2(j, g) QG(5, j, g)
#define HQ3(j, g) QG(6, j, g)
#define HQ4(j, g) QG(7, j, g)
#define HQ5(j, g) do { if (BB) { if ((j) == 0) EG(0, 0u, g); if ((j) == 1) EG(0, 4096u, g); } else { if ((j) == 0) EG(0, dsub, g); if ((j) == 1) EG(1, dsub, g); } } while (0)
#define HQ6(j, g) do { if (BB) { if ((j) == 0) EG(1, 0u, g); if ((j) == 1) EG(1, 4096u, g); } else { if ((j) == 0) EG(2, dsub, g); } } while (0)
#define HQ7(j, g) do { if (BB) { if ((j) == 0) EG(2, 0u, g); if ((j) == 1) EG(2, 4096u, g); } else { if ((j) == 0) EG(3, dsub, g); if ((j) == 1) EG(4, dsub, g); } } while (0)
    unsigned tch = 0u;
#define TOUCH(srd, soff, stride) asm volatile("buffer_load_dword %0, %1, %2, %3 offen" : "+v"(tch) : "v"(lane * (stride)), "s"(srd), "s"(soff) : "memory")
#define TB(i) do { if (BB && TCH_B) TOUCH(((i) & 2) ? sV : sK, ((i) & 2) ? ((i) & 1) * 4096u : 8192u + ((i) >> 2) * 8192u + ((i) & 1) * 4096u, 64u); } while (0)
    constexpr int NTCH = BB ? 0 : TCH_WO + TCH_KV, TE = (BB && TCH_B) ? 1 : 0;
    if (!BB && ADELAY > 0) __builtin_amdgcn_s_sleep(ADELAY);
    QLD(0); QLD(1);
#pragma unroll
    for (int db = 0; db < 4; ++db) { o[db][0] = f32x16{}; o[db][1] = f32x16{}; asm volatile("" : "+a"(o[db][0]), "+a"(o[db][1])); }
    QLD(2);
    if (!BB) {
      const srd_t st = MKSRD((half ? p.woB : p.woA) + (size_t)(g8 >> 1) * 1048576u + (TCH_WO == 4 ? (idx * 2u + item) * 32768u : ((g8 & 1u) * 32u + idx * 2u + item) * 16384u), 32768);
#pragma unroll
      for (int i = 0; i < TCH_WO; ++i) TOUCH(st, i * 8192u, 128u);
#pragma unroll
      for (int i = 0; i < TCH_KV; ++i) TOUCH(item ? sV : sK, RO((2u * i + (idx & 1u)) * 8192u), 128u);
    }
    TB(0);
    waitv<2 * QE + NTCH + TE>(); BARRIER(); QCMP(0, HQ0, QE); TB(1);
    STAMP(1);
    waitv<2 * QE + NTCH + 2 * TE>(); BARRIER(); QCMP(1, HQ1, QE); TB(2);
    waitv<2 * QE + NTCH + 3 * TE>(); BARRIER(); QCMP(2, HQ2, QE); TB(3);
    waitv<2 * QE + 3 * TE>(); BARRIER(); QCMP(3, HQ3, QE); TB(4);
    waitv<2 * QE + 3 * TE>(); BARRIER(); QCMP(4, HQ4, QE);
    waitv<2 * QE + 2 * TE>(); BARRIER(); QCMP(5, HQ5, 8);
    waitv<QE + 8 + TE>(); BARRIER(); QCMP(6, HQ6, (BB ? 8 : 4));
    waitv<BB ? 16 : 12>(); BARRIER(); QCMP(7, HQ7, 8);
    PIN(tch);
    STAMP(2);
    const unsigned sp = BB ? reg + 4u * SLOT : ldsb + item * (2u * REG) + 8u * SLOT + half * SLOT, psp = BB ? preg + 4u * SLOT : sp ^ SLOT;
#define QOUT(g) do { lds_wru4(sp + (g) * 2048u + lane * 16u, pack8(acc[(g) >> 1][(g) & 1], 0, C2)); lds_wru4(sp + (g) * 2048u + 1024u + lane * 16u, pack8(acc[(g) >> 1][(g) & 1], 1, C2)); } while (0)
#define QLAST(g) do { QM1(7, 0, g); QM1(7, 1, g); QM1(7, 2, g); QM1(7, 3, g); } while (0)
    QLAST(0); SBAR(); QLAST(1); SBAR(); QLAST(2); QOUT(0); SBAR(); QLAST(3); QOUT(1); SBAR(); QOUT(2); QOUT(3);
    BARRIER();
    const unsigned lo = (half ? psp : sp) + lane * 16u, hi = (half ? sp : psp) + lane * 16u, q0 = half * 4096u, q1 = 4096u - q0;
#pragma unroll
    for (int j = 0; j < 4; ++j) {
      qf[0][j] = lds_rd128(lo + q0 + j * 1024u); qf[0][4 + j] = lds_rd128(hi + q0 + j * 1024u);
      qf[1][j] = lds_rd128(lo + q1 + j * 1024u); qf[1][4 + j] = lds_rd128(hi + q1 + j * 1024u);
    }
  }
  PIN(epoch_v);
  const u64 tag = mix64(((u64)rfl((unsigned)(epoch_v >> 32)) << 32) | rfl((unsigned)epoch_v));

  unsigned tA = (NS / 2u + 1u) * 8192u + dsub, tB = (NS / 2u) * 8192u + dsub;
  const unsigned dring = ring + dsub;
  const unsigned kb = ring + l31 * 256u + ((hh ^ l15) * 16u);
  const unsigned vb = ring + (4u * hh + ((lane >> 2) & 3u)) * 256u + (((lane >> 2) & 3u) << 6) + (g4 & 1u) * 32u + (lane & 3u) * 8u;
  f32x16 sA[2], sB[2], negm[2];
  bf16x8 kf[8]; s16x4 vlo[4][2], vhi[4][2]; u32x4 pw[2][2];
  float f0 = 1.f, f1 = 1.f, ls0 = 0.f, lt0 = 0.f, ls1 = 0.f, lt1 = 0.f;
#define QK0(D, a, bq) asm volatile("v_mfma_f32_32x32x16_bf16 %0, %1, %2, 0" : "=&v"(D) : "v"(a), "a"(bq))
#define QKC(D, a, bq, C) asm volatile("v_mfma_f32_32x32x16_bf16 %0, %1, %2, %3" : "=&v"(D) : "v"(a), "a"(bq), "v"(C))
#define QKA(D, a, bq) asm volatile("v_mfma_f32_32x32x16_bf16 %0, %1, %2, %0" : "+v"(D) : "v"(a), "a"(bq))
#define WAITE(k) do { waitv<PE * (k)>(); if (BB) { WAITL(); } else { BARRIER(); } } while (0)
#define WAITR() WAITE(NS - 2)
#define EXP2(x) __builtin_amdgcn_exp2f(x)

  STAMP(3);
  WAITE(BB ? 2 : 4);
#pragma unroll
  for (int s = 0; s < 8; ++s) kf[s] = lds_rd128(kb ^ (32u * s));
  QK0(sA[0], kf[0], qf[0][0]); QK0(sA[1], kf[0], qf[1][0]);
#pragma unroll
  for (int s = 1; s < 8; ++s) { QKA(sA[0], kf[s], qf[0][s]); QKA(sA[1], kf[s], qf[1][s]);
    if (s == 2) { if (BB) ELEM(3); else ELEM(5); } if (s == 4 && !BB) ELEM(6); if (s == 6 && !BB) ELEM(7); }
  WAITR();
  if (BB) asm volatile("s_barrier" ::: "memory");
  ELEM(NS);
#pragma unroll
  for (int s = 0; s < 8; ++s) kf[s] = lds_rd128((kb + SLOT) ^ (32u * s));
  asm volatile("s_nop 7\n\ts_nop 7" : "+v"(sA[0]), "+v"(sA[1]));
#pragma unroll
  for (int qb = 0; qb < 2; ++qb) {
    const float m0 = swap_max(max8hi(max8lo(sA[qb]), sA[qb]));
#pragma unroll
    for (int r = 0; r < 16; ++r) { sA[qb][r] -= m0; negm[qb][r] = -m0; }
    PIN(negm[qb]);
#pragma unroll
    for (int r = 0; r < 8; ++r) sA[qb][r] = EXP2(sA[qb][r]);
  }
#pragma unroll
  for (int r = 0; r < 8; r += 2) { ls0 += sA[0][r]; lt0 += sA[0][r + 1]; ls1 += sA[1][r]; lt1 += sA[1][r + 1]; }

  unsigned so1 = 1u * SLOT, so2 = 2u * SLOT, so3 = 3u * SLOT;
#define ADV2(x) x = (x + 2u * SLOT) & (NS * SLOT - 1u)
#define VRD(db, ks) do { const unsigned a_ = (va ^ (64u * (db))) + 4096u * (ks); vlo[db][ks] = lds_tr(a_); vhi[db][ks] = lds_tr(a_ + 2048u); } while (0)
#define GAPA(C, N, s, QB, LAST) do { \
    if ((s) == 0) QKC(N[QB], kf[0], qf[QB][0], negm[QB]); else QKA(N[QB], kf[s], qf[QB][s]); \
    if ((s) < 4) { C[QB][8 + 2 * (s)] = EXP2(C[QB][8 + 2 * (s)]); C[QB][9 + 2 * (s)] = EXP2(C[QB][9 + 2 * (s)]); pw[QB][0][(s) & 3] = cvtpk(C[QB][2 * (s)], C[QB][2 * (s) + 1]); PIN(C[QB]); PIN(pw[QB][0]); } \
    else { ls##QB += C[QB][2 * (s)]; lt##QB += C[QB][2 * (s) + 1]; pw[QB][1][(s) & 3] = cvtpk(C[QB][2 * (s)], C[QB][2 * (s) + 1]); PIN(pw[QB][1]); PIN(ls##QB); PIN(lt##QB); } \
    if ((s) < 4) { VRD((2 * (s) + QB) & 3, ((s) >> 1) & 1); } \
    if (BB && QB == 0 && (s) == 6) SETM0(dring + so1 + 4096u); \
    if (LAST) { if (BB) { \
      if (QB == 1 && (s) == 4) dma2<0, NT_WO>(wvo[0], wvo[1], sW, wso); \
      if (QB == 1 && (s) == 5) dma2<2048, NT_WO>(wvo[2], wvo[3], sW, wso); \
      if (QB == 1 && (s) == 6) dma2<0, NT_WO>(wvo[0], wvo[1], sW, wso + 32768u); \
      if (QB == 1 && (s) == 7) dma2<2048, NT_WO>(wvo[2], wvo[3], sW, wso + 32768u); } \
    } else { \
      if (QB == 1 && (s) == 4) dma2<0, NTK>(kvo[0], kvo[1], sK, RO(tA)); \
      if (QB == 1 && (s) == 5) dma2<2048, NTK>(kvo[2], kvo[3], sK, RO(tA)); \
      if (BB && QB == 1 && (s) == 6) dma2<0, NTK>(kvo[0], kvo[1], sK, RO(tA) + 4096u); \
      if (BB && QB == 1 && (s) == 7) dma2<2048, NTK>(kvo[2], kvo[3], sK, RO(tA) + 4096u); } \
    SBAR(); } while (0)
#define PHASE_A(C, N, L) do { \
    if (!BB && ASLP > 0) __builtin_amdgcn_s_sleep(ASLP); \
    SETM0(dring + so1); WAITR(); SBAR(); \
    const unsigned va = vb + so2; \
    GAPA(C, N, 0, 0, L); GAPA(C, N, 0, 1, L); GAPA(C, N, 1, 0, L); GAPA(C, N, 1, 1, L); GAPA(C, N, 2, 0, L); GAPA(C, N, 2, 1, L); GAPA(C, N, 3, 0, L); GAPA(C, N, 3, 1, L); \
    GAPA(C, N, 4, 0, L); GAPA(C, N, 4, 1, L); GAPA(C, N, 5, 0, L); GAPA(C, N, 5, 1, L); GAPA(C, N, 6, 0, L); GAPA(C, N, 6, 1, L); GAPA(C, N, 7, 0, L); GAPA(C, N, 7, 1, L); \
    tA += 8192u; } while (0)
#define PVM(i) o[((i) >> 1) & 3][(i) & 1] = MFMA32(cat8(vlo[((i) >> 1) & 3][(i) >> 3], vhi[((i) >> 1) & 3][(i) >> 3]), __builtin_bit_cast(bf16x8, pw[(i) & 1][(i) >> 3]), o[((i) >> 1) & 3][(i) & 1])
#define KRD(i) kf[i] = lds_rd128(ka ^ (32u * (i)))
#define GAPE(N, j) do { PVM(8 + (j)); N[0][j] = EXP2(N[0][j]); N[1][j] = EXP2(N[1][j]); \
    if ((j) > 0) { if ((j) & 1) { ls0 += N[0][((j) + 15) & 15]; ls1 += N[1][((j) + 15) & 15]; } else { lt0 += N[0][((j) + 15) & 15]; lt1 += N[1][((j) + 15) & 15]; } } \
    PIN(N[0]); PIN(N[1]); PIN(ls0); PIN(ls1); PIN(lt0); PIN(lt1); SBAR(); } while (0)
#define DMAB(OFF, i0, i1, hi, LAST) do { if (LAST) { if (BB) dma2<OFF, NT_WO>(wvo[i0], wvo[i1], sW, wso + 65536u + (hi) * 32768u); } \
    else if (BB || !(hi)) dma2<OFF, NTV>(vvo, vvo, sV, RO(tB) + (hi) * 4096u); } while (0)
#define PHASE_B(N, LAST) do { \
    SETM0(dring + so2); if (LAST) { WAITL(); } else { WAITR(); } SBAR(); \
    const unsigned ka = kb + so3; \
    PVM(0); KRD(0); KRD(1); DMAB(0, 0, 1, 0, LAST); SBAR(); \
    PVM(1); KRD(2); KRD(3); DMAB(2048, 2, 3, 0, LAST); if (BB) SETM0(dring + so2 + 4096u); SBAR(); \
    PVM(2); KRD(4); float ma0 = max3f(N[0][0], N[0][1], N[0][2]), ma1 = max3f(N[1][0], N[1][1], N[1][2]), mb0 = max3f(N[0][3], N[0][4], N[0][5]), mb1 = max3f(N[1][3], N[1][4], N[1][5]); SBAR(); \
    PVM(3); KRD(5); ma0 = max3f(ma0, N[0][6], N[0][7]); ma1 = max3f(ma1, N[1][6], N[1][7]); mb0 = max3f(mb0, N[0][8], N[0][9]); mb1 = max3f(mb1, N[1][8], N[1][9]); SBAR(); \
    PVM(4); KRD(6); ma0 = max3f(ma0, N[0][10], N[0][11]); ma1 = max3f(ma1, N[1][10], N[1][11]); mb0 = max3f(mb0, N[0][12], N[0][13]); mb1 = max3f(mb1, N[1][12], N[1][13]); SBAR(); \
    PVM(5); ma0 = max3f(ma0, N[0][14], N[0][15]); ma1 = max3f(ma1, N[1][14], N[1][15]); const float mbb = max2f(mb0, mb1); KRD(7); SBAR(); \
    PVM(6); const float mall = max3f(ma0, ma1, mbb); DMAB(0, 0, 1, 1, LAST); const bool grew = __any(mall > THR); SBAR(); \
    PVM(7); DMAB(2048, 2, 3, 1, LAST); SBAR(); \
    if (__builtin_expect(grew, 0)) {     \
      const float d0 = max2f(swap_max(max2f(ma0, mb0)), 0.f), d1 = max2f(swap_max(max2f(ma1, mb1)), 0.f); \
      f0 = EXP2(-d0); f1 = EXP2(-d1); ls0 *= f0; lt0 *= f0; ls1 *= f1; lt1 *= f1; \
      sub_tile(N[0], d0); sub_tile(N[1], d1); sub_tile(negm[0], d0); sub_tile(negm[1], d1); \
    } \
    SBAR(); \
    GAPE(N, 0); GAPE(N, 1); GAPE(N, 2); GAPE(N, 3); GAPE(N, 4); GAPE(N, 5); GAPE(N, 6); GAPE(N, 7); \
    lt0 += N[0][7]; lt1 += N[1][7]; \
    if (__builtin_expect(grew, 0)) { \
      asm volatile("s_nop 7\n\ts_nop 7" ::: "memory"); \
      _Pragma("unroll") for (int db = 0; db < 4; ++db) { scale_acc(o[db][0], f0); scale_acc(o[db][1], f1); } \
    } \
    ADV2(so1); ADV2(so2); ADV2(so3); tB += 8192u; } while (0)

  const unsigned kq = g8 >> 1, ob = 4u * (g8 & 1u) + 2u * (idx >> 3) + item, nt = idx & 7u, wrot = 4u * (idx >> 3) + 2u * (g8 & 1u);
  unsigned wvo[4];
  const srd_t sW = MKSRD((BB ? p.woB : p.woA) + (size_t)kq * 1048576u + nt * 128u, 1048576 - 896);
  const unsigned wso = ((wid + wrot) & 7u) * 131072u;
#define WDMA(hi) do { _Pragma("unroll") for (int j_ = 0; j_ < 4; ++j_) dma4<NT_WO>(reg + (1u + 2u * (hi)) * SLOT + j_ * 4096u, wvo, sW, (wso ^ ((hi) * 524288u)) + j_ * 32768u); } while (0)
  STAMP(4);
  for (unsigned t = 0; t < 14u; t += 2u) {
    if (BB && t == TSIG && tid == 0u) __hip_atomic_store((GAS u64*)(p.ws + WS_FLAG3) + (size_t)blockIdx.x * 8u, tag, RLX_AGENT);
    if (t == 4u) { STAMP(12); } if (t == 8u) { STAMP(13); } if (t == 12u) { STAMP(14); }
    PHASE_A(sA, sB, false); PHASE_B(sB, false);
    PHASE_A(sB, sA, false); PHASE_B(sA, false);
  }
#pragma unroll
  for (int j = 0; j < 4; ++j) wvo[j] = (lane >> 3) * 1024u + (((lane & 7u) ^ ((g4 & 1u) << 2)) * 16u) + j * 7168u;
  if (BB && TSIG == 14u && tid == 0u) __hip_atomic_store((GAS u64*)(p.ws + WS_FLAG3) + (size_t)blockIdx.x * 8u, tag, RLX_AGENT);
  PHASE_A(sA, sB, true); PHASE_B(sB, true);
  STAMP(5);
  {
#pragma unroll
    for (int r = 8; r < 16; ++r) { sB[0][r] = EXP2(sB[0][r]); sB[1][r] = EXP2(sB[1][r]); ls0 += sB[0][r]; ls1 += sB[1][r]; }
#pragma unroll
    for (int i = 0; i < 8; ++i) { pw[0][i >> 2][i & 3] = cvtpk(sB[0][2 * i], sB[0][2 * i + 1]); pw[1][i >> 2][i & 3] = cvtpk(sB[1][2 * i], sB[1][2 * i + 1]); }
    PIN(pw[0][0]); PIN(pw[0][1]); PIN(pw[1][0]); PIN(pw[1][1]);
    SBAR();
    WAITE(BB ? 2 : 0);
    const unsigned va = vb + so2;
#pragma unroll
    for (int i = 0; i < 8; ++i) VRD(i & 3, i >> 2);
    PVM(1); PVM(3); PVM(5); PVM(7); PVM(9); PVM(11); PVM(13); PVM(15);
    if (BB) { WAITL(); } else { BARRIER(); }
    SBAR();
    PVM(0); PVM(2); PVM(4); PVM(6); PVM(8); PVM(10); PVM(12); PVM(14);
  }
  STAMP(6);
  const srd_t sO = MKSRD(p.ws + WS_O, 16 << 20);
  GAS u64* flags = (GAS u64*)(p.ws + WS_FLAG);
  {
    const float l0 = swap_add(ls0 + lt0), l1 = swap_add(ls1 + lt1);
    const float mhat[2] = {-negm[0][0], -negm[1][0]};
    const unsigned xo = reg + 3u * SLOT + lane * 16u, xi = preg + 3u * SLOT + lane * 16u;
    const unsigned mo = reg + lane * 8u, mi = preg + lane * 8u;
#pragma unroll
    for (int db = 0; db < 4; ++db)
#pragma unroll
      for (int r4 = 0; r4 < 4; ++r4) lds_wrf4(xo + (db * 4 + r4) * 1024u, f32x4{o[db][1][4 * r4], o[db][1][4 * r4 + 1], o[db][1][4 * r4 + 2], o[db][1][4 * r4 + 3]});
    *(LAS f32x2*)(unsigned long)mo = f32x2{mhat[1], l1};
    BARRIER();
    const f32x2 ml = *(const LAS f32x2*)(unsigned long)mi;
    const float mm = __builtin_fmaxf(mhat[0], ml[0]);
    float a0 = EXP2(mhat[0] - mm), a1 = EXP2(ml[0] - mm);
    const float inv = 1.0f / (a0 * l0 + a1 * ml[1]);
    a0 *= inv; a1 *= inv;
    const unsigned oidx = (((BB ? 1u : 0u) * 8u + b) * 32u + h) * 2u + half;
#pragma unroll
    for (int db = 0; db < 4; ++db)
#pragma unroll
      for (int ks = 0; ks < 2; ++ks) {
        const f32x4 x0 = lds_rdf4(xi + (db * 4 + 2 * ks) * 1024u), x1 = lds_rdf4(xi + (db * 4 + 2 * ks + 1) * 1024u);
        float y[8];
#pragma unroll
        for (int e = 0; e < 4; ++e) {
          y[e] = a0 * o[db][0][8 * ks + e] + a1 * x0[e];
          y[4 + e] = a0 * o[db][0][8 * ks + 4 + e] + a1 * x1[e];
        }
        u32x4 v = {cvtpk(y[0], y[1]), cvtpk(y[2], y[3]), cvtpk(y[4], y[5]), cvtpk(y[6], y[7])};
        __builtin_amdgcn_raw_buffer_store_b128(v, sO, oidx * 8192u + lane * 16u + (db * 2 + ks) * 1024u, 0, 16);
      }
    waitv<0>();
    if (lane == 0) __hip_atomic_store(flags + (size_t)oidx * 8u, tag, RLX_AGENT);
  }
  STAMP(7);
  {
    constexpr unsigned br = BB ? 1u : 0u;
    if (!BB && TCH_END > 0) {
      const unsigned hb = 4u * g8 + (idx & 3u), bb2 = 2u * (idx >> 2) + item;
      const size_t off = ((size_t)(bb2 * 32u + hb) * 1024u + half * 512u) * 256u;
      const srd_t tK = MKSRD(p.kB + off, 131072), tV = MKSRD(p.vB + off, 131072);
      unsigned tch = 0u;
#pragma unroll
      for (int i = 16 - TCH_END; i < 16; ++i) { TOUCH(tK, i * 8192u, 128u); TOUCH(tV, i * 8192u, 128u); }
      waitv<0>(); PIN(tch);
    }
    if (!BB && TSIG < 16u && wid == 0u) poll_flags<32>((GAS u64*)(p.ws + WS_FLAG3) + (size_t)(blockIdx.x - 128u) * 8u, tag);
    BARRIER();
    if (!BB) WDMA(0);
    WDMA(1);
    const unsigned q2 = (lane >> 2) & 3u;
    const unsigned tl = ldsb + half * (2u * REG) + SLOT + (4u * hh + q2) * 128u + (g4 & 1u) * 32u + (lane & 3u) * 8u;
    const unsigned tb0 = tl + (((kq ^ (q2 >> 1)) & 1u) * 64u), tb1 = tb0 ^ 64u;
    const bool owner = (half == (kq >> 1));
    GAS u64* flags2 = (GAS u64*)(p.ws + WS_FLAG2);
#define HSL(i) (((i) & 1) + 4 * ((i) >> 1))
#define WFRAG(tb, i, f) cat8(lds_tr((tb) + ((i) & 1) * REG + ((i) >> 1) * 16384u + (f) * 2048u), lds_tr((tb) + ((i) & 1) * REG + ((i) >> 1) * 16384u + (f) * 2048u + 1024u))
    const unsigned fidx = ((br * 8u + ob) * 32u + 8u * kq) * 2u;
    const unsigned hrot = 2u * half + wrot;
    poll_flags<PSLP>(flags + (size_t)(fidx + ((((lane >> 1) & 1u) + (lane & 4u) + hrot) & 7u) * 2u + (lane & 1u)) * 8u, tag);
    STAMP(8);
    bf16x8 of[3][16];
    const unsigned ovo = lane * 16u, obase = fidx * 8192u, q0 = half * 8192u, q1 = 8192u - q0;
#define OLDH(i, c) do { const unsigned s_ = obase + ((HSL(i) + hrot) & 7u) * 16384u + (c) * 4096u; oload4(&of[(i) % 3][4 * (c)], ovo, sO, s_ + q0); oload4(&of[(i) % 3][8 + 4 * (c)], ovo, sO, s_ + q1); } while (0)
    f32x16 acc[2][2];
#define OHALF(i, c, N) do { wait_o8<N>(&of[(i) % 3][4 * (c)], &of[(i) % 3][8 + 4 * (c)]); \
    _Pragma("unroll") for (int f = 4 * (c); f < 4 * (c) + 4; ++f) { \
      const bf16x8 w0 = wn[f & 3][0], w1 = wn[f & 3][1]; \
      if (2 * (i) + (c) < 7) { wn[f & 3][0] = WFRAG(tb0, (2 * (i) + (c) + 1) >> 1, (f + 4) & 7); wn[f & 3][1] = WFRAG(tb1, (2 * (i) + (c) + 1) >> 1, (f + 4) & 7); } \
      if ((i) == 0 && f == 0) { const f32x16 z_ = {}; acc[0][0] = MFMA32(w0, of[0][0], z_); acc[0][1] = MFMA32(w1, of[0][0], z_); acc[1][0] = MFMA32(w0, of[0][8], z_); acc[1][1] = MFMA32(w1, of[0][8], z_); } \
      else { acc[0][0] = MFMA32(w0, of[(i) % 3][f], acc[0][0]); acc[0][1] = MFMA32(w1, of[(i) % 3][f], acc[0][1]); acc[1][0] = MFMA32(w0, of[(i) % 3][8 + f], acc[1][0]); acc[1][1] = MFMA32(w1, of[(i) % 3][8 + f], acc[1][1]); } } \
    SBAR(); } while (0)
    OLDH(0, 0); OLDH(0, 1); OLDH(1, 0);
    asm volatile("s_barrier" ::: "memory");
    bf16x8 wn[4][2];
#pragma unroll
    for (int f = 0; f < 4; ++f) { wn[f][0] = WFRAG(tb0, 0, f); wn[f][1] = WFRAG(tb1, 0, f); }
    OHALF(0, 0, 16); OLDH(1, 1); SBAR();
    OHALF(0, 1, 16); OLDH(2, 0); SBAR();
    OHALF(1, 0, 16); OLDH(2, 1); SBAR();
    OHALF(1, 1, 16); OLDH(3, 0); SBAR();
    OHALF(2, 0, 16); OLDH(3, 1); SBAR();
    OHALF(2, 1, 16);
    OHALF(3, 0, 8);
    OHALF(3, 1, 0);
    STAMP(9);
    const unsigned xo = reg + lane * 16u, xi = preg + lane * 16u;
#pragma unroll
    for (int nb = 0; nb < 2; ++nb)
#pragma unroll
      for (int r4 = 0; r4 < 4; ++r4) lds_wrf4(xo + (nb * 4 + r4) * 1024u, f32x4{acc[1][nb][4 * r4], acc[1][nb][4 * r4 + 1], acc[1][nb][4 * r4 + 2], acc[1][nb][4 * r4 + 3]});
    BARRIER();
    const unsigned tidx = (br * 8u + ob) * 8u + nt;
    const unsigned pbase = (unsigned)WS_PART + tidx * 65536u + kq * 4096u + lane * 16u;
    f32x4 y0[4];
#pragma unroll
    for (int r4 = 0; r4 < 4; ++r4) {
      const f32x4 y1 = lds_rdf4(xi + (4 + r4) * 1024u) + f32x4{acc[0][1][4 * r4], acc[0][1][4 * r4 + 1], acc[0][1][4 * r4 + 2], acc[0][1][4 * r4 + 3]};
      __builtin_amdgcn_raw_buffer_store_b128(__builtin_bit_cast(u32x4, y1), sO, pbase + (2u * half + ((kq & 1u) ^ 1u)) * 16384u + r4 * 1024u, 0, 16);
    }
#pragma unroll
    for (int r4 = 0; r4 < 4; ++r4) y0[r4] = lds_rdf4(xi + r4 * 1024u) + f32x4{acc[0][0][4 * r4], acc[0][0][4 * r4 + 1], acc[0][0][4 * r4 + 2], acc[0][0][4 * r4 + 3]};
    if (!owner) {
#pragma unroll
      for (int r4 = 0; r4 < 4; ++r4) __builtin_amdgcn_raw_buffer_store_b128(__builtin_bit_cast(u32x4, y0[r4]), sO, pbase + (2u * half + (kq & 1u)) * 16384u + r4 * 1024u, 0, 16);
    }
    waitv<0>();
    if (lane == 0) __hip_atomic_store(flags2 + (size_t)((tidx * 2u + half) * 4u + kq) * 8u, tag, RLX_AGENT);
    STAMP(10);
    if (owner) {
      poll_flags<PSLP>(flags2 + (size_t)((tidx * 2u + half) * 4u + (lane & 3u)) * 8u, tag);
      STAMP(11);
      const unsigned rbase = (unsigned)WS_PART + tidx * 65536u + kq * 16384u + lane * 16u;
#pragma unroll
      for (int j = 1; j < 4; ++j)
#pragma unroll
        for (int r4 = 0; r4 < 4; ++r4) y0[r4] += __builtin_bit_cast(f32x4, __builtin_amdgcn_raw_buffer_load_b128(sO, rbase + (kq ^ j) * 4096u + r4 * 1024u, 0, 16));
      char* op = p.out + (size_t)(ob * 64u + half * 32u + l31) * 2048u + (br * 512u + nt * 64u + (kq & 1u) * 32u + 4u * hh) * 2u;
#pragma unroll
      for (int r4 = 0; r4 < 4; ++r4) *(u32x2*)(op + 16 * r4) = u32x2{cvtpk(y0[r4][0], y0[r4][1]), cvtpk(y0[r4][2], y0[r4][3])};
    } else if (!BB && blockIdx.x == 128u && item == 0u) {
#pragma unroll 1
      for (unsigned spins = 0; spins < (1u << 21); ++spins) {
        bool ok = true;
#pragma unroll
        for (int k = 0; k < 16; ++k) ok &= (__hip_atomic_load(flags + (size_t)(k * 64 + lane) * 8u, RLX_AGENT) == tag);
        if (__all(ok)) break;
        __builtin_amdgcn_s_sleep(16);
      }
      if (lane == 0) __hip_atomic_store((GAS u64*)(p.ws + WS_EPOCH), tag, RLX_AGENT);
    }
  }
  STAMP(15);
  STAMP_OUT(p.ws, blockIdx.x * 4u + wid, BB ? 0 : 16, 16);
}

__global__ __launch_bounds__(256, 1) void k1_attn(P1 p) {
  extern __shared__ __attribute__((aligned(16))) char lds[];
  const unsigned ldsb = (unsigned)(unsigned long)lds;
  if (blockIdx.x < 128u) body<true>(p, ldsb); else body<false>(p, ldsb);
}

extern "C" void kernel_launch(void* const* d_in, const int* in_sizes, int n_in,
                              void* d_out, int out_size, void* d_ws, size_t ws_size,
                              hipStream_t stream) {
  (void)in_sizes; (void)n_in; (void)out_size; (void)ws_size;
  (void)hipFuncSetAttribute((const void*)k1_attn, hipFuncAttributeMaxDynamicSharedMemorySize, K1_LDS);
  P1 p1;
  p1.xA = (const char*)d_in[0]; p1.kA = (const char*)d_in[1]; p1.vA = (const char*)d_in[2]; p1.wqA = (const char*)d_in[3]; p1.woA = (const char*)d_in[4];
  p1.xB = (const char*)d_in[5]; p1.kB = (const char*)d_in[6]; p1.vB = (const char*)d_in[7]; p1.wqB = (const char*)d_in[8]; p1.woB = (const char*)d_in[9];
  p1.ws = (char*)d_ws; p1.out = (char*)d_out;
  hipLaunchKernelGGL(k1_attn, dim3(256), dim3(256), K1_LDS, stream, p1);
}
```

```cpp
#include <hip/hip_runtime.h>
#include <hip/hip_bf16.h>

typedef float    f32x16 __attribute__((ext_vector_type(16)));
typedef float    f32x4  __attribute__((ext_vector_type(4)));
typedef float    f32x2  __attribute__((ext_vector_type(2)));
typedef __bf16   bf16x8 __attribute__((ext_vector_type(8)));
typedef __bf16   bf16x2 __attribute__((ext_vector_type(2)));
typedef short    s16x4  __attribute__((ext_vector_type(4)));
typedef unsigned u32x4  __attribute__((ext_vector_type(4)));
typedef unsigned long long u64;
typedef __amdgpu_buffer_rsrc_t srd_t;
#define DI __device__ __forceinline__
#define LAS __attribute__((address_space(3)))
#define GAS __attribute__((address_space(1)))
#define SBAR() __builtin_amdgcn_sched_barrier(0)
#define PIN(x) asm volatile("" : "+v"(x))
#define MFMA32(a, b, c) __builtin_amdgcn_mfma_f32_32x32x16_bf16(a, b, c, 0, 0, 0)
#define RLX_AGENT __ATOMIC_RELAXED, __HIP_MEMORY_SCOPE_AGENT
#define MKSRD(ptr, n) __builtin_amdgcn_make_buffer_rsrc((void*)(ptr), (short)0, (n), 0x00020000)

constexpr unsigned SLOT = 8192u;
constexpr int K1_LDS = 163840;
constexpr float C2 = 0.08838834764831845f * 1.4426950408889634f;
constexpr float THR = 8.0f / C2;
constexpr size_t WS_PART = 0;
constexpr size_t WS_FLAG = 16u << 20;
constexpr size_t WS_FLAG3 = WS_FLAG + 65536;
constexpr size_t WS_EPOCH = WS_FLAG3 + 16384;

DI unsigned rfl(unsigned x) { return __builtin_amdgcn_readfirstlane(x); }
DI unsigned cvtpk(float lo, float hi) { f32x2 v = {lo, hi}; bf16x2 b = __builtin_convertvector(v, bf16x2); return __builtin_bit_cast(unsigned, b); }
DI bf16x8 lds_rd128(unsigned a) { return *(const LAS bf16x8*)(unsigned long)a; }
DI f32x4 lds_rdf4(unsigned a) { return *(const LAS f32x4*)(unsigned long)a; }
DI void lds_wrf4(unsigned a, f32x4 v) { *(LAS f32x4*)(unsigned long)a = v; }
DI void lds_wru4(unsigned a, u32x4 v) { *(LAS u32x4*)(unsigned long)a = v; }
DI s16x4 lds_tr(unsigned a) { return __builtin_amdgcn_ds_read_tr16_b64_v4i16((LAS s16x4*)(unsigned long)a); }
DI bf16x8 cat8(s16x4 lo, s16x4 hi) { return __builtin_bit_cast(bf16x8, __builtin_shufflevector(lo, hi, 0, 1, 2, 3, 4, 5, 6, 7)); }
DI float max3f(float a, float b, float c) { float r; asm("v_max3_f32 %0, %1, %2, %3" : "=v"(r) : "v"(a), "v"(b), "v"(c)); return r; }
DI float max2f(float a, float b) { float r; asm("v_max_f32_e32 %0, %1, %2" : "=v"(r) : "v"(a), "v"(b)); return r; }
DI float swap_max(float m) { auto rr = __builtin_amdgcn_permlane32_swap(__float_as_uint(m), __float_as_uint(m), false, false); return max2f(__uint_as_float(rr[0]), __uint_as_float(rr[1])); }
DI float swap_add(float m) { auto rr = __builtin_amdgcn_permlane32_swap(__float_as_uint(m), __float_as_uint(m), false, false); return __uint_as_float(rr[0]) + __uint_as_float(rr[1]); }
DI float max8lo(const f32x16& p) { float a = max3f(p[0], p[1], p[2]), b = max3f(p[3], p[4], p[5]); return max3f(max3f(a, p[6], p[7]), b, b); }
DI float max8hi(float m, const f32x16& p) { float a = max3f(m, p[8], p[9]), b = max3f(p[10], p[11], p[12]); a = max3f(a, p[13], p[14]); return max3f(a, b, p[15]); }
DI void scale_acc(f32x16& x, float f) {
#pragma unroll
  for (int r = 0; r < 16; ++r) { float e = x[r], t;
    asm volatile("v_accvgpr_read_b32 %1, %0\n\tv_mul_f32 %1, %1, %2\n\tv_accvgpr_write_b32 %0, %1" : "+a"(e), "=&v"(t) : "v"(f));
    x[r] = e; }
}
DI void sub_tile(f32x16& x, float d) {
#pragma unroll
  for (int r = 0; r < 16; ++r) { float e = x[r]; asm volatile("v_sub_f32_e32 %0, %0, %1" : "+v"(e) : "v"(d)); x[r] = e; }
}
DI u64 mix64(u64 z) { z += 0x9E3779B97F4A7C15ull; z = (z ^ (z >> 30)) * 0xBF58476D1CE4E5B9ull; z = (z ^ (z >> 27)) * 0x94D049BB133111EBull; return z ^ (z >> 31); }


#define SETM0(x) asm volatile("s_mov_b32 m0, %0" :: "s"(x) : "memory")
#define SETM0N(x) asm volatile("s_mov_b32 m0, %0\n\ts_nop 0" :: "s"(x) : "memory")
template <int OFF, bool NT> DI void dma2(unsigned vo0, unsigned vo1, srd_t srd, unsigned soff) {
  if constexpr (NT)
    asm volatile("buffer_load_dwordx4 %0, %2, %3 offen offset:%4 nt lds\n\tbuffer_load_dwordx4 %1, %2, %3 offen offset:%5 nt lds"
                 :: "v"(vo0), "v"(vo1), "s"(srd), "s"(soff), "i"(OFF), "i"(OFF + 1024) : "memory");
  else
    asm volatile("buffer_load_dwordx4 %0, %2, %3 offen offset:%4 lds\n\tbuffer_load_dwordx4 %1, %2, %3 offen offset:%5 lds"
                 :: "v"(vo0), "v"(vo1), "s"(srd), "s"(soff), "i"(OFF), "i"(OFF + 1024) : "memory");
}
template <int OFF, bool NT> DI void dma1(unsigned vo, srd_t srd, unsigned soff) {
  if constexpr (NT) asm volatile("buffer_load_dwordx4 %0, %1, %2 offen offset:%3 nt lds" :: "v"(vo), "s"(srd), "s"(soff), "i"(OFF) : "memory");
  else asm volatile("buffer_load_dwordx4 %0, %1, %2 offen offset:%3 lds" :: "v"(vo), "s"(srd), "s"(soff), "i"(OFF) : "memory");
}
template <bool NT> DI void dma4(unsigned m0v, const unsigned (&vo)[4], srd_t srd, unsigned soff) { SETM0N(m0v); dma2<0, NT>(vo[0], vo[1], srd, soff); dma2<2048, NT>(vo[2], vo[3], srd, soff); }
template <int N> DI void waitv() { asm volatile("s_waitcnt vmcnt(%0)" :: "i"(N > 63 ? 63 : N) : "memory"); }
#define WAITL() do { asm volatile("" ::: "memory"); __builtin_amdgcn_s_waitcnt(0xC07F); asm volatile("" ::: "memory"); } while (0)
#define BARRIER() do { WAITL(); asm volatile("s_barrier" ::: "memory"); } while (0)

template <int SLP> DI void poll_flags(GAS u64* f, u64 tag) {
#pragma unroll 1
  for (unsigned spins = 0; spins < (1u << 21); ++spins) {
    const bool ok = (__hip_atomic_load(f, RLX_AGENT) == tag);
    if (__all(ok)) break;
    __builtin_amdgcn_s_sleep(SLP);
  }
  asm volatile("" ::: "memory");
}

constexpr int ASLP = 13;
constexpr int TCH_KV = 2;
constexpr bool NT_WO = false;

struct P1 {
  const char *xA, *kA, *vA, *wqA, *woA, *xB, *kB, *vB, *wqB, *woB;
  char* ws;
  char* out;
};

DI u32x4 pack8(const f32x16& x, int s) {
  return u32x4{cvtpk(x[8 * s], x[8 * s + 1]), cvtpk(x[8 * s + 2], x[8 * s + 3]), cvtpk(x[8 * s + 4], x[8 * s + 5]), cvtpk(x[8 * s + 6], x[8 * s + 7])};
}

constexpr unsigned REG = 5u * SLOT;

template <bool BB> DI void body(const P1& p, const unsigned ldsb) {
  constexpr unsigned NS = BB ? 4u : 8u;
  constexpr int PE = BB ? 8 : 4;
  constexpr bool NTK = true, NTV = true;
  const unsigned tid = threadIdx.x, lane = tid & 63u, wid = rfl(tid >> 6);
  const unsigned g8 = blockIdx.x & 7u, idx = (blockIdx.x >> 3) & 15u;
  const unsigned half = wid & 1u, item = wid >> 1;
  const unsigned h0 = 4u * g8 + 2u * (idx & 1u), b = idx >> 1, h = h0 + item;
  const unsigned l31 = lane & 31u, hh = lane >> 5, l15 = lane & 15u, g4 = lane >> 4;
  const unsigned reg = ldsb + wid * REG, preg = ldsb + (wid ^ 1u) * REG;
  const unsigned ring = BB ? reg : ldsb + half * (2u * REG);
  const unsigned dsub = BB ? 0u : item * 4096u;

  u64 epoch_v;
  asm volatile("global_load_dwordx2 %0, %1, off sc1" : "=v"(epoch_v) : "v"(p.ws + WS_EPOCH) : "memory");
  const size_t kvoff = BB ? ((size_t)(b * 32u + h) * 1024u + half * 512u) * 256u : ((size_t)(b * 8u + g8) * 1024u + half * 512u) * 256u;
  const srd_t sK = MKSRD((BB ? p.kB : p.kA) + kvoff, 512 * 256), sV = MKSRD((BB ? p.vB : p.vA) + kvoff, 512 * 256);
  unsigned kvo[4];
#pragma unroll
  for (int i = 0; i < 4; ++i) kvo[i] = g4 * 256u + (((l15 ^ g4) ^ (4u * i)) * 16u);
  const unsigned vvo = g4 * 256u + ((l15 ^ (g4 << 2)) * 16u);
  const unsigned vv4[4] = {vvo, vvo, vvo, vvo};
#define ELEM_H(n, j) do { if ((n) < 2 || ((n) & 1)) dma4<NTK>(ring + ((n) % NS) * SLOT + (j), kvo, sK, ((n) < 2 ? (n) : ((n) + 1) / 2) * 8192u + (j)); \
                          else dma4<NTV>(ring + ((n) % NS) * SLOT + (j), vv4, sV, ((n) - 2) / 2 * 8192u + (j)); } while (0)
#define ELEM(n) do { if (BB) { ELEM_H(n, 0u); ELEM_H(n, 4096u); } else ELEM_H(n, dsub); } while (0)

  bf16x8 qf[2][8];
  f32x16 o[4][2];
  {
    constexpr int QE = 10;
    const unsigned rot = b + 4u * (idx & 1u);
    const srd_t sw = MKSRD((BB ? p.wqB : p.wqA) + h0 * 256u, 4 << 20), sx = MKSRD((BB ? p.xB : p.xA) + (size_t)b * 65536u, 65536);
    unsigned wvo[4], xvo[4];
#pragma unroll
    for (int i = 0; i < 4; ++i) {
      wvo[i] = g4 * 8192u + ((l15 ^ (g4 << 2)) * 16u) + i * (32768u - 1024u);
      xvo[i] = (lane >> 3) * 1024u + (((lane & 7u) ^ ((4u * i + g4) & 7u)) * 16u) + i * (8192u - 1024u);
    }
#define WOFF(s) ((BB ? (s) % 4 : ((s) % 4 == 0 ? 0u : (s) % 4 == 1 ? 8u : (s) % 4 == 2 ? 3u : 6u)) * SLOT)
#define XOFF(s) ((BB ? 5u * ((s) % 4) + 4u : ((s) % 4 == 0 ? 2u : (s) % 4 == 1 ? 12u : (s) % 4 == 2 ? 5u : 15u)) * SLOT)
    const unsigned wdst = ldsb + (BB ? item * REG + half * 4096u : wid * 4096u), xdst = ldsb + wid * 2048u;
#define QLD1(s, j) do { const unsigned cv_ = ((s) + rot) & 7u; \
      if ((j) == 0) dma4<false>(wdst + WOFF(s), wvo, sw, (cv_ * 64u + wid * 16u) * 8192u); \
      if ((j) == 1) dma4<false>(wdst + WOFF(s) + 2u * REG, wvo, sw, (cv_ * 64u + wid * 16u) * 8192u + 256u); \
      if ((j) == 2) { SETM0N(xdst + XOFF(s)); dma2<0, false>(xvo[0], xvo[1], sx, wid * 16384u + cv_ * 128u); } } while (0)
#define QLD(s) do { QLD1(s, 0); QLD1(s, 1); QLD1(s, 2); } while (0)
    const unsigned p4 = lane & 3u, q4 = (lane >> 2) & 3u;
    const unsigned wa0 = ((ldsb + (8u * hh + q4) * 256u + (q4 << 6) + (g4 & 1u) * 32u + (((p4 & 1u) << 1) | (p4 >> 1)) * 8u) ^ (128u * half)) + item * (2u * REG), wa1 = wa0 ^ 64u;
    const unsigned xa = ldsb + l31 * 128u + ((hh ^ ((l31 >> 1) & 7u)) * 16u);
    f32x16 acc[2][2];
    bf16x8 xf[2][2][4], wf[2][2][4];
#define QM1(s, ks, g) do { if ((s) == 0 && (ks) == 0) asm volatile("v_mfma_f32_32x32x16_bf16 %0, %1, %2, 0" : "=&v"(acc[(g) >> 1][(g) & 1]) : "v"(wf[(s) & 1][(g) & 1][ks]), "v"(xf[(s) & 1][(g) >> 1][ks])); \
      else asm volatile("v_mfma_f32_32x32x16_bf16 %0, %1, %2, %0" : "+v"(acc[(g) >> 1][(g) & 1]) : "v"(wf[(s) & 1][(g) & 1][ks]), "v"(xf[(s) & 1][(g) >> 1][ks])); } while (0)
#define WKS(ks) (BB ? ((ks) >> 1) * REG + ((ks) & 1) * 4096u : (ks) * 4096u)
#define DG(m0v, vo, srd, soff, NT, cnt, g) do { if ((g) < 0) SETM0(m0v); else if ((g) < (cnt)) dma1<((g) & 3) * 1024, NT>(vo[(g) & 3], srd, soff); } while (0)
#define QG(s, j, g) do { const unsigned cv_ = ((s) + rot) & 7u; \
      if ((j) == 0) DG(wdst + WOFF(s), wvo, sw, (cv_ * 64u + wid * 16u) * 8192u, false, 4, g); \
      if ((j) == 1) DG(wdst + WOFF(s) + 2u * REG, wvo, sw, (cv_ * 64u + wid * 16u) * 8192u + 256u, false, 4, g); \
      if ((j) == 2) DG(xdst + XOFF(s), xvo, sx, wid * 16384u + cv_ * 128u, false, 2, g); } while (0)
#define EG(n, j, g) do { if ((n) < 2 || ((n) & 1)) DG(ring + ((n) % NS) * SLOT + (j), kvo, sK, ((n) < 2 ? (n) : ((n) + 1) / 2) * 8192u + (j), NTK, 4, g); \
                         else DG(ring + ((n) % NS) * SLOT + (j), vv4, sV, ((n) - 2) / 2 * 8192u + (j), NTV, 4, g); } while (0)
#define QOP(HOOK, NOP, G, v) do { if ((v) < (NOP) && ((v) * 16) / (NOP) == (G)) { HOOK((v) >> 2, (v) & 3); if (((v) & 3) == 3 && (v) + 1 < (NOP)) { HOOK(((v) >> 2) + 1, -1); } } } while (0)
#define QGAP(s, ks, g, HOOK, NOP) do { if ((s) > 0) QM1((s) - 1, ks, g); else asm volatile("s_nop 0"); \
        if ((g) == 0) { xf[(s) & 1][0][ks] = lds_rd128(x_ ^ (32u * (ks))); xf[(s) & 1][1][ks] = lds_rd128((x_ ^ (32u * (ks))) + 4096u); } \
        if ((g) == 1) wf[(s) & 1][0][ks] = cat8(lds_tr(w0_ + WKS(ks)), lds_tr(w0_ + WKS(ks) + 1024u)); \
        if ((g) == 2) wf[(s) & 1][1][ks] = cat8(lds_tr(w1_ + WKS(ks)), lds_tr(w1_ + WKS(ks) + 1024u)); \
        SBAR(); \
        QOP(HOOK, NOP, 4 * (ks) + (g), 0); QOP(HOOK, NOP, 4 * (ks) + (g), 1); QOP(HOOK, NOP, 4 * (ks) + (g), 2); QOP(HOOK, NOP, 4 * (ks) + (g), 3); \
        QOP(HOOK, NOP, 4 * (ks) + (g), 4); QOP(HOOK, NOP, 4 * (ks) + (g), 5); QOP(HOOK, NOP, 4 * (ks) + (g), 6); QOP(HOOK, NOP, 4 * (ks) + (g), 7); \
        QOP(HOOK, NOP, 4 * (ks) + (g), 8); QOP(HOOK, NOP, 4 * (ks) + (g), 9); QOP(HOOK, NOP, 4 * (ks) + (g), 10); QOP(HOOK, NOP, 4 * (ks) + (g), 11); \
        QOP(HOOK, NOP, 4 * (ks) + (g), 12); QOP(HOOK, NOP, 4 * (ks) + (g), 13); QOP(HOOK, NOP, 4 * (ks) + (g), 14); QOP(HOOK, NOP, 4 * (ks) + (g), 15); \
        SBAR(); } while (0)
#define QKS(s, ks, HOOK, NOP) do { QGAP(s, ks, 0, HOOK, NOP); QGAP(s, ks, 1, HOOK, NOP); QGAP(s, ks, 2, HOOK, NOP); QGAP(s, ks, 3, HOOK, NOP); } while (0)
#define QCMP(s, HOOK, NOP) do { const unsigned w0_ = wa0 + WOFF(s), w1_ = wa1 + WOFF(s), x_ = xa + XOFF(s); HOOK(0, -1); SBAR(); \
      QKS(s, 0, HOOK, NOP); QKS(s, 1, HOOK, NOP); QKS(s, 2, HOOK, NOP); QKS(s, 3, HOOK, NOP); } while (0)
#define HQ0(j, g) QG(3, j, g)
#define HQ1(j, g) QG(4, j, g)
#define HQ2(j, g) QG(5, j, g)
#define HQ3(j, g) QG(6, j, g)
#define HQ4(j, g) QG(7, j, g)
#define HQ5(j, g) do { if (BB) { if ((j) == 0) EG(0, 0u, g); if ((j) == 1) EG(0, 4096u, g); } else { if ((j) == 0) EG(0, dsub, g); if ((j) == 1) EG(1, dsub, g); } } while (0)
#define HQ6(j, g) do { if (BB) { if ((j) == 0) EG(1, 0u, g); if ((j) == 1) EG(1, 4096u, g); } else { if ((j) == 0) EG(2, dsub, g); } } while (0)
#define HQ7(j, g) do { if (BB) { if ((j) == 0) EG(2, 0u, g); if ((j) == 1) EG(2, 4096u, g); } else { if ((j) == 0) EG(3, dsub, g); if ((j) == 1) EG(4, dsub, g); } } while (0)
    unsigned tch = 0u;
#define TOUCH(srd, soff, stride) asm volatile("buffer_load_dword %0, %1, %2, %3 offen" : "+v"(tch) : "v"(lane * (stride)), "s"(srd), "s"(soff) : "memory")
    constexpr int NTCH = BB ? 0 : TCH_KV;
    QLD(0); QLD(1);
#pragma unroll
    for (int db = 0; db < 4; ++db) { o[db][0] = f32x16{}; o[db][1] = f32x16{}; asm volatile("" : "+a"(o[db][0]), "+a"(o[db][1])); }
    QLD(2);
#pragma unroll
    for (int i = 0; i < NTCH; ++i) TOUCH(item ? sV : sK, (2u * i + (idx & 1u)) * 8192u, 128u);
    waitv<2 * QE + NTCH>(); BARRIER(); QCMP(0, HQ0, QE);
    waitv<2 * QE + NTCH>(); BARRIER(); QCMP(1, HQ1, QE);
    waitv<2 * QE + NTCH>(); BARRIER(); QCMP(2, HQ2, QE);
    waitv<2 * QE>(); BARRIER(); QCMP(3, HQ3, QE);
    waitv<2 * QE>(); BARRIER(); QCMP(4, HQ4, QE);
    waitv<2 * QE>(); BARRIER(); QCMP(5, HQ5, 8);
    waitv<QE + 8>(); BARRIER(); QCMP(6, HQ6, (BB ? 8 : 4));
    waitv<BB ? 16 : 12>(); BARRIER(); QCMP(7, HQ7, 8);
    PIN(tch);
    const unsigned sp = BB ? reg + 4u * SLOT : ldsb + item * (2u * REG) + 8u * SLOT + half * SLOT, psp = BB ? preg + 4u * SLOT : sp ^ SLOT;
#define QO1(g, h2) lds_wru4(sp + (g) * 2048u + (h2) * 1024u + lane * 16u, pack8(acc[(g) >> 1][(g) & 1], h2))
    QM1(7, 0, 0); QM1(7, 1, 0); QM1(7, 2, 0); QM1(7, 3, 0); SBAR();
    if (BB) { BARRIER(); }
    QM1(7, 0, 1); QM1(7, 1, 1); QM1(7, 2, 1); QM1(7, 3, 1); asm volatile("s_nop 7" : "+v"(acc[0][0])); SBAR();
    QM1(7, 0, 2); QO1(0, 0); SBAR(); QM1(7, 1, 2); QM1(7, 2, 2); QO1(0, 1); SBAR(); QM1(7, 3, 2); PIN(acc[0][1]); SBAR();
    QM1(7, 0, 3); QO1(1, 0); SBAR(); QM1(7, 1, 3); QM1(7, 2, 3); QO1(1, 1); SBAR(); QM1(7, 3, 3); PIN(acc[1][0]); SBAR();
    QO1(2, 0); QO1(2, 1); SBAR(); asm volatile("s_nop 7" : "+v"(acc[1][1])); QO1(3, 0); QO1(3, 1);
    BARRIER();
    const unsigned lo = (half ? psp : sp) + lane * 16u, hi = (half ? sp : psp) + lane * 16u, q0 = half * 4096u, q1 = 4096u - q0;
#pragma unroll
    for (int j = 0; j < 4; ++j) {
      qf[0][j] = lds_rd128(lo + q0 + j * 1024u); qf[0][4 + j] = lds_rd128(hi + q0 + j * 1024u);
      qf[1][j] = lds_rd128(lo + q1 + j * 1024u); qf[1][4 + j] = lds_rd128(hi + q1 + j * 1024u);
    }
  }

  PIN(epoch_v);
  const u64 tag = mix64(((u64)rfl((unsigned)(epoch_v >> 32)) << 32) | rfl((unsigned)epoch_v));

  unsigned tA = (NS / 2u + 1u) * 8192u + dsub, tB = (NS / 2u) * 8192u + dsub;
  const unsigned dring = ring + dsub;
  const unsigned kb = ring + l31 * 256u + ((hh ^ l15) * 16u);
  const unsigned vb = ring + (4u * hh + ((lane >> 2) & 3u)) * 256u + (((lane >> 2) & 3u) << 6) + (g4 & 1u) * 32u + (lane & 3u) * 8u;
  f32x16 sA[2], sB[2], negm[2];
  bf16x8 kf[8]; s16x4 vlo[4][2], vhi[4][2]; u32x4 pw[2][2];
  float f0 = 1.f, f1 = 1.f, ls0 = 0.f, lt0 = 0.f, ls1 = 0.f, lt1 = 0.f;
#define QK0(D, a, bq) asm volatile("v_mfma_f32_32x32x16_bf16 %0, %1, %2, 0" : "=&v"(D) : "v"(a), "a"(bq))
#define QKC(D, a, bq, C) asm volatile("v_mfma_f32_32x32x16_bf16 %0, %1, %2, %3" : "=&v"(D) : "v"(a), "a"(bq), "v"(C))
#define QKA(D, a, bq) asm volatile("v_mfma_f32_32x32x16_bf16 %0, %1, %2, %0" : "+v"(D) : "v"(a), "a"(bq))
#define WAITE(k) do { waitv<PE * (k)>(); if (BB) { WAITL(); } else { BARRIER(); } } while (0)
#define WAITR() WAITE(NS - 2)
#define EXP2(x) __builtin_amdgcn_exp2f((x) * C2)
  WAITE(BB ? 2 : 4);
#pragma unroll
  for (int s = 0; s < 8; ++s) kf[s] = lds_rd128(kb ^ (32u * s));
  QK0(sA[0], kf[0], qf[0][0]); QK0(sA[1], kf[0], qf[1][0]);
#pragma unroll
  for (int s = 1; s < 8; ++s) { QKA(sA[0], kf[s], qf[0][s]); QKA(sA[1], kf[s], qf[1][s]);
    if (s == 2) { if (BB) ELEM(3); else ELEM(5); } if (s == 4 && !BB) ELEM(6); if (s == 6 && !BB) ELEM(7); }
  WAITR();
  if (BB) asm volatile("s_barrier" ::: "memory");
  ELEM(NS);
#pragma unroll
  for (int s = 0; s < 8; ++s) kf[s] = lds_rd128((kb + SLOT) ^ (32u * s));
  asm volatile("s_nop 7\n\ts_nop 7" : "+v"(sA[0]), "+v"(sA[1]));
#pragma unroll
  for (int qb = 0; qb < 2; ++qb) {
    const float m0 = swap_max(max8hi(max8lo(sA[qb]), sA[qb]));
#pragma unroll
    for (int r = 0; r < 16; ++r) { sA[qb][r] -= m0; negm[qb][r] = -m0; }
    PIN(negm[qb]);
#pragma unroll
    for (int r = 0; r < 8; ++r) sA[qb][r] = EXP2(sA[qb][r]);
  }
#pragma unroll
  for (int r = 0; r < 8; r += 2) { ls0 += sA[0][r]; lt0 += sA[0][r + 1]; ls1 += sA[1][r]; lt1 += sA[1][r + 1]; }

  unsigned so1 = 1u * SLOT, so2 = 2u * SLOT, so3 = 3u * SLOT;
#define ADV2(x) x = (x + 2u * SLOT) & (NS * SLOT - 1u)
#define VRD(db, ks) do { const unsigned a_ = (va ^ (64u * (db))) + 4096u * (ks); vlo[db][ks] = lds_tr(a_); vhi[db][ks] = lds_tr(a_ + 2048u); } while (0)
#define GAPA(C, N, s, QB, LAST) do { \
    if ((s) == 0) QKC(N[QB], kf[0], qf[QB][0], negm[QB]); else QKA(N[QB], kf[s], qf[QB][s]); \
    if ((s) < 4) { float e0_ = EXP2(C[QB][(8 + 2 * (s)) & 15]), e1_ = EXP2(C[QB][(9 + 2 * (s)) & 15]); PIN(e0_); PIN(e1_); C[QB][(8 + 2 * (s)) & 15] = e0_; C[QB][(9 + 2 * (s)) & 15] = e1_; \
      pw[QB][0][(s) & 3] = cvtpk(C[QB][2 * (s)], C[QB][2 * (s) + 1]); PIN(pw[QB][0]); } \
    else { ls##QB += C[QB][2 * (s)]; lt##QB += C[QB][2 * (s) + 1]; pw[QB][1][(s) & 3] = cvtpk(C[QB][2 * (s)], C[QB][2 * (s) + 1]); PIN(pw[QB][1]); PIN(ls##QB); PIN(lt##QB); } \
    if ((s) < 4) { VRD((2 * (s) + QB) & 3, ((s) >> 1) & 1); } \
    if (BB && QB == 0 && (s) == 6) SETM0(dring + so1 + 4096u); \
    if (LAST) { if (BB) { \
      if (QB == 1 && (s) == 4) dma2<0, NT_WO>(wvo[0], wvo[1], sW, WSO(0)); \
      if (QB == 1 && (s) == 5) dma2<2048, NT_WO>(wvo[2], wvo[3], sW, WSO(0)); \
      if (QB == 1 && (s) == 6) dma2<0, NT_WO>(wvo[0], wvo[1], sW, WSO(0) + 16384u); \
      if (QB == 1 && (s) == 7) dma2<2048, NT_WO>(wvo[2], wvo[3], sW, WSO(0) + 16384u); } \
    } else { \
      if (QB == 1 && (s) == 4) dma2<0, NTK>(kvo[0], kvo[1], sK, tA); \
      if (QB == 1 && (s) == 5) dma2<2048, NTK>(kvo[2], kvo[3], sK, tA); \
      if (BB && QB == 1 && (s) == 6) dma2<0, NTK>(kvo[0], kvo[1], sK, tA + 4096u); \
      if (BB && QB == 1 && (s) == 7) dma2<2048, NTK>(kvo[2], kvo[3], sK, tA + 4096u); } \
    SBAR(); } while (0)
#define PHASE_A(C, N, L) do { \
    if (!BB && ASLP > 0) __builtin_amdgcn_s_sleep(ASLP); \
    SETM0(dring + so1); WAITR(); SBAR(); \
    const unsigned va = vb + so2; \
    GAPA(C, N, 0, 0, L); GAPA(C, N, 0, 1, L); GAPA(C, N, 1, 0, L); GAPA(C, N, 1, 1, L); GAPA(C, N, 2, 0, L); GAPA(C, N, 2, 1, L); GAPA(C, N, 3, 0, L); GAPA(C, N, 3, 1, L); \
    GAPA(C, N, 4, 0, L); GAPA(C, N, 4, 1, L); GAPA(C, N, 5, 0, L); GAPA(C, N, 5, 1, L); GAPA(C, N, 6, 0, L); GAPA(C, N, 6, 1, L); GAPA(C, N, 7, 0, L); GAPA(C, N, 7, 1, L); \
    tA += 8192u; } while (0)
#define PVM(i) o[((i) >> 1) & 3][(i) & 1] = MFMA32(cat8(vlo[((i) >> 1) & 3][(i) >> 3], vhi[((i) >> 1) & 3][(i) >> 3]), __builtin_bit_cast(bf16x8, pw[(i) & 1][(i) >> 3]), o[((i) >> 1) & 3][(i) & 1])
#define KRD(i) kf[i] = lds_rd128(ka ^ (32u * (i)))
#define GAPE(N, j) do { PVM(8 + (j)); N[0][j] = EXP2(N[0][j]); N[1][j] = EXP2(N[1][j]); \
    if ((j) > 0) { if ((j) & 1) { ls0 += N[0][((j) + 15) & 15]; ls1 += N[1][((j) + 15) & 15]; } else { lt0 += N[0][((j) + 15) & 15]; lt1 += N[1][((j) + 15) & 15]; } } \
    PIN(N[0]); PIN(N[1]); PIN(ls0); PIN(ls1); PIN(lt0); PIN(lt1); SBAR(); } while (0)
#define DMAB(OFF, i0, i1, hi, LAST) do { if (LAST) { if (BB) dma2<OFF, NT_WO>(wvo[i0], wvo[i1], sW, WSO(1) + (hi) * 16384u); } \
    else if (BB || !(hi)) dma2<OFF, NTV>(vvo, vvo, sV, tB + (hi) * 4096u); } while (0)
#define PHASE_B(N, LAST) do { \
    SETM0(dring + so2); if (LAST) { WAITL(); asm volatile("s_nop 7"); } else { WAITR(); } SBAR(); \
    const unsigned ka = kb + so3; \
    PVM(0); KRD(0); KRD(1); DMAB(0, 0, 1, 0, LAST); SBAR(); \
    PVM(1); KRD(2); KRD(3); DMAB(2048, 2, 3, 0, LAST); if (BB) SETM0(dring + so2 + 4096u); SBAR(); \
    PVM(2); KRD(4); float ma0 = max3f(N[0][0], N[0][1], N[0][2]), ma1 = max3f(N[1][0], N[1][1], N[1][2]), mb0 = max3f(N[0][3], N[0][4], N[0][5]), mb1 = max3f(N[1][3], N[1][4], N[1][5]); SBAR(); \
    PVM(3); KRD(5); ma0 = max3f(ma0, N[0][6], N[0][7]); ma1 = max3f(ma1, N[1][6], N[1][7]); mb0 = max3f(mb0, N[0][8], N[0][9]); mb1 = max3f(mb1, N[1][8], N[1][9]); SBAR(); \
    PVM(4); KRD(6); ma0 = max3f(ma0, N[0][10], N[0][11]); ma1 = max3f(ma1, N[1][10], N[1][11]); mb0 = max3f(mb0, N[0][12], N[0][13]); mb1 = max3f(mb1, N[1][12], N[1][13]); SBAR(); \
    PVM(5); ma0 = max3f(ma0, N[0][14], N[0][15]); ma1 = max3f(ma1, N[1][14], N[1][15]); const float mbb = max2f(mb0, mb1); KRD(7); SBAR(); \
    PVM(6); const float mall = max3f(ma0, ma1, mbb); DMAB(0, 0, 1, 1, LAST); const bool grew = __any(mall > THR); SBAR(); \
    PVM(7); DMAB(2048, 2, 3, 1, LAST); SBAR(); \
    if (__builtin_expect(grew, 0)) {     \
      const float d0 = max2f(swap_max(max2f(ma0, mb0)), 0.f), d1 = max2f(swap_max(max2f(ma1, mb1)), 0.f); \
      f0 = EXP2(-d0); f1 = EXP2(-d1); ls0 *= f0; lt0 *= f0; ls1 *= f1; lt1 *= f1; \
      sub_tile(N[0], d0); sub_tile(N[1], d1); sub_tile(negm[0], d0); sub_tile(negm[1], d1); \
    } \
    SBAR(); \
    GAPE(N, 0); GAPE(N, 1); GAPE(N, 2); GAPE(N, 3); GAPE(N, 4); GAPE(N, 5); GAPE(N, 6); GAPE(N, 7); \
    lt0 += N[0][7]; lt1 += N[1][7]; \
    if (__builtin_expect(grew, 0)) { \
      asm volatile("s_nop 7\n\ts_nop 7" ::: "memory"); \
      _Pragma("unroll") for (int db = 0; db < 4; ++db) { scale_acc(o[db][0], f0); scale_acc(o[db][1], f1); } \
    } \
    ADV2(so1); ADV2(so2); ADV2(so3); tB += 8192u; } while (0)

  unsigned wvo[4];
  const srd_t sW = MKSRD((BB ? p.woB : p.woA) + (size_t)h0 * 131072u + wid * 256u, 262144 - 768);
#define WSO(i) ((((i) + b) & 7u) * 32768u)
#define WDMA(i, slot) do { dma4<NT_WO>(reg + (slot) * SLOT, wvo, sW, WSO(i)); dma4<NT_WO>(reg + (slot) * SLOT + 4096u, wvo, sW, WSO(i) + 16384u); } while (0)
  if (tid == 0u) __hip_atomic_store((GAS u64*)(p.ws + WS_FLAG3) + (size_t)blockIdx.x * 8u, tag, RLX_AGENT);
  for (unsigned t = 0; t < 14u; t += 2u) {
    PHASE_A(sA, sB, false); PHASE_B(sB, false);
    PHASE_A(sB, sA, false); PHASE_B(sA, false);
  }
#pragma unroll
  for (int j = 0; j < 4; ++j) wvo[j] = g4 * 1024u + ((l15 ^ (g4 << 2)) * 16u) + j * 3072u;
  PHASE_A(sA, sB, true); PHASE_B(sB, true);
  float l0, l1;
  {
#pragma unroll
    for (int r = 8; r < 16; ++r) { sB[0][r] = EXP2(sB[0][r]); sB[1][r] = EXP2(sB[1][r]); ls0 += sB[0][r]; ls1 += sB[1][r]; }
#pragma unroll
    for (int i = 0; i < 8; ++i) { pw[0][i >> 2][i & 3] = cvtpk(sB[0][2 * i], sB[0][2 * i + 1]); pw[1][i >> 2][i & 3] = cvtpk(sB[1][2 * i], sB[1][2 * i + 1]); }
    PIN(pw[0][0]); PIN(pw[0][1]); PIN(pw[1][0]); PIN(pw[1][1]);
    l0 = swap_add(ls0 + lt0); l1 = swap_add(ls1 + lt1); PIN(l0); PIN(l1);
    SBAR();
    WAITE(BB ? 2 : 0);
    const unsigned va = vb + so2;
#pragma unroll
    for (int i = 0; i < 8; ++i) VRD(i & 3, i >> 2);
    PVM(1); PVM(3); PVM(5); PVM(7); PVM(9); PVM(11); PVM(13); PVM(15);
    if (BB) { WAITL(); } else { BARRIER(); }
    SBAR();
    if (!BB) { WDMA(0, 1u); WDMA(1, 2u); }
    const unsigned xo = reg + 3u * SLOT + lane * 16u;
#define XW(i) lds_wrf4(xo + (i) * 1024u, f32x4{o[(i) >> 2][1][4 * ((i) & 3)], o[(i) >> 2][1][4 * ((i) & 3) + 1], o[(i) >> 2][1][4 * ((i) & 3) + 2], o[(i) >> 2][1][4 * ((i) & 3) + 3]})
    *(LAS f32x2*)(unsigned long)(reg + lane * 8u) = f32x2{-negm[1][0], l1};
    PVM(0); XW(0); XW(1); SBAR(); PVM(2); XW(2); XW(3); SBAR(); PVM(4); XW(4); XW(5); SBAR(); PVM(6); XW(6); XW(7); SBAR();
    PVM(8); XW(8); XW(9); SBAR(); PVM(10); XW(10); XW(11); SBAR(); PVM(12); XW(12); XW(13); SBAR(); PVM(14); XW(14); XW(15); SBAR();
  }
  s16x4 wlo[2][4][2], whi[2][4][2];
  const unsigned wb = vb - ring + reg;
#define WRD(set, slot, nb, ks) do { const unsigned a_ = (wb ^ (64u * (nb))) + (slot) * SLOT + 4096u * (ks); wlo[set][nb][ks] = lds_tr(a_); whi[set][nb][ks] = lds_tr(a_ + 2048u); } while (0)
#define WG(i, slot, hi, g) DG(reg + (slot) * SLOT + (hi) * 4096u, wvo, sW, WSO(i) + (hi) * 16384u, NT_WO, 4, g)
  {
    const unsigned xi = preg + 3u * SLOT + lane * 16u;
    if (BB) {
      waitv<8>();
#pragma unroll
      for (int i = 0; i < 8; ++i) WRD(0, 1u, i & 3, i >> 2);
    }
    BARRIER();
    const f32x2 ml = *(const LAS f32x2*)(unsigned long)(preg + lane * 8u);
    f32x4 xr[16];
#pragma unroll
    for (int i = 0; i < 16; ++i) xr[i] = lds_rdf4(xi + i * 1024u);
    asm volatile("" : "+a"(o[0][0]), "+a"(o[1][0]), "+a"(o[2][0]), "+a"(o[3][0]));
    if (BB) WDMA(2, 1u);
    const float m0 = -negm[0][0], mm = __builtin_fmaxf(m0, ml[0]);
    float a0 = EXP2(m0 - mm), a1 = EXP2(ml[0] - mm);
    const float inv = __builtin_amdgcn_rcpf(a0 * l0 + a1 * ml[1]);
    a0 *= inv; a1 *= inv;
    WAITL();
#pragma unroll
    for (int db = 0; db < 4; ++db)
#pragma unroll
      for (int ks = 0; ks < 2; ++ks) {
        float y[8];
#pragma unroll
        for (int e = 0; e < 4; ++e) {
          y[e] = a0 * o[db][0][8 * ks + e] + a1 * xr[db * 4 + 2 * ks][e];
          y[4 + e] = a0 * o[db][0][8 * ks + 4 + e] + a1 * xr[db * 4 + 2 * ks + 1][e];
        }
        lds_wru4(xi + (db * 2 + ks) * 1024u, u32x4{cvtpk(y[0], y[1]), cvtpk(y[2], y[3]), cvtpk(y[4], y[5]), cvtpk(y[6], y[7])});
      }
    if (!BB) {
      waitv<8>();
#pragma unroll
      for (int i = 0; i < 8; ++i) WRD(0, 1u, i & 3, i >> 2);
      WAITL(); WDMA(2, 1u);
    }
    waitv<8>();
#pragma unroll
    for (int i = 0; i < 8; ++i) WRD(1, 2u, i & 3, i >> 2);
    BARRIER();
  }
  constexpr unsigned br = BB ? 1u : 0u;
  const srd_t sP = MKSRD(p.ws + WS_PART, 16 << 20);
  GAS u64* flags = (GAS u64*)(p.ws + WS_FLAG);
  const unsigned hp = h0 >> 1;
  {
    bf16x8 of[8][2][2];
#define ORD(i) do { const unsigned ti_ = ((i) + b) & 7u, a_ = ldsb + (ti_ >> 2) * (2u * REG) + 3u * SLOT + (ti_ & 3u) * 2048u + lane * 16u; \
      of[i][0][0] = lds_rd128(a_ + REG); of[i][1][0] = lds_rd128(a_); of[i][0][1] = lds_rd128(a_ + REG + 1024u); of[i][1][1] = lds_rd128(a_ + 1024u); } while (0)
    ORD(0);
    f32x16 acc[4][2];
    const f32x16 z16 = {};
#define OM(i, nb, ks, qb) do { acc[nb][qb] = MFMA32(cat8(wlo[(i) & 1][nb][ks], whi[(i) & 1][nb][ks]), of[i][qb][ks], ((i) == 0 && (ks) == 0) ? z16 : acc[nb][qb]); \
      asm volatile("" : "+a"(acc[nb][qb])); } while (0)
#define WG2(v, TA, SA, TB_, SB) do { if ((v) < 8) WG((TA) & 7, SA, ((v) >> 2) & 1, (v) & 3); else WG((TB_) & 7, SB, ((v) >> 2) & 1, (v) & 3); \
      if ((v) == 3) WG((TA) & 7, SA, 1, -1); if ((v) == 7 && (TB_) >= 0) WG((TB_) & 7, SB, 0, -1); if ((v) == 11) WG((TB_) & 7, SB, 1, -1); } while (0)
#define OX(i, x, RS, TA, SA, TB_, SB) do { \
      OM(i, (x) & 3, (x) >> 2, 0); if ((i) > 0) WRD(((i) + 1) & 1, RS, (x) & 3, (x) >> 2); if ((x) == 0) ORD((i) + 1); SBAR(); \
      if ((TB_) >= 0) WG2(2 * (x), TA, SA, TB_, SB); else if ((TA) >= 0) WG2(x, TA, SA, TB_, SB); \
      SBAR(); OM(i, (x) & 3, (x) >> 2, 1); SBAR(); \
      if ((TB_) >= 0) WG2(2 * (x) + 1, TA, SA, TB_, SB); \
      SBAR(); } while (0)
#define OT(i, RS, N, TA, SA, TB_, SB) do { if ((TA) >= 0) WG((TA) & 7, SA, 0, -1); waitv<N>(); if ((i) >= 2) { WAITL(); } SBAR(); \
      OX(i, 0, RS, TA, SA, TB_, SB); OX(i, 1, RS, TA, SA, TB_, SB); OX(i, 2, RS, TA, SA, TB_, SB); OX(i, 3, RS, TA, SA, TB_, SB); \
      OX(i, 4, RS, TA, SA, TB_, SB); OX(i, 5, RS, TA, SA, TB_, SB); OX(i, 6, RS, TA, SA, TB_, SB); OX(i, 7, RS, TA, SA, TB_, SB); } while (0)
    OT(0, 0u, 8, 3, 2u, 4, 4u);
    OT(1, 1u, 16, 5, 0u, -1, 0u);
    OT(2, 2u, 16, 6, 1u, -1, 0u);
    OT(3, 4u, 16, 7, 2u, -1, 0u);
    OT(4, 0u, 16, -1, 0u, -1, 0u);
    OT(5, 1u, 8, -1, 0u, -1, 0u);
    const unsigned pb = (br * 8u + b) * 1048576u + 8u * wid * 16384u + hp * 1024u + lane * 16u;
#define PST(nb, qb, s2) __builtin_amdgcn_raw_buffer_store_b128(pack8(acc[nb][qb], s2), sP, pb + ((qb) * 32 + (nb) * 2 + (s2)) * 16384u, 0, 16)
    waitv<0>(); SBAR();
    ORD(7);
#pragma unroll
    for (int i = 0; i < 8; ++i) WRD(1, 2u, i >> 1, i & 1);
    SBAR();
#define OF(nb, pn) do { \
      OM(6, nb, 0, 0); OM(6, nb, 0, 1); SBAR(); OM(6, nb, 1, 0); if ((pn) >= 0) PST((pn) & 3, 0, 0); SBAR(); OM(6, nb, 1, 1); SBAR(); OM(7, nb, 0, 0); if ((pn) >= 0) PST((pn) & 3, 0, 1); SBAR(); \
      OM(7, nb, 0, 1); SBAR(); OM(7, nb, 1, 0); if ((pn) >= 0) PST((pn) & 3, 1, 0); SBAR(); OM(7, nb, 1, 1); if ((pn) >= 0) PST((pn) & 3, 1, 1); SBAR(); } while (0)
    OF(0, -1); OF(1, 0); OF(2, 1); OF(3, 2);
    PST(3, 0, 0); PST(3, 0, 1); PST(3, 1, 0); PST(3, 1, 1);
    waitv<0>();
    if (lane == 0) __hip_atomic_store(flags + (size_t)(((br * 8u + b) * 4u + wid) * 16u + hp) * 8u, tag, RLX_AGENT);
  }
  {
    const unsigned u = hp * 4u + wid;
    poll_flags<2>(flags + (size_t)(((br * 8u + b) * 4u + ((u >> 3) & 3u)) * 16u + l15) * 8u, tag);
    const unsigned rb = (br * 8u + b) * 1048576u + u * 16384u + lane * 16u;
    u32x4 pv[16];
#pragma unroll
    for (int j = 0; j < 16; ++j) pv[j] = __builtin_amdgcn_raw_buffer_load_b128(sP, rb + j * 1024u, 0, 16);
    float y[8] = {0.f, 0.f, 0.f, 0.f, 0.f, 0.f, 0.f, 0.f};
#pragma unroll
    for (int j = 0; j < 16; ++j)
#pragma unroll
      for (int e = 0; e < 4; ++e) { y[2 * e] += __uint_as_float(pv[j][e] << 16); y[2 * e + 1] += __uint_as_float(pv[j][e] & 0xffff0000u); }
    const auto r0 = __builtin_amdgcn_permlane32_swap(cvtpk(y[0], y[1]), cvtpk(y[4], y[5]), false, false);
    const auto r1 = __builtin_amdgcn_permlane32_swap(cvtpk(y[2], y[3]), cvtpk(y[6], y[7]), false, false);
    *(u32x4*)(p.out + (size_t)(b * 64u + (u >> 5) * 32u + l31) * 2048u + (br * 512u + (u & 31u) * 16u + 8u * hh) * 2u) = u32x4{r0[0], r1[0], r0[1], r1[1]};
    if (!BB && blockIdx.x == 128u && wid == 0u) {
#pragma unroll 1
      for (unsigned spins = 0; spins < (1u << 21); ++spins) {
        bool ok = true;
#pragma unroll
        for (int k = 0; k < 4; ++k) ok &= (__hip_atomic_load((GAS u64*)(p.ws + WS_FLAG3) + (size_t)(k * 64 + lane) * 8u, RLX_AGENT) == tag);
        if (__all(ok)) break;
        __builtin_amdgcn_s_sleep(8);
      }
      if (lane == 0) __hip_atomic_store((GAS u64*)(p.ws + WS_EPOCH), tag, RLX_AGENT);
    }
  }
}

__global__ __launch_bounds__(256, 1) void k1_attn(P1 p) {
  extern __shared__ __attribute__((aligned(16))) char lds[];
  const unsigned ldsb = (unsigned)(unsigned long)lds;
  if (blockIdx.x < 128u) body<true>(p, ldsb); else body<false>(p, ldsb);
}

extern "C" void kernel_launch(void* const* d_in, const int* in_sizes, int n_in,
                              void* d_out, int out_size, void* d_ws, size_t ws_size,
                              hipStream_t stream) {
  (void)in_sizes; (void)n_in; (void)out_size; (void)ws_size;
  (void)hipFuncSetAttribute((const void*)k1_attn, hipFuncAttributeMaxDynamicSharedMemorySize, K1_LDS);
  P1 p1;
  p1.xA = (const char*)d_in[0]; p1.kA = (const char*)d_in[1]; p1.vA = (const char*)d_in[2]; p1.wqA = (const char*)d_in[3]; p1.woA = (const char*)d_in[4];
  p1.xB = (const char*)d_in[5]; p1.kB = (const char*)d_in[6]; p1.vB = (const char*)d_in[7]; p1.wqB = (const char*)d_in[8]; p1.woB = (const char*)d_in[9];
  p1.ws = (char*)d_ws; p1.out = (char*)d_out;
  hipLaunchKernelGGL(k1_attn, dim3(256), dim3(256), K1_LDS, stream, p1);
}
```

```cpp
#include <hip/hip_runtime.h>
#include <hip/hip_bf16.h>

typedef float    f32x16 __attribute__((ext_vector_type(16)));
typedef float    f32x4  __attribute__((ext_vector_type(4)));
typedef float    f32x2  __attribute__((ext_vector_type(2)));
typedef __bf16   bf16x8 __attribute__((ext_vector_type(8)));
typedef __bf16   bf16x2 __attribute__((ext_vector_type(2)));
typedef short    s16x4  __attribute__((ext_vector_type(4)));
typedef unsigned u32x4  __attribute__((ext_vector_type(4)));
typedef unsigned long long u64;
typedef __amdgpu_buffer_rsrc_t srd_t;
#define DI __device__ __forceinline__
#define LAS __attribute__((address_space(3)))
#define GAS __attribute__((address_space(1)))
#define SBAR() __builtin_amdgcn_sched_barrier(0)
#define PIN(x) asm volatile("" : "+v"(x))
#define MFMA32(a, b, c) __builtin_amdgcn_mfma_f32_32x32x16_bf16(a, b, c, 0, 0, 0)
#define RLX_AGENT __ATOMIC_RELAXED, __HIP_MEMORY_SCOPE_AGENT
#define MKSRD(ptr, n) __builtin_amdgcn_make_buffer_rsrc((void*)(ptr), (short)0, (n), 0x00020000)

constexpr unsigned SLOT = 8192u;
constexpr int K1_LDS = 163840;
constexpr float C2 = 0.08838834764831845f * 1.4426950408889634f;
#define THR_ 8.0f
constexpr float THR = THR_ / C2;
constexpr size_t WS_PART = 0;
constexpr size_t WS_FLAG = 16u << 20;
constexpr size_t WS_FLAG3 = WS_FLAG + 65536;
constexpr size_t WS_EPOCH = WS_FLAG3 + 16384;

DI unsigned rfl(unsigned x) { return __builtin_amdgcn_readfirstlane(x); }
DI unsigned cvtpk(float lo, float hi) { f32x2 v = {lo, hi}; bf16x2 b = __builtin_convertvector(v, bf16x2); return __builtin_bit_cast(unsigned, b); }
DI bf16x8 lds_rd128(unsigned a) { return *(const LAS bf16x8*)(unsigned long)a; }
DI f32x4 lds_rdf4(unsigned a) { return *(const LAS f32x4*)(unsigned long)a; }
DI void lds_wrf4(unsigned a, f32x4 v) { *(LAS f32x4*)(unsigned long)a = v; }
DI void lds_wru4(unsigned a, u32x4 v) { *(LAS u32x4*)(unsigned long)a = v; }
DI s16x4 lds_tr(unsigned a) { return __builtin_amdgcn_ds_read_tr16_b64_v4i16((LAS s16x4*)(unsigned long)a); }
DI bf16x8 cat8(s16x4 lo, s16x4 hi) { return __builtin_bit_cast(bf16x8, __builtin_shufflevector(lo, hi, 0, 1, 2, 3, 4, 5, 6, 7)); }
DI float max3f(float a, float b, float c) { float r; asm("v_max3_f32 %0, %1, %2, %3" : "=v"(r) : "v"(a), "v"(b), "v"(c)); return r; }
DI float max2f(float a, float b) { float r; asm("v_max_f32_e32 %0, %1, %2" : "=v"(r) : "v"(a), "v"(b)); return r; }
DI float swap_max(float m) { auto rr = __builtin_amdgcn_permlane32_swap(__float_as_uint(m), __float_as_uint(m), false, false); return max2f(__uint_as_float(rr[0]), __uint_as_float(rr[1])); }
DI float swap_add(float m) { auto rr = __builtin_amdgcn_permlane32_swap(__float_as_uint(m), __float_as_uint(m), false, false); return __uint_as_float(rr[0]) + __uint_as_float(rr[1]); }
DI float max8lo(const f32x16& p) { float a = max3f(p[0], p[1], p[2]), b = max3f(p[3], p[4], p[5]); return max3f(max3f(a, p[6], p[7]), b, b); }
DI float max8hi(float m, const f32x16& p) { float a = max3f(m, p[8], p[9]), b = max3f(p[10], p[11], p[12]); a = max3f(a, p[13], p[14]); return max3f(a, b, p[15]); }
DI void scale_acc(f32x16& x, float f) {
#pragma unroll
  for (int r = 0; r < 16; ++r) { float e = x[r], t;
    asm volatile("v_accvgpr_read_b32 %1, %0\n\tv_mul_f32 %1, %1, %2\n\tv_accvgpr_write_b32 %0, %1" : "+a"(e), "=&v"(t) : "v"(f));
    x[r] = e; }
}
DI void sub_tile(f32x16& x, float d) {
#pragma unroll
  for (int r = 0; r < 16; ++r) { float e = x[r]; asm volatile("v_sub_f32_e32 %0, %0, %1" : "+v"(e) : "v"(d)); x[r] = e; }
}
DI u64 mix64(u64 z) { z += 0x9E3779B97F4A7C15ull; z = (z ^ (z >> 30)) * 0xBF58476D1CE4E5B9ull; z = (z ^ (z >> 27)) * 0x94D049BB133111EBull; return z ^ (z >> 31); }

#define SETM0(x) asm volatile("s_mov_b32 m0, %0" :: "s"(x) : "memory")
#define SETM0N(x) asm volatile("s_mov_b32 m0, %0\n\ts_nop 0" :: "s"(x) : "memory")
template <int OFF, bool NT> DI void dma2(unsigned vo0, unsigned vo1, srd_t srd, unsigned soff) {
  if constexpr (NT)
    asm volatile("buffer_load_dwordx4 %0, %2, %3 offen offset:%4 nt lds\n\tbuffer_load_dwordx4 %1, %2, %3 offen offset:%5 nt lds"
                 :: "v"(vo0), "v"(vo1), "s"(srd), "s"(soff), "i"(OFF), "i"(OFF + 1024) : "memory");
  else
    asm volatile("buffer_load_dwordx4 %0, %2, %3 offen offset:%4 lds\n\tbuffer_load_dwordx4 %1, %2, %3 offen offset:%5 lds"
                 :: "v"(vo0), "v"(vo1), "s"(srd), "s"(soff), "i"(OFF), "i"(OFF + 1024) : "memory");
}
template <int OFF, bool NT> DI void dma1(unsigned vo, srd_t srd, unsigned soff) {
  if constexpr (NT) asm volatile("buffer_load_dwordx4 %0, %1, %2 offen offset:%3 nt lds" :: "v"(vo), "s"(srd), "s"(soff), "i"(OFF) : "memory");
  else asm volatile("buffer_load_dwordx4 %0, %1, %2 offen offset:%3 lds" :: "v"(vo), "s"(srd), "s"(soff), "i"(OFF) : "memory");
}
template <bool NT> DI void dma4(unsigned m0v, const unsigned (&vo)[4], srd_t srd, unsigned soff) { SETM0N(m0v); dma2<0, NT>(vo[0], vo[1], srd, soff); dma2<2048, NT>(vo[2], vo[3], srd, soff); }
template <int N> DI void waitv() { asm volatile("s_waitcnt vmcnt(%0)" :: "i"(N > 63 ? 63 : N) : "memory"); }
#define WAITL() do { asm volatile("" ::: "memory"); __builtin_amdgcn_s_waitcnt(0xC07F); asm volatile("" ::: "memory"); } while (0)
#define BARRIER() do { WAITL(); asm volatile("s_barrier" ::: "memory"); } while (0)

template <int SLP> DI void poll_flags(GAS u64* f, u64 tag) {
#pragma unroll 1
  for (unsigned spins = 0; spins < (1u << 21); ++spins) {
    const bool ok = (__hip_atomic_load(f, RLX_AGENT) == tag);
    if (__all(ok)) break;
    __builtin_amdgcn_s_sleep(SLP);
  }
  asm volatile("" ::: "memory");
}

#define STAMP(i)
#define STAMP_DECL(n)
#define STAMP_OUT(base, widx, id0, n)
#define KVREC (1024 * 256)
#define ASLP 8
#define TPOL ""
#define LATE 0
#define TCH_KV 2
constexpr bool NT_WO = false;

struct P1 {
  const char *xA, *kA, *vA, *wqA, *woA, *xB, *kB, *vB, *wqB, *woB;
  char* ws;
  char* out;
};

DI u32x4 pack8(const f32x16& x, int s) {
  return u32x4{cvtpk(x[8 * s], x[8 * s + 1]), cvtpk(x[8 * s + 2], x[8 * s + 3]), cvtpk(x[8 * s + 4], x[8 * s + 5]), cvtpk(x[8 * s + 6], x[8 * s + 7])};
}

constexpr unsigned ESZ = 16384u;
constexpr unsigned HREG = 5u * ESZ;

template <bool BB> DI void body(const P1& p, const unsigned ldsb) {
  STAMP_DECL(16); STAMP(0);
  constexpr int PE = BB ? 8 : 4;
  const unsigned tid = threadIdx.x, lane = tid & 63u, wid = rfl(tid >> 6);
  const unsigned g8 = blockIdx.x & 7u, idx = (blockIdx.x >> 3) & 15u;
  const unsigned qb = BB ? (wid & 1u) : (idx & 1u), item = wid >> 1;
  const unsigned h0 = BB ? 4u * g8 + 2u * (idx & 1u) : 4u * g8, b = idx >> 1;
  const unsigned l31 = lane & 31u, hh = lane >> 5, l15 = lane & 15u, g4 = lane >> 4;
  const unsigned hreg = ldsb + item * HREG;
  const unsigned ring = BB ? hreg : ldsb;
  const unsigned dsub = BB ? qb * 8192u : wid * 4096u;

  u64 epoch_v;
  asm volatile("global_load_dwordx2 %0, %1, off sc1" : "=v"(epoch_v) : "v"(p.ws + WS_EPOCH) : "memory");
  const size_t kvoff = (BB ? (size_t)(b * 32u + h0 + item) : (size_t)(b * 8u + g8)) * 262144u;
  const srd_t sK = MKSRD((BB ? p.kB : p.kA) + kvoff, KVREC), sV = MKSRD((BB ? p.vB : p.vA) + kvoff, KVREC);
  unsigned kvo[4];
#pragma unroll
  for (int i = 0; i < 4; ++i) kvo[i] = g4 * 256u + (((l15 ^ g4) ^ (4u * i)) * 16u);
  const unsigned vvo = g4 * 256u + ((l15 ^ (g4 << 2)) * 16u);
  const unsigned vv4[4] = {vvo, vvo, vvo, vvo};
  const unsigned dring = ring + dsub;
#define DG(m0v, vo, srd, soff, NT, cnt, g) do { if ((g) < 0) SETM0(m0v); else if ((g) < (cnt)) dma1<((g) & 3) * 1024, NT>(vo[(g) & 3], srd, soff); } while (0)
#define EG(n, j, g) do { if ((n) < 2 || ((n) & 1)) DG(dring + ((n) & 3) * ESZ + (j) * 4096u, kvo, sK, ((n) < 2 ? (n) : ((n) + 1) / 2) * ESZ + dsub + (j) * 4096u, true, 4, g); \
                         else DG(dring + ((n) & 3) * ESZ + (j) * 4096u, vv4, sV, ((n) - 2) / 2 * ESZ + dsub + (j) * 4096u, true, 4, g); } while (0)
#define EG4(n, j) do { EG(n, j, -1); asm volatile("s_nop 0"); EG(n, j, 0); EG(n, j, 1); EG(n, j, 2); EG(n, j, 3); } while (0)
#define ELEM(n) do { EG4(n, 0); if (BB) EG4(n, 1); } while (0)

  const unsigned gbuf = ldsb + 4u * ESZ + wid * 24576u;
#define GT(i) (((i) + idx) & 15u)
#define GD(i, hi, g) DG(gbuf + ((i) % 3) * 8192u + (hi) * 4096u, gvo, gsr, (GT(i) * 2u + (hi)) * GPITCH16, false, 4, g)
#define GD8(i) do { GD(i, 0, -1); asm volatile("s_nop 0"); GD(i, 0, 0); GD(i, 0, 1); GD(i, 0, 2); GD(i, 0, 3); GD(i, 1, -1); asm volatile("s_nop 0"); GD(i, 1, 0); GD(i, 1, 1); GD(i, 1, 2); GD(i, 1, 3); } while (0)
#define GW(i, x) gw[(i) & 1][x] = cat8(lds_tr((ga ^ (64u * ((x) & 3))) + ((i) % 3) * 8192u + ((x) >> 2) * 4096u), lds_tr((ga ^ (64u * ((x) & 3))) + ((i) % 3) * 8192u + ((x) >> 2) * 4096u + GJ2))
#define GX(i, x) do { \
      if ((i) == 0 && (x) < 4) asm volatile("v_mfma_f32_32x32x16_bf16 %0, %1, %2, 0" : "=&v"(acc[(x) & 3]) : "v"(gw[(i) & 1][x]), "v"(gb[(i) & 1][(x) >> 2])); \
      else asm volatile("v_mfma_f32_32x32x16_bf16 %0, %1, %2, %0" : "+v"(acc[(x) & 3]) : "v"(gw[(i) & 1][x]), "v"(gb[(i) & 1][(x) >> 2])); \
      if ((i) < 15) { GW((i) + 1, x); if ((x) == 0) GB((i) + 1); } SBAR(); \
      if ((i) < 13) { GD((i) + 3, (x) >> 2, (x) & 3); if ((x) == 3) GD((i) + 3, 1, -1); } SBAR(); } while (0)
#define GTILE(i, XTRA) do { if ((i) < 13) GD((i) + 3, 0, -1); waitv<((i) < 14 ? 8 : 0) + (XTRA)>(); WAITL(); SBAR(); \
      GX(i, 0); GX(i, 1); GX(i, 2); GX(i, 3); GX(i, 4); GX(i, 5); GX(i, 6); GX(i, 7); } while (0)
#define GFIRST() do { GB(0); GW(0, 0); GW(0, 1); GW(0, 2); GW(0, 3); GW(0, 4); GW(0, 5); GW(0, 6); GW(0, 7); } while (0)

  bf16x8 qf[8];
  f32x16 o[4];
  unsigned tch = 0u;
#define TOUCH(srd, soff) asm volatile("buffer_load_dword %0, %1, %2, %3 offen" : "+v"(tch) : "v"(lane * 128u), "s"(srd), "s"(soff) : "memory")
#define QO(g) do { qf[2 * (g)] = __builtin_bit_cast(bf16x8, pack8(acc[g], 0)); qf[2 * (g) + 1] = __builtin_bit_cast(bf16x8, pack8(acc[g], 1)); asm volatile("" : "+a"(qf[2 * (g)]), "+a"(qf[2 * (g) + 1])); } while (0)
  const unsigned p4 = lane & 3u, q4 = (lane >> 2) & 3u;
  const unsigned wl = (8u * hh + q4) * 256u + (q4 << 6) + (g4 & 1u) * 32u + (((p4 & 1u) << 1) | (p4 >> 1)) * 8u;
  if constexpr (!BB) {
    constexpr unsigned GPITCH16 = 16u * 8192u, GJ2 = 1024u;
    const srd_t gsr = MKSRD(p.wqA + (h0 + wid) * 256u, 4 << 20), sx = MKSRD(p.xA + (size_t)(b * 64u + qb * 32u) * 1024u, 32768);
    unsigned gvo[4], xvo[4];
#pragma unroll
    for (int i = 0; i < 4; ++i) { gvo[i] = g4 * 8192u + ((l15 ^ (g4 << 2)) * 16u) + i * (32768u - 1024u); xvo[i] = g4 * 1024u + (((l15 ^ g4) ^ (4u * i)) * 16u) + i * 3072u; }
    dma4<false>(ldsb + 2u * ESZ + wid * 8192u, xvo, sx, wid * 256u); dma4<false>(ldsb + 2u * ESZ + wid * 8192u + 4096u, xvo, sx, wid * 256u + 16384u);
    GD8(0); GD8(1);
#pragma unroll
    for (int db = 0; db < 4; ++db) { o[db] = f32x16{}; asm volatile("" : "+a"(o[db])); }
    GD8(2);
#pragma unroll
    for (int i = 0; i < TCH_KV; ++i) TOUCH(item ? sV : sK, (4u * i + 2u * qb + (wid & 1u)) * 8192u);
    const unsigned ga = gbuf + wl, xb = ldsb + 2u * ESZ + l31 * 256u + ((hh ^ l15) * 16u);
    f32x16 acc[4]; bf16x8 gw[2][8], gb[2][2];
#define GB(i) do { const unsigned a_ = (xb ^ ((GT(i) & 3u) * 64u)) + (GT(i) >> 2) * 8192u; gb[(i) & 1][0] = lds_rd128(a_); gb[(i) & 1][1] = lds_rd128(a_ ^ 32u); } while (0)
    waitv<16 + TCH_KV>(); BARRIER();
    GFIRST();
    STAMP(1);
    GTILE(0, TCH_KV); GTILE(1, TCH_KV); GTILE(2, 0); GTILE(3, 0); GTILE(4, 0); GTILE(5, 0); GTILE(6, 0); GTILE(7, 0);
    GTILE(8, 0); GTILE(9, 0); GTILE(10, 0); GTILE(11, 0); GTILE(12, 0);
    ELEM(0); ELEM(1);
    GTILE(13, 8); GTILE(14, 8);
    BARRIER();
    ELEM(2);
    GTILE(15, 12);
#undef GB
    PIN(tch);
    STAMP(2);
    asm volatile("s_nop 7\n\ts_nop 7" : "+v"(acc[0]), "+v"(acc[1]), "+v"(acc[2]), "+v"(acc[3]));
    QO(0); QO(1); QO(2); QO(3); SBAR();
  } else {
    constexpr int QE = 10;
    const unsigned rot = b + 4u * (idx & 1u);
    const srd_t sw = MKSRD(p.wqB + h0 * 256u, 4 << 20), sx = MKSRD(p.xB + (size_t)b * 65536u, 65536);
    unsigned wvo[4], xvo[4];
#pragma unroll
    for (int i = 0; i < 4; ++i) {
      wvo[i] = g4 * 8192u + ((l15 ^ (g4 << 2)) * 16u) + i * (32768u - 1024u);
      xvo[i] = (lane >> 3) * 1024u + (((lane & 7u) ^ ((4u * i + g4) & 7u)) * 16u) + i * (8192u - 1024u);
    }
#define WOFF(s) (((s) & 3) * ESZ)
#define XOFF(s) ((((s) >> 1) & 1) * HREG + 4u * ESZ + ((s) & 1) * 8192u)
    const unsigned wdst = ldsb + wid * 4096u, xdst = ldsb + wid * 2048u;
#define QG(s, j, g) do { const unsigned cv_ = ((s) + rot) & 7u; \
      if ((j) == 0) DG(wdst + WOFF(s), wvo, sw, (cv_ * 64u + wid * 16u) * 8192u, false, 4, g); \
      if ((j) == 1) DG(wdst + WOFF(s) + HREG, wvo, sw, (cv_ * 64u + wid * 16u) * 8192u + 256u, false, 4, g); \
      if ((j) == 2) DG(xdst + XOFF(s), xvo, sx, wid * 16384u + cv_ * 128u, false, 2, g); } while (0)
#define QG4(s, j) do { QG(s, j, -1); asm volatile("s_nop 0"); QG(s, j, 0); QG(s, j, 1); QG(s, j, 2); QG(s, j, 3); } while (0)
#define QLD(s) do { QG4(s, 0); QG4(s, 1); QG4(s, 2); } while (0)
    const unsigned wa = hreg + wl;
    const unsigned xa = ldsb + qb * 4096u + l31 * 128u + ((hh ^ ((l31 >> 1) & 7u)) * 16u);
    f32x16 acc[4];
    bf16x8 xf[2][4], wf[2][4][4];
#define QM1(s, ks, g) do { if ((s) == 0 && (ks) == 0) asm volatile("v_mfma_f32_32x32x16_bf16 %0, %1, %2, 0" : "=&v"(acc[g]) : "v"(wf[(s) & 1][g][ks]), "v"(xf[(s) & 1][ks])); \
      else asm volatile("v_mfma_f32_32x32x16_bf16 %0, %1, %2, %0" : "+v"(acc[g]) : "v"(wf[(s) & 1][g][ks]), "v"(xf[(s) & 1][ks])); } while (0)
#define QOP(HOOK, NOP, G, v) do { if ((v) < (NOP) && ((v) * 16) / (NOP) == (G)) { HOOK((v) >> 2, (v) & 3); if (((v) & 3) == 3 && (v) + 1 < (NOP)) { HOOK(((v) >> 2) + 1, -1); } } } while (0)
#define QGAP(s, ks, g, HOOK, NOP) do { if ((s) > 0) QM1((s) - 1, ks, g); else asm volatile("s_nop 0"); \
        if ((g) == 0) xf[(s) & 1][ks] = lds_rd128((xa ^ (32u * (ks))) + XOFF(s)); \
        wf[(s) & 1][g][ks] = cat8(lds_tr((wa ^ (64u * (g))) + WOFF(s) + (ks) * 4096u), lds_tr((wa ^ (64u * (g))) + WOFF(s) + (ks) * 4096u + 1024u)); \
        SBAR(); \
        QOP(HOOK, NOP, 4 * (ks) + (g), 0); QOP(HOOK, NOP, 4 * (ks) + (g), 1); QOP(HOOK, NOP, 4 * (ks) + (g), 2); QOP(HOOK, NOP, 4 * (ks) + (g), 3); \
        QOP(HOOK, NOP, 4 * (ks) + (g), 4); QOP(HOOK, NOP, 4 * (ks) + (g), 5); QOP(HOOK, NOP, 4 * (ks) + (g), 6); QOP(HOOK, NOP, 4 * (ks) + (g), 7); \
        QOP(HOOK, NOP, 4 * (ks) + (g), 8); QOP(HOOK, NOP, 4 * (ks) + (g), 9); \
        SBAR(); } while (0)
#define QKS(s, ks, HOOK, NOP) do { QGAP(s, ks, 0, HOOK, NOP); QGAP(s, ks, 1, HOOK, NOP); QGAP(s, ks, 2, HOOK, NOP); QGAP(s, ks, 3, HOOK, NOP); } while (0)
#define QCMP(s, HOOK, NOP) do { HOOK(0, -1); SBAR(); QKS(s, 0, HOOK, NOP); QKS(s, 1, HOOK, NOP); QKS(s, 2, HOOK, NOP); QKS(s, 3, HOOK, NOP); } while (0)
#define HQ0(j, g) QG(3, j, g)
#define HQ1(j, g) QG(4, j, g)
#define HQ2(j, g) QG(5, j, g)
#define HQ3(j, g) QG(6, j, g)
#define HQ4(j, g) QG(7, j, g)
#define HQ5(j, g) EG(0, j, g)
#define HQ6(j, g) EG(1, j, g)
#define HQ7(j, g) EG(2, j, g)
    QLD(0); QLD(1);
#pragma unroll
    for (int db = 0; db < 4; ++db) { o[db] = f32x16{}; asm volatile("" : "+a"(o[db])); }
    QLD(2);
    waitv<2 * QE>(); BARRIER(); QCMP(0, HQ0, QE);
    STAMP(1);
    waitv<2 * QE>(); BARRIER(); QCMP(1, HQ1, QE);
    waitv<2 * QE>(); BARRIER(); QCMP(2, HQ2, QE);
    waitv<2 * QE>(); BARRIER(); QCMP(3, HQ3, QE);
    waitv<2 * QE>(); BARRIER(); QCMP(4, HQ4, QE);
    waitv<2 * QE>(); BARRIER(); QCMP(5, HQ5, PE);
    waitv<QE + PE>(); BARRIER(); QCMP(6, HQ6, PE);
    waitv<2 * PE>(); BARRIER(); QCMP(7, HQ7, PE);
    STAMP(2);
    QM1(7, 0, 0); QM1(7, 1, 0); QM1(7, 2, 0); QM1(7, 3, 0); SBAR();
    QM1(7, 0, 1); QM1(7, 1, 1); QM1(7, 2, 1); QM1(7, 3, 1); asm volatile("s_nop 7" : "+v"(acc[0])); SBAR();
    QM1(7, 0, 2); QM1(7, 1, 2); QO(0); SBAR(); QM1(7, 2, 2); QM1(7, 3, 2); PIN(acc[1]); SBAR();
    QM1(7, 0, 3); QM1(7, 1, 3); QO(1); SBAR(); QM1(7, 2, 3); QM1(7, 3, 3); PIN(acc[2]); SBAR();
    QO(2); SBAR(); asm volatile("s_nop 3" : "+v"(acc[3])); QO(3); SBAR();
  }
  PIN(epoch_v);
  const u64 tag = mix64(((u64)rfl((unsigned)(epoch_v >> 32)) << 32) | rfl((unsigned)epoch_v));

  unsigned tA = 3u * ESZ + dsub, tB = 2u * ESZ + dsub;
  const unsigned kb = ring + l31 * 256u + ((hh ^ l15) * 16u);
  const unsigned vl = (4u * hh + ((lane >> 2) & 3u)) * 256u + (((lane >> 2) & 3u) << 6) + (g4 & 1u) * 32u + (lane & 3u) * 8u;
  const unsigned vb = ring + vl;
  f32x16 sA[2], sB[2], negm;
  bf16x8 kf[2][8]; s16x4 vlo[4][4], vhi[4][4]; u32x4 pw[2][2];
  float fsc = 1.f, ls0 = 0.f, lt0 = 0.f, ls1 = 0.f, lt1 = 0.f;
#define QK0(D, a, bq) asm volatile("v_mfma_f32_32x32x16_bf16 %0, %1, %2, 0" : "=&v"(D) : "v"(a), "a"(bq))
#define QKC(D, a, bq, C) asm volatile("v_mfma_f32_32x32x16_bf16 %0, %1, %2, %3" : "=&v"(D) : "v"(a), "a"(bq), "v"(C))
#define QKA(D, a, bq) asm volatile("v_mfma_f32_32x32x16_bf16 %0, %1, %2, %0" : "+v"(D) : "v"(a), "a"(bq))
#define WAITE(k) do { waitv<PE * (k)>(); BARRIER(); } while (0)
#define EXP2(x) __builtin_amdgcn_exp2f((x) * C2)
#define KRD(s) do { const unsigned a_ = ka ^ (32u * (s)); kf[0][s] = lds_rd128(a_); kf[1][s] = lds_rd128(a_ + 8192u); } while (0)

  STAMP(3);
  WAITE(2);
  {
    const unsigned ka = kb;
#pragma unroll
    for (int s = 0; s < 8; ++s) KRD(s);
  }
  QK0(sA[0], kf[0][0], qf[0]); QK0(sA[1], kf[1][0], qf[0]);
#pragma unroll
  for (int s = 1; s < 8; ++s) { QKA(sA[0], kf[0][s], qf[s]); QKA(sA[1], kf[1][s], qf[s]); if (s == 2) ELEM(3); }
  WAITE(2);
  ELEM(4);
  {
    const unsigned ka = kb + ESZ;
#pragma unroll
    for (int s = 0; s < 8; ++s) KRD(s);
  }
  asm volatile("s_nop 7\n\ts_nop 7" : "+v"(sA[0]), "+v"(sA[1]));
  {
    const float m0 = swap_max(max2f(max8hi(max8lo(sA[0]), sA[0]), max8hi(max8lo(sA[1]), sA[1])));
#pragma unroll
    for (int r = 0; r < 16; ++r) { sA[0][r] -= m0; sA[1][r] -= m0; negm[r] = -m0; }
    PIN(negm);
#pragma unroll
    for (int r = 0; r < 8; ++r) { sA[0][r] = EXP2(sA[0][r]); sA[1][r] = EXP2(sA[1][r]); }
#pragma unroll
    for (int r = 0; r < 8; r += 2) { ls0 += sA[0][r]; lt0 += sA[0][r + 1]; ls1 += sA[1][r]; lt1 += sA[1][r + 1]; }
  }
  unsigned wvo[4];
  const srd_t sW = MKSRD((BB ? p.woB : p.woA) + (size_t)h0 * 131072u + wid * 256u, (BB ? 262144 : 524288) - 768);
  const unsigned wbuf = BB ? hreg + qb * 8192u : ldsb + ESZ + wid * 8192u;
  const unsigned bhi = b >> 2;
#define WSO(i) ((((i) + b) & 3u) * 65536u + ((((i) >> 2) & 1u) ^ bhi) * 128u)
#define WG(i, hi, g) DG(wbuf + (1u + ((i) & 3)) * ESZ + (hi) * 4096u, wvo, sW, WSO(i) + (hi) * 32768u, NT_WO, 4, g)
#define WOP(v, TA, TB_) do { if ((v) < 8) WG(TA, ((v) >> 2) & 1, (v) & 3); else WG(TB_, ((v) >> 2) & 1, (v) & 3); \
    if ((v) == 3) WG(TA, 1, -1); if ((v) == 7 && (TB_) >= 0) WG((TB_) & 7, 0, -1); if ((v) == 11) WG((TB_) & 7, 1, -1); } while (0)

#define VRD(db, k4) do { const unsigned a_ = (vb ^ (64u * (db))) + S2 + 4096u * (k4); vlo[db][k4] = lds_tr(a_); vhi[db][k4] = lds_tr(a_ + 2048u); } while (0)
#define GAPA(C, N, s, KB, LAST) do { \
    if ((s) == 0) QKC(N[KB], kf[KB][0], qf[0], negm); else QKA(N[KB], kf[KB][s], qf[s]); \
    if ((s) < 4) { float e0_ = EXP2(C[KB][(8 + 2 * (s)) & 15]), e1_ = EXP2(C[KB][(9 + 2 * (s)) & 15]); PIN(e0_); PIN(e1_); C[KB][(8 + 2 * (s)) & 15] = e0_; C[KB][(9 + 2 * (s)) & 15] = e1_; \
      pw[KB][0][(s) & 3] = cvtpk(C[KB][2 * (s)], C[KB][2 * (s) + 1]); PIN(pw[KB][0]); } \
    else { ls##KB += C[KB][2 * (s)]; lt##KB += C[KB][2 * (s) + 1]; pw[KB][1][(s) & 3] = cvtpk(C[KB][2 * (s)], C[KB][2 * (s) + 1]); PIN(pw[KB][1]); PIN(ls##KB); PIN(lt##KB); } \
    if ((s) < 4) VRD((2 * (s) + KB) & 3, VK4(((s) >> 1) & 1)); else if ((s) < 6) { VRD(2 * KB, VK4(((s) & 1) + 2)); VRD(2 * KB + 1, VK4(((s) & 1) + 2)); } \
    SBAR(); \
    if (LAST) { if (BB) WOP(2 * (s) + KB, 0, 2); } \
    else if (KB == 1 && (BB || (s) < 4)) { dma1<((s) & 3) * 1024, true>(kvo[(s) & 3], sK, tA + ((s) >> 2) * 4096u); if (BB && (s) == 3) SETM0(dring + S1 + 4096u); } \
    SBAR(); } while (0)
#define VK4(i) ((i) == 0 ? 0 : (i) == 1 ? 2 : (i) == 2 ? 1 : 3)
#define PHASE_A(C, N, ODD, L) do { \
    constexpr unsigned S1 = (ODD ? 3u : 1u) * ESZ, S2 = (ODD ? 0u : 2u) * ESZ; \
    if (!BB && ASLP > 0) __builtin_amdgcn_s_sleep(ASLP); \
    if (L) { if (BB) WG(0, 0, -1); } else SETM0(dring + S1); \
    WAITE(L ? 1 : 2); SBAR(); \
    GAPA(C, N, 0, 0, L); GAPA(C, N, 0, 1, L); GAPA(C, N, 1, 0, L); GAPA(C, N, 1, 1, L); GAPA(C, N, 2, 0, L); GAPA(C, N, 2, 1, L); GAPA(C, N, 3, 0, L); GAPA(C, N, 3, 1, L); \
    GAPA(C, N, 4, 0, L); GAPA(C, N, 4, 1, L); GAPA(C, N, 5, 0, L); GAPA(C, N, 5, 1, L); GAPA(C, N, 6, 0, L); GAPA(C, N, 6, 1, L); GAPA(C, N, 7, 0, L); GAPA(C, N, 7, 1, L); \
    tA += ESZ; } while (0)
#define PVM(i) o[(i) & 3] = MFMA32(cat8(vlo[(i) & 3][VK4((i) >> 2)], vhi[(i) & 3][VK4((i) >> 2)]), __builtin_bit_cast(bf16x8, pw[((i) >> 2) & 1][(i) >> 3]), o[(i) & 3])
#define DMAB(v, LAST) do { SBAR(); if (LAST) { if (BB) WOP(v, 1, 3); } \
    else if (((v) & 1) == 0 && (BB || (v) < 8)) { dma1<(((v) >> 1) & 3) * 1024, true>(vvo, sV, tB + ((v) >> 3) * 4096u); if (BB && (v) == 6) SETM0(dring + S2 + 4096u); } SBAR(); } while (0)
#define GAPE(N, j, LAST) do { PVM(8 + (j)); N[0][j] = EXP2(N[0][j]); N[1][j] = EXP2(N[1][j]); \
    if ((j) > 0) { if ((j) & 1) { ls0 += N[0][((j) + 15) & 15]; ls1 += N[1][((j) + 15) & 15]; } else { lt0 += N[0][((j) + 15) & 15]; lt1 += N[1][((j) + 15) & 15]; } } \
    PIN(N[0]); PIN(N[1]); PIN(ls0); PIN(ls1); PIN(lt0); PIN(lt1); DMAB(8 + (j), LAST); } while (0)
#define PHASE_B(N, ODD, LAST) do { \
    constexpr unsigned S2 = (ODD ? 0u : 2u) * ESZ, S3 = (ODD ? 1u : 3u) * ESZ; \
    if (LAST) { if (BB) WG(1, 0, -1); BARRIER(); asm volatile("s_nop 7"); } else { SETM0(dring + S2); WAITE(2); } SBAR(); \
    const unsigned ka = kb + S3; \
    PVM(0); KRD(0); DMAB(0, LAST); \
    PVM(1); KRD(1); DMAB(1, LAST); \
    PVM(2); KRD(2); float ma0 = max3f(N[0][0], N[0][1], N[0][2]), ma1 = max3f(N[1][0], N[1][1], N[1][2]), mb0 = max3f(N[0][3], N[0][4], N[0][5]), mb1 = max3f(N[1][3], N[1][4], N[1][5]); DMAB(2, LAST); \
    PVM(3); KRD(3); ma0 = max3f(ma0, N[0][6], N[0][7]); ma1 = max3f(ma1, N[1][6], N[1][7]); mb0 = max3f(mb0, N[0][8], N[0][9]); mb1 = max3f(mb1, N[1][8], N[1][9]); DMAB(3, LAST); \
    PVM(4); KRD(4); ma0 = max3f(ma0, N[0][10], N[0][11]); ma1 = max3f(ma1, N[1][10], N[1][11]); mb0 = max3f(mb0, N[0][12], N[0][13]); mb1 = max3f(mb1, N[1][12], N[1][13]); DMAB(4, LAST); \
    PVM(5); ma0 = max3f(ma0, N[0][14], N[0][15]); ma1 = max3f(ma1, N[1][14], N[1][15]); const float mbb = max2f(mb0, mb1); KRD(5); DMAB(5, LAST); \
    PVM(6); const float mall = max3f(ma0, ma1, mbb); KRD(6); const bool grew = __any(mall > THR); DMAB(6, LAST); \
    PVM(7); KRD(7); DMAB(7, LAST); \
    if (__builtin_expect(grew, 0)) {     \
      const float d0 = max2f(swap_max(mall), 0.f); \
      fsc = EXP2(-d0); ls0 *= fsc; lt0 *= fsc; ls1 *= fsc; lt1 *= fsc; \
      sub_tile(N[0], d0); sub_tile(N[1], d0); sub_tile(negm, d0); \
    } \
    SBAR(); \
    GAPE(N, 0, LAST); GAPE(N, 1, LAST); GAPE(N, 2, LAST); GAPE(N, 3, LAST); GAPE(N, 4, LAST); GAPE(N, 5, LAST); GAPE(N, 6, LAST); GAPE(N, 7, LAST); \
    lt0 += N[0][7]; lt1 += N[1][7]; \
    if (__builtin_expect(grew, 0)) { \
      asm volatile("s_nop 7\n\ts_nop 7" ::: "memory"); \
      scale_acc(o[0], fsc); scale_acc(o[1], fsc); scale_acc(o[2], fsc); scale_acc(o[3], fsc); \
    } \
    tB += ESZ; } while (0)

  if (tid == 0u) __hip_atomic_store((GAS u64*)(p.ws + WS_FLAG3) + (size_t)blockIdx.x * 8u, tag, RLX_AGENT);
  STAMP(4);
  for (unsigned t = 0; t < 14u; t += 2u) {
    if (t == 4u) { STAMP(12); } if (t == 8u) { STAMP(13); } if (t == 12u) { STAMP(14); }
    PHASE_A(sA, sB, false, false); PHASE_B(sB, false, false);
    PHASE_A(sB, sA, true, false);  PHASE_B(sA, true, false);
  }
#pragma unroll
  for (int j = 0; j < 4; ++j) wvo[j] = BB ? (lane >> 3) * 1024u + (((lane & 7u) ^ ((g4 & 1u) << 2)) * 16u) + j * 7168u : g4 * 1024u + ((l15 ^ (g4 << 2)) * 16u) + j * 3072u;
  constexpr unsigned GPITCH16 = 16u * 1024u, GJ2 = 2048u;
  const srd_t gsr = sW; const unsigned (&gvo)[4] = wvo;
  PHASE_A(sA, sB, false, true);
  if (!BB) { GD8(0); GD8(1); GD8(2); }
  PHASE_B(sB, false, true);
  STAMP(5);
  bf16x8 wfr[2][8];
  const unsigned wb = wbuf + (4u * hh + q4) * 128u + (q4 >> 1) * 64u + (g4 & 1u) * 32u + p4 * 8u;
#define WRD(i, x) do { const unsigned a_ = (wb ^ (64u * ((x) & 1))) + (1u + ((i) & 3)) * ESZ + 2048u * ((x) >> 1); wfr[(i) & 1][x] = cat8(lds_tr(a_), lds_tr(a_ + 1024u)); } while (0)
  {
    constexpr unsigned S2 = 0u;
#pragma unroll
    for (int r = 8; r < 16; ++r) { sB[0][r] = EXP2(sB[0][r]); sB[1][r] = EXP2(sB[1][r]); ls0 += sB[0][r]; ls1 += sB[1][r]; }
#pragma unroll
    for (int i = 0; i < 8; ++i) { pw[0][i >> 2][i & 3] = cvtpk(sB[0][2 * i], sB[0][2 * i + 1]); pw[1][i >> 2][i & 3] = cvtpk(sB[1][2 * i], sB[1][2 * i + 1]); }
    PIN(pw[0][0]); PIN(pw[0][1]); PIN(pw[1][0]); PIN(pw[1][1]);
    float inv = __builtin_amdgcn_rcpf(swap_add((ls0 + lt0) + (ls1 + lt1))); PIN(inv);
    SBAR();
    waitv<BB ? 32 : 24>(); BARRIER();
#pragma unroll
    for (int i = 0; i < 16; ++i) VRD(i >> 2, VK4(i & 3));
#define PINA(x) asm volatile("" : "+a"(x))
#define PV4(db) do { PVM(db); PVM(4 + (db)); PVM(8 + (db)); PVM(12 + (db)); PINA(o[db]); } while (0)
#define OWR(db) do { const f32x16 y_ = o[db] * inv; lds_wru4(wbuf + (2 * (db)) * 1024u + lane * 16u, pack8(y_, 0)); lds_wru4(wbuf + (2 * (db) + 1) * 1024u + lane * 16u, pack8(y_, 1)); } while (0)
    PV4(0); SBAR(); if (BB) { BARRIER(); } PV4(1); SBAR();
    PV4(2); OWR(0); SBAR(); PV4(3); OWR(1); SBAR();
    if (BB) {
      waitv<24>();
#pragma unroll
      for (int i = 0; i < 8; ++i) WRD(0, i);
    }
    OWR(2); OWR(3);
    STAMP(6);
    BARRIER();
  }
  STAMP(7);
  constexpr unsigned br = BB ? 1u : 0u;
  const srd_t sP = MKSRD(p.ws + WS_PART, 16 << 20);
  GAS u64* flags = (GAS u64*)(p.ws + WS_FLAG);
  const unsigned hp = BB ? (h0 >> 1) : g8;
  const unsigned fgrp = BB ? wid * 16u : (qb * 4u + wid) * 8u;
  if constexpr (!BB) {
    f32x16 acc[4]; bf16x8 gw[2][8], gb[2][2];
    const unsigned ga = gbuf + vl;
#define GB(i) do { const unsigned a_ = ldsb + ESZ + GT(i) * 2048u + lane * 16u; gb[(i) & 1][0] = lds_rd128(a_); gb[(i) & 1][1] = lds_rd128(a_ + 1024u); } while (0)
    waitv<16>();
    GFIRST();
    GTILE(0, 0); GTILE(1, 0); GTILE(2, 0); GTILE(3, 0); GTILE(4, 0); GTILE(5, 0); GTILE(6, 0); GTILE(7, 0);
    GTILE(8, 0); GTILE(9, 0); GTILE(10, 0); GTILE(11, 0); GTILE(12, 0); GTILE(13, 0); GTILE(14, 0); GTILE(15, 0);
    STAMP(9);
    asm volatile("s_nop 7\n\ts_nop 7" : "+v"(acc[0]), "+v"(acc[1]), "+v"(acc[2]), "+v"(acc[3]));
    const unsigned pb = b * 1048576u + (qb * 32u + 8u * wid) * 16384u + hp * 1024u + lane * 16u;
#pragma unroll
    for (int i = 0; i < 8; ++i) __builtin_amdgcn_raw_buffer_store_b128(pack8(acc[i >> 1], i & 1), sP, pb + i * 16384u, 0, 16);
  } else {
    bf16x8 of[4][2][4];
#define ORD(i, x) do { const unsigned r_ = ((i) + b) & 3u; of[i][(x) & 1][(x) >> 1] = lds_rd128(ldsb + (r_ >> 1) * HREG + (r_ & 1u) * 4096u + lane * 16u + ((x) & 1) * 8192u + ((x) >> 1) * 1024u); } while (0)
    ORD(0, 0); ORD(0, 1); ORD(0, 2); ORD(0, 3); ORD(0, 4); ORD(0, 5); ORD(0, 6); ORD(0, 7);
    f32x16 acc[4][2];
    const f32x16 z16 = {};
    const unsigned pb = (br * 8u + b) * 1048576u + 8u * wid * 16384u + hp * 1024u + lane * 16u;
    const unsigned pb0 = pb + bhi * 65536u, pb1 = pb + (bhi ^ 1u) * 65536u;
#define PST(nb, q, s2) __builtin_amdgcn_raw_buffer_store_b128(pack8(acc[nb][q], s2), sP, (((nb) >> 1) ? pb1 : pb0) + ((q) * 32 + ((nb) & 1) * 2 + (s2)) * 16384u, 0, 16)
#define OM(i, x, q) do { acc[2 * ((i) >> 2) + ((x) & 1)][q] = MFMA32(wfr[(i) & 1][x], of[(i) & 3][q][(x) >> 1], (((i) & 3) == 0 && (x) < 2) ? z16 : acc[2 * ((i) >> 2) + ((x) & 1)][q]); \
      PINA(acc[2 * ((i) >> 2) + ((x) & 1)][q]); } while (0)
#define OX(i, x) do { \
      OM(i, x, 0); WRD((i) + 1, x); if ((i) < 3) ORD(((i) + 1) & 3, x); SBAR(); \
      if ((i) < 4) WOP(x, ((i) + 4) & 7, -1); \
      SBAR(); OM(i, x, 1); if ((i) >= 4 && ((x) & 1)) PST((i) & 1, ((x) >> 2) & 1, ((x) >> 1) & 1); SBAR(); } while (0)
#define OT(i, N) do { if ((i) < 4) WG(((i) + 4) & 7, 0, -1); waitv<N>(); if ((i) > 0) { WAITL(); } SBAR(); \
      OX(i, 0); OX(i, 1); OX(i, 2); OX(i, 3); OX(i, 4); OX(i, 5); OX(i, 6); OX(i, 7); } while (0)
    OT(0, 8); OT(1, 16); OT(2, 16); OT(3, 16); OT(4, 16); OT(5, 12);
    STAMP(9);
    waitv<8>(); SBAR();
#pragma unroll
    for (int i = 0; i < 8; ++i) WRD(7, (2 * i + (i >> 2)) & 7);
    SBAR();
#define OF(n, q, pn, pq) do { \
      OM(6, n, q); OM(6, 2 + (n), q); OM(6, 4 + (n), q); SBAR(); OM(6, 6 + (n), q); if ((pn) >= 0) PST(2 + ((pn) & 1), pq, 0); SBAR(); \
      OM(7, n, q); OM(7, 2 + (n), q); SBAR(); OM(7, 4 + (n), q); if ((pn) >= 0) PST(2 + ((pn) & 1), pq, 1); SBAR(); OM(7, 6 + (n), q); SBAR(); } while (0)
    OF(0, 0, -1, 0); OF(0, 1, 0, 0); OF(1, 0, 0, 1); OF(1, 1, 1, 0);
    PST(3, 1, 0); PST(3, 1, 1);
  }
  waitv<0>();
  if (lane == 0) __hip_atomic_store(flags + (size_t)((br * 8u + b) * 64u + fgrp + hp) * 8u, tag, RLX_AGENT);
  STAMP(10);
  {
    constexpr int NP = BB ? 16 : 8;
    const unsigned u = BB ? hp * 4u + wid : qb * 32u + hp * 4u + wid;
    poll_flags<2>(flags + (size_t)((br * 8u + b) * 64u + (BB ? ((u >> 3) & 3u) * 16u + l15 : (u & 56u) + (lane & 7u))) * 8u, tag);
    STAMP(11);
    const unsigned rb = (br * 8u + b) * 1048576u + u * 16384u + lane * 16u;
    u32x4 pv[NP];
#pragma unroll
    for (int j = 0; j < NP; ++j) pv[j] = __builtin_amdgcn_raw_buffer_load_b128(sP, rb + j * 1024u, 0, 16);
    float y[8] = {0.f, 0.f, 0.f, 0.f, 0.f, 0.f, 0.f, 0.f};
#pragma unroll
    for (int j = 0; j < NP; ++j)
#pragma unroll
      for (int e = 0; e < 4; ++e) { y[2 * e] += __uint_as_float(pv[j][e] << 16); y[2 * e + 1] += __uint_as_float(pv[j][e] & 0xffff0000u); }
    const auto r0 = __builtin_amdgcn_permlane32_swap(cvtpk(y[0], y[1]), cvtpk(y[4], y[5]), false, false);
    const auto r1 = __builtin_amdgcn_permlane32_swap(cvtpk(y[2], y[3]), cvtpk(y[6], y[7]), false, false);
    *(u32x4*)(p.out + (size_t)(b * 64u + (u >> 5) * 32u + l31) * 2048u + (br * 512u + (u & 31u) * 16u + 8u * hh) * 2u) = u32x4{r0[0], r1[0], r0[1], r1[1]};
    if (!BB && blockIdx.x == 128u && wid == 0u) {
#pragma unroll 1
      for (unsigned spins = 0; spins < (1u << 21); ++spins) {
        bool ok = true;
#pragma unroll
        for (int k = 0; k < 4; ++k) ok &= (__hip_atomic_load((GAS u64*)(p.ws + WS_FLAG3) + (size_t)(k * 64 + lane) * 8u, RLX_AGENT) == tag);
        if (__all(ok)) break;
        __builtin_amdgcn_s_sleep(8);
      }
      if (lane == 0) __hip_atomic_store((GAS u64*)(p.ws + WS_EPOCH), tag, RLX_AGENT);
    }
  }
  STAMP(15);
  STAMP_OUT(p.ws, blockIdx.x * 4u + wid, BB ? 0 : 16, 16);
}

__global__ __launch_bounds__(256, 1) void k1_attn(P1 p) {
  extern __shared__ __attribute__((aligned(16))) char lds[];
  const unsigned ldsb = (unsigned)(unsigned long)lds;
  if (blockIdx.x < 128u) body<true>(p, ldsb); else body<false>(p, ldsb);
}

extern "C" void kernel_launch(void* const* d_in, const int* in_sizes, int n_in,
                              void* d_out, int out_size, void* d_ws, size_t ws_size,
                              hipStream_t stream) {
  (void)in_sizes; (void)n_in; (void)out_size; (void)ws_size;
  (void)hipFuncSetAttribute((const void*)k1_attn, hipFuncAttributeMaxDynamicSharedMemorySize, K1_LDS);
  P1 p1;
  p1.xA = (const char*)d_in[0]; p1.kA = (const char*)d_in[1]; p1.vA = (const char*)d_in[2]; p1.wqA = (const char*)d_in[3]; p1.woA = (const char*)d_in[4];
  p1.xB = (const char*)d_in[5]; p1.kB = (const char*)d_in[6]; p1.vB = (const char*)d_in[7]; p1.wqB = (const char*)d_in[8]; p1.woB = (const char*)d_in[9];
  p1.ws = (char*)d_ws; p1.out = (char*)d_out;
  hipLaunchKernelGGL(k1_attn, dim3(256), dim3(256), K1_LDS, stream, p1);
}
```

```cpp
#include <hip/hip_runtime.h>
#include <hip/hip_bf16.h>

typedef float    f32x16 __attribute__((ext_vector_type(16)));
typedef float    f32x2  __attribute__((ext_vector_type(2)));
typedef __bf16   bf16x8 __attribute__((ext_vector_type(8)));
typedef __bf16   bf16x2 __attribute__((ext_vector_type(2)));
typedef short    s16x4  __attribute__((ext_vector_type(4)));
typedef unsigned u32x4  __attribute__((ext_vector_type(4)));
typedef unsigned long long u64;
typedef __amdgpu_buffer_rsrc_t srd_t;
#define DI __device__ __forceinline__
#define LAS __attribute__((address_space(3)))
#define GAS __attribute__((address_space(1)))
#define SBAR() __builtin_amdgcn_sched_barrier(0)
#define PIN(x) asm volatile("" : "+v"(x))
#define MFMA32(a, b, c) __builtin_amdgcn_mfma_f32_32x32x16_bf16(a, b, c, 0, 0, 0)
#define RLX_AGENT __ATOMIC_RELAXED, __HIP_MEMORY_SCOPE_AGENT
#define MKSRD(ptr, n) __builtin_amdgcn_make_buffer_rsrc((void*)(ptr), (short)0, (n), 0x00020000)

constexpr int K1_LDS = 163840;
constexpr float C2 = 0.08838834764831845f * 1.4426950408889634f;
constexpr float THR = 8.0f / C2;
constexpr size_t WS_PART = 0;
constexpr size_t WS_FLAG = 16u << 20;
constexpr size_t WS_FLAG3 = WS_FLAG + 65536;
constexpr size_t WS_EPOCH = WS_FLAG3 + 16384;

DI unsigned rfl(unsigned x) { return __builtin_amdgcn_readfirstlane(x); }
DI unsigned cvtpk(float lo, float hi) { f32x2 v = {lo, hi}; bf16x2 b = __builtin_convertvector(v, bf16x2); return __builtin_bit_cast(unsigned, b); }
DI bf16x8 lds_rd128(unsigned a) { return *(const LAS bf16x8*)(unsigned long)a; }
DI void lds_wru4(unsigned a, u32x4 v) { *(LAS u32x4*)(unsigned long)a = v; }
DI s16x4 lds_tr(unsigned a) { return __builtin_amdgcn_ds_read_tr16_b64_v4i16((LAS s16x4*)(unsigned long)a); }
DI bf16x8 cat8(s16x4 lo, s16x4 hi) { return __builtin_bit_cast(bf16x8, __builtin_shufflevector(lo, hi, 0, 1, 2, 3, 4, 5, 6, 7)); }
DI float max3f(float a, float b, float c) { float r; asm("v_max3_f32 %0, %1, %2, %3" : "=v"(r) : "v"(a), "v"(b), "v"(c)); return r; }
DI float max2f(float a, float b) { float r; asm("v_max_f32_e32 %0, %1, %2" : "=v"(r) : "v"(a), "v"(b)); return r; }
DI float swap_max(float m) { auto rr = __builtin_amdgcn_permlane32_swap(__float_as_uint(m), __float_as_uint(m), false, false); return max2f(__uint_as_float(rr[0]), __uint_as_float(rr[1])); }
DI float swap_add(float m) { auto rr = __builtin_amdgcn_permlane32_swap(__float_as_uint(m), __float_as_uint(m), false, false); return __uint_as_float(rr[0]) + __uint_as_float(rr[1]); }
DI float max8lo(const f32x16& p) { float a = max3f(p[0], p[1], p[2]), b = max3f(p[3], p[4], p[5]); return max3f(max3f(a, p[6], p[7]), b, b); }
DI float max8hi(float m, const f32x16& p) { float a = max3f(m, p[8], p[9]), b = max3f(p[10], p[11], p[12]); a = max3f(a, p[13], p[14]); return max3f(a, b, p[15]); }
DI void scale_acc(f32x16& x, float f) {
#pragma unroll
  for (int r = 0; r < 16; ++r) { float e = x[r], t;
    asm volatile("v_accvgpr_read_b32 %1, %0\n\tv_mul_f32 %1, %1, %2\n\tv_accvgpr_write_b32 %0, %1" : "+a"(e), "=&v"(t) : "v"(f));
    x[r] = e; }
}
DI void sub_tile(f32x16& x, float d) {
#pragma unroll
  for (int r = 0; r < 16; ++r) { float e = x[r]; asm volatile("v_sub_f32_e32 %0, %0, %1" : "+v"(e) : "v"(d)); x[r] = e; }
}
DI u64 mix64(u64 z) { z += 0x9E3779B97F4A7C15ull; z = (z ^ (z >> 30)) * 0xBF58476D1CE4E5B9ull; z = (z ^ (z >> 27)) * 0x94D049BB133111EBull; return z ^ (z >> 31); }


#define SETM0(x) asm volatile("s_mov_b32 m0, %0" :: "s"(x) : "memory")
#define SETM0N(x) asm volatile("s_mov_b32 m0, %0\n\ts_nop 0" :: "s"(x) : "memory")
template <int OFF, bool NT> DI void dma2(unsigned vo0, unsigned vo1, srd_t srd, unsigned soff) {
  if constexpr (NT)
    asm volatile("buffer_load_dwordx4 %0, %2, %3 offen offset:%4 nt lds\n\tbuffer_load_dwordx4 %1, %2, %3 offen offset:%5 nt lds"
                 :: "v"(vo0), "v"(vo1), "s"(srd), "s"(soff), "i"(OFF), "i"(OFF + 1024) : "memory");
  else
    asm volatile("buffer_load_dwordx4 %0, %2, %3 offen offset:%4 lds\n\tbuffer_load_dwordx4 %1, %2, %3 offen offset:%5 lds"
                 :: "v"(vo0), "v"(vo1), "s"(srd), "s"(soff), "i"(OFF), "i"(OFF + 1024) : "memory");
}
template <int OFF, bool NT> DI void dma1(unsigned vo, srd_t srd, unsigned soff) {
  if constexpr (NT) asm volatile("buffer_load_dwordx4 %0, %1, %2 offen offset:%3 nt lds" :: "v"(vo), "s"(srd), "s"(soff), "i"(OFF) : "memory");
  else asm volatile("buffer_load_dwordx4 %0, %1, %2 offen offset:%3 lds" :: "v"(vo), "s"(srd), "s"(soff), "i"(OFF) : "memory");
}
template <bool NT> DI void dma4(unsigned m0v, const unsigned (&vo)[4], srd_t srd, unsigned soff) { SETM0N(m0v); dma2<0, NT>(vo[0], vo[1], srd, soff); dma2<2048, NT>(vo[2], vo[3], srd, soff); }
template <int N> DI void waitv() { asm volatile("s_waitcnt vmcnt(%0)" :: "i"(N > 63 ? 63 : N) : "memory"); }
#define WAITL() do { asm volatile("" ::: "memory"); __builtin_amdgcn_s_waitcnt(0xC07F); asm volatile("" ::: "memory"); } while (0)
#define BARRIER() do { WAITL(); asm volatile("s_barrier" ::: "memory"); } while (0)

template <int SLP> DI void poll_flags(GAS u64* f, u64 tag) {
#pragma unroll 1
  for (unsigned spins = 0; spins < (1u << 21); ++spins) {
    const bool ok = (__hip_atomic_load(f, RLX_AGENT) == tag);
    if (__all(ok)) break;
    __builtin_amdgcn_s_sleep(SLP);
  }
  asm volatile("" ::: "memory");
}

constexpr int ASLP = 6;
constexpr int TCH_KV = 2;
constexpr bool NT_WO = false;

struct P1 {
  const char *xA, *kA, *vA, *wqA, *woA, *xB, *kB, *vB, *wqB, *woB;
  char* ws;
  char* out;
};

DI u32x4 pack8(const f32x16& x, int s) {
  return u32x4{cvtpk(x[8 * s], x[8 * s + 1]), cvtpk(x[8 * s + 2], x[8 * s + 3]), cvtpk(x[8 * s + 4], x[8 * s + 5]), cvtpk(x[8 * s + 6], x[8 * s + 7])};
}

constexpr unsigned ESZ = 16384u;
constexpr unsigned HREG = 5u * ESZ;

template <bool BB> DI void body(const P1& p, const unsigned ldsb) {
  constexpr int PE = BB ? 8 : 4;
  constexpr bool NTS = true;
  const unsigned tid = threadIdx.x, lane = tid & 63u, wid = rfl(tid >> 6);
  const unsigned g8 = blockIdx.x & 7u, idx = (blockIdx.x >> 3) & 15u;
  const unsigned qb = BB ? (wid & 1u) : (idx & 1u), item = wid >> 1;
  const unsigned h0 = BB ? 4u * g8 + 2u * (idx & 1u) : 4u * g8, b = idx >> 1;
  const unsigned l31 = lane & 31u, hh = lane >> 5, l15 = lane & 15u, g4 = lane >> 4;
  const unsigned hreg = ldsb + item * HREG;
  const unsigned ring = BB ? hreg : ldsb;
  const unsigned dsub = BB ? qb * 8192u : wid * 4096u;

  u64 epoch_v;
  asm volatile("global_load_dwordx2 %0, %1, off sc1" : "=v"(epoch_v) : "v"(p.ws + WS_EPOCH) : "memory");
  const size_t kvoff = (BB ? (size_t)(b * 32u + h0 + item) : (size_t)(b * 8u + g8)) * 262144u;
  const srd_t sK = MKSRD((BB ? p.kB : p.kA) + kvoff, 262144), sV = MKSRD((BB ? p.vB : p.vA) + kvoff, 262144);
  unsigned kvo[4];
#pragma unroll
  for (int i = 0; i < 4; ++i) kvo[i] = g4 * 256u + (((l15 ^ g4) ^ (4u * i)) * 16u);
  const unsigned vvo = g4 * 256u + ((l15 ^ (g4 << 2)) * 16u);
  const unsigned vv4[4] = {vvo, vvo, vvo, vvo};
  const unsigned dring = ring + dsub;
  const unsigned rr = ((BB ? (idx * 2u + item) * 3u + g8 : b * 2u + g8 * 5u + 1u) & 15u) * ESZ;
#define RO(x) ((((x) + rr) & 0x3ffffu) | ((x) & 0x40000u))
#define DG(m0v, vo, srd, soff, NT, cnt, g) do { if ((g) < 0) SETM0(m0v); else if ((g) < (cnt)) dma1<((g) & 3) * 1024, NT>(vo[(g) & 3], srd, soff); } while (0)
#define EG(n, j, g) do { if ((n) < 2 || ((n) & 1)) DG(dring + ((n) & 3) * ESZ + (j) * 4096u, kvo, sK, RO(((n) < 2 ? (n) : ((n) + 1) / 2) * ESZ + dsub) + (j) * 4096u, NTS, 4, g); \
                         else DG(dring + ((n) & 3) * ESZ + (j) * 4096u, vv4, sV, RO(((n) - 2) / 2 * ESZ + dsub) + (j) * 4096u, NTS, 4, g); } while (0)
#define EG4(n, j) do { EG(n, j, -1); asm volatile("s_nop 0"); EG(n, j, 0); EG(n, j, 1); EG(n, j, 2); EG(n, j, 3); } while (0)
#define ELEM(n) do { EG4(n, 0); if (BB) EG4(n, 1); } while (0)

  const unsigned gbuf = ldsb + 4u * ESZ + wid * 24576u;
#define GT(i) (((i) + idx) & 15u)
#define GD(i, hi, g) DG(gbuf + ((i) % 3) * 8192u + (hi) * 4096u, gvo, gsr, (GT(i) * 2u + (hi)) * GPITCH16, false, 4, g)
#define GD8(i) do { GD(i, 0, -1); asm volatile("s_nop 0"); GD(i, 0, 0); GD(i, 0, 1); GD(i, 0, 2); GD(i, 0, 3); GD(i, 1, -1); asm volatile("s_nop 0"); GD(i, 1, 0); GD(i, 1, 1); GD(i, 1, 2); GD(i, 1, 3); } while (0)
#define GW(i, x) gw[(i) & 1][x] = cat8(lds_tr((ga ^ (64u * ((x) & 3))) + ((i) % 3) * 8192u + ((x) >> 2) * 4096u), lds_tr((ga ^ (64u * ((x) & 3))) + ((i) % 3) * 8192u + ((x) >> 2) * 4096u + GJ2))
#define GX(i, x) do { \
      if ((i) == 0 && (x) < 4) asm volatile("v_mfma_f32_32x32x16_bf16 %0, %1, %2, 0" : "=&v"(acc[(x) & 3]) : "v"(gw[(i) & 1][x]), "v"(gb[(i) & 1][(x) >> 2])); \
      else asm volatile("v_mfma_f32_32x32x16_bf16 %0, %1, %2, %0" : "+v"(acc[(x) & 3]) : "v"(gw[(i) & 1][x]), "v"(gb[(i) & 1][(x) >> 2])); \
      if ((i) < 15) { GW((i) + 1, x); if ((x) == 0) GB((i) + 1); } SBAR(); \
      if ((i) < 13) { GD((i) + 3, (x) >> 2, (x) & 3); if ((x) == 3) GD((i) + 3, 1, -1); } SBAR(); } while (0)
#define GTILE(i, XTRA) do { if ((i) < 13) GD((i) + 3, 0, -1); waitv<((i) < 14 ? 8 : 0) + (XTRA)>(); WAITL(); SBAR(); \
      GX(i, 0); GX(i, 1); GX(i, 2); GX(i, 3); GX(i, 4); GX(i, 5); GX(i, 6); GX(i, 7); } while (0)
#define GFIRST() do { GB(0); GW(0, 0); GW(0, 1); GW(0, 2); GW(0, 3); GW(0, 4); GW(0, 5); GW(0, 6); GW(0, 7); } while (0)

  bf16x8 qf[8];
  f32x16 o[4];
  unsigned tch = 0u;
#define TOUCH(srd, soff) asm volatile("buffer_load_dword %0, %1, %2, %3 offen" : "+v"(tch) : "v"(lane * 128u), "s"(srd), "s"(soff) : "memory")
#define QO(g) do { qf[2 * (g)] = __builtin_bit_cast(bf16x8, pack8(acc[g], 0)); qf[2 * (g) + 1] = __builtin_bit_cast(bf16x8, pack8(acc[g], 1)); asm volatile("" : "+a"(qf[2 * (g)]), "+a"(qf[2 * (g) + 1])); } while (0)
  const unsigned p4 = lane & 3u, q4 = (lane >> 2) & 3u;
  const unsigned wl = (8u * hh + q4) * 256u + (q4 << 6) + (g4 & 1u) * 32u + (((p4 & 1u) << 1) | (p4 >> 1)) * 8u;
  if constexpr (!BB) {
    constexpr unsigned GPITCH16 = 16u * 8192u, GJ2 = 1024u;
    const srd_t gsr = MKSRD(p.wqA + (h0 + wid) * 256u, 4 << 20), sx = MKSRD(p.xA + (size_t)(b * 64u + qb * 32u) * 1024u, 32768);
    unsigned gvo[4], xvo[4];
#pragma unroll
    for (int i = 0; i < 4; ++i) { gvo[i] = g4 * 8192u + ((l15 ^ (g4 << 2)) * 16u) + i * (32768u - 1024u); xvo[i] = g4 * 1024u + (((l15 ^ g4) ^ (4u * i)) * 16u) + i * 3072u; }
    dma4<false>(ldsb + 2u * ESZ + wid * 8192u, xvo, sx, wid * 256u); dma4<false>(ldsb + 2u * ESZ + wid * 8192u + 4096u, xvo, sx, wid * 256u + 16384u);
    GD8(0); GD8(1);
#pragma unroll
    for (int db = 0; db < 4; ++db) { o[db] = f32x16{}; asm volatile("" : "+a"(o[db])); }
    GD8(2);
#pragma unroll
    for (int i = 0; i < TCH_KV; ++i) TOUCH(item ? sV : sK, RO((4u * i + 2u * qb + (wid & 1u)) * 8192u));
    const unsigned ga = gbuf + wl, xb = ldsb + 2u * ESZ + l31 * 256u + ((hh ^ l15) * 16u);
    f32x16 acc[4]; bf16x8 gw[2][8], gb[2][2];
#define GB(i) do { const unsigned a_ = (xb ^ ((GT(i) & 3u) * 64u)) + (GT(i) >> 2) * 8192u; gb[(i) & 1][0] = lds_rd128(a_); gb[(i) & 1][1] = lds_rd128(a_ ^ 32u); } while (0)
    waitv<16 + TCH_KV>(); BARRIER();
    GFIRST();
    GTILE(0, TCH_KV); GTILE(1, TCH_KV); GTILE(2, 0); GTILE(3, 0); GTILE(4, 0); GTILE(5, 0); GTILE(6, 0); GTILE(7, 0);
    GTILE(8, 0); GTILE(9, 0); GTILE(10, 0); GTILE(11, 0); GTILE(12, 0);
    ELEM(0); ELEM(1);
    GTILE(13, 8); GTILE(14, 8);
    BARRIER();
    ELEM(2);
    GTILE(15, 12);
#undef GB
    PIN(tch);
    asm volatile("s_nop 7\n\ts_nop 7" : "+v"(acc[0]), "+v"(acc[1]), "+v"(acc[2]), "+v"(acc[3]));
    QO(0); QO(1); QO(2); QO(3); SBAR();
  } else {
    constexpr int QE = 10;
    const unsigned rot = b + 4u * (idx & 1u);
    const srd_t sw = MKSRD(p.wqB + h0 * 256u, 4 << 20), sx = MKSRD(p.xB + (size_t)b * 65536u, 65536);
    unsigned wvo[4], xvo[4];
#pragma unroll
    for (int i = 0; i < 4; ++i) {
      wvo[i] = g4 * 8192u + ((l15 ^ (g4 << 2)) * 16u) + i * (32768u - 1024u);
      xvo[i] = (lane >> 3) * 1024u + (((lane & 7u) ^ ((4u * i + g4) & 7u)) * 16u) + i * (8192u - 1024u);
    }
#define WOFF(s) (((s) & 3) * ESZ)
#define XOFF(s) ((((s) >> 1) & 1) * HREG + 4u * ESZ + ((s) & 1) * 8192u)
    const unsigned wdst = ldsb + wid * 4096u, xdst = ldsb + wid * 2048u;
#define QG(s, j, g) do { const unsigned cv_ = ((s) + rot) & 7u; \
      if ((j) == 0) DG(wdst + WOFF(s), wvo, sw, (cv_ * 64u + wid * 16u) * 8192u, false, 4, g); \
      if ((j) == 1) DG(wdst + WOFF(s) + HREG, wvo, sw, (cv_ * 64u + wid * 16u) * 8192u + 256u, false, 4, g); \
      if ((j) == 2) DG(xdst + XOFF(s), xvo, sx, wid * 16384u + cv_ * 128u, false, 2, g); } while (0)
#define QG4(s, j) do { QG(s, j, -1); asm volatile("s_nop 0"); QG(s, j, 0); QG(s, j, 1); QG(s, j, 2); QG(s, j, 3); } while (0)
#define QLD(s) do { QG4(s, 0); QG4(s, 1); QG4(s, 2); } while (0)
    const unsigned wa = hreg + wl;
    const unsigned xa = ldsb + qb * 4096u + l31 * 128u + ((hh ^ ((l31 >> 1) & 7u)) * 16u);
    f32x16 acc[4];
    bf16x8 xf[2][4], wf[2][4][4];
#define QM1(s, ks, g) do { if ((s) == 0 && (ks) == 0) asm volatile("v_mfma_f32_32x32x16_bf16 %0, %1, %2, 0" : "=&v"(acc[g]) : "v"(wf[(s) & 1][g][ks]), "v"(xf[(s) & 1][ks])); \
      else asm volatile("v_mfma_f32_32x32x16_bf16 %0, %1, %2, %0" : "+v"(acc[g]) : "v"(wf[(s) & 1][g][ks]), "v"(xf[(s) & 1][ks])); } while (0)
#define QOP(HOOK, NOP, G, v) do { if ((v) < (NOP) && ((v) * 16) / (NOP) == (G)) { HOOK((v) >> 2, (v) & 3); if (((v) & 3) == 3 && (v) + 1 < (NOP)) { HOOK(((v) >> 2) + 1, -1); } } } while (0)
#define QGAP(s, ks, g, HOOK, NOP) do { if ((s) > 0) QM1((s) - 1, ks, g); else asm volatile("s_nop 0"); \
        if ((g) == 0) xf[(s) & 1][ks] = lds_rd128((xa ^ (32u * (ks))) + XOFF(s)); \
        wf[(s) & 1][g][ks] = cat8(lds_tr((wa ^ (64u * (g))) + WOFF(s) + (ks) * 4096u), lds_tr((wa ^ (64u * (g))) + WOFF(s) + (ks) * 4096u + 1024u)); \
        SBAR(); \
        QOP(HOOK, NOP, 4 * (ks) + (g), 0); QOP(HOOK, NOP, 4 * (ks) + (g), 1); QOP(HOOK, NOP, 4 * (ks) + (g), 2); QOP(HOOK, NOP, 4 * (ks) + (g), 3); \
        QOP(HOOK, NOP, 4 * (ks) + (g), 4); QOP(HOOK, NOP, 4 * (ks) + (g), 5); QOP(HOOK, NOP, 4 * (ks) + (g), 6); QOP(HOOK, NOP, 4 * (ks) + (g), 7); \
        QOP(HOOK, NOP, 4 * (ks) + (g), 8); QOP(HOOK, NOP, 4 * (ks) + (g), 9); \
        SBAR(); } while (0)
#define QKS(s, ks, HOOK, NOP) do { QGAP(s, ks, 0, HOOK, NOP); QGAP(s, ks, 1, HOOK, NOP); QGAP(s, ks, 2, HOOK, NOP); QGAP(s, ks, 3, HOOK, NOP); } while (0)
#define QCMP(s, HOOK, NOP) do { HOOK(0, -1); SBAR(); QKS(s, 0, HOOK, NOP); QKS(s, 1, HOOK, NOP); QKS(s, 2, HOOK, NOP); QKS(s, 3, HOOK, NOP); } while (0)
#define HQ0(j, g) QG(3, j, g)
#define HQ1(j, g) QG(4, j, g)
#define HQ2(j, g) QG(5, j, g)
#define HQ3(j, g) QG(6, j, g)
#define HQ4(j, g) QG(7, j, g)
#define HQ5(j, g) EG(0, j, g)
#define HQ6(j, g) EG(1, j, g)
#define HQ7(j, g) EG(2, j, g)
    QLD(0); QLD(1);
#pragma unroll
    for (int db = 0; db < 4; ++db) { o[db] = f32x16{}; asm volatile("" : "+a"(o[db])); }
    QLD(2);
    waitv<2 * QE>(); BARRIER(); QCMP(0, HQ0, QE);
    waitv<2 * QE>(); BARRIER(); QCMP(1, HQ1, QE);
    waitv<2 * QE>(); BARRIER(); QCMP(2, HQ2, QE);
    waitv<2 * QE>(); BARRIER(); QCMP(3, HQ3, QE);
    waitv<2 * QE>(); BARRIER(); QCMP(4, HQ4, QE);
    waitv<2 * QE>(); BARRIER(); QCMP(5, HQ5, PE);
    waitv<QE + PE>(); BARRIER(); QCMP(6, HQ6, PE);
    waitv<2 * PE>(); BARRIER(); QCMP(7, HQ7, PE);
    QM1(7, 0, 0); QM1(7, 1, 0); QM1(7, 2, 0); QM1(7, 3, 0); SBAR();
    QM1(7, 0, 1); QM1(7, 1, 1); QM1(7, 2, 1); QM1(7, 3, 1); asm volatile("s_nop 7" : "+v"(acc[0])); SBAR();
    QM1(7, 0, 2); QM1(7, 1, 2); QO(0); SBAR(); QM1(7, 2, 2); QM1(7, 3, 2); PIN(acc[1]); SBAR();
    QM1(7, 0, 3); QM1(7, 1, 3); QO(1); SBAR(); QM1(7, 2, 3); QM1(7, 3, 3); PIN(acc[2]); SBAR();
    QO(2); SBAR(); asm volatile("s_nop 3" : "+v"(acc[3])); QO(3); SBAR();
  }
  PIN(epoch_v);
  const u64 tag = mix64(((u64)rfl((unsigned)(epoch_v >> 32)) << 32) | rfl((unsigned)epoch_v));

  unsigned tA = 3u * ESZ + dsub, tB = 2u * ESZ + dsub;
  const unsigned kb = ring + l31 * 256u + ((hh ^ l15) * 16u);
  const unsigned vl = (4u * hh + ((lane >> 2) & 3u)) * 256u + (((lane >> 2) & 3u) << 6) + (g4 & 1u) * 32u + (lane & 3u) * 8u;
  const unsigned vb = ring + vl;
  f32x16 sA[2], sB[2], negm;
  bf16x8 kf[2][8]; s16x4 vlo[4][4], vhi[4][4]; u32x4 pw[2][2];
  float fsc = 1.f, ls0 = 0.f, lt0 = 0.f, ls1 = 0.f, lt1 = 0.f;
#define QK0(D, a, bq) asm volatile("v_mfma_f32_32x32x16_bf16 %0, %1, %2, 0" : "=&v"(D) : "v"(a), "a"(bq))
#define QKC(D, a, bq, C) asm volatile("v_mfma_f32_32x32x16_bf16 %0, %1, %2, %3" : "=&v"(D) : "v"(a), "a"(bq), "v"(C))
#define QKA(D, a, bq) asm volatile("v_mfma_f32_32x32x16_bf16 %0, %1, %2, %0" : "+v"(D) : "v"(a), "a"(bq))
#define WAITE(k) do { waitv<PE * (k)>(); BARRIER(); } while (0)
#define EXP2(x) __builtin_amdgcn_exp2f((x) * C2)
#define KRD(s) do { const unsigned a_ = ka ^ (32u * (s)); kf[0][s] = lds_rd128(a_); kf[1][s] = lds_rd128(a_ + 8192u); } while (0)

  WAITE(2);
  {
    const unsigned ka = kb;
#pragma unroll
    for (int s = 0; s < 8; ++s) KRD(s);
  }
  QK0(sA[0], kf[0][0], qf[0]); QK0(sA[1], kf[1][0], qf[0]);
#pragma unroll
  for (int s = 1; s < 8; ++s) { QKA(sA[0], kf[0][s], qf[s]); QKA(sA[1], kf[1][s], qf[s]); if (s == 2) ELEM(3); }
  WAITE(2);
  ELEM(4);
  {
    const unsigned ka = kb + ESZ;
#pragma unroll
    for (int s = 0; s < 8; ++s) KRD(s);
  }
  asm volatile("s_nop 7\n\ts_nop 7" : "+v"(sA[0]), "+v"(sA[1]));
  {
    const float m0 = swap_max(max2f(max8hi(max8lo(sA[0]), sA[0]), max8hi(max8lo(sA[1]), sA[1])));
#pragma unroll
    for (int r = 0; r < 16; ++r) { sA[0][r] -= m0; sA[1][r] -= m0; negm[r] = -m0; }
    PIN(negm);
#pragma unroll
    for (int r = 0; r < 8; ++r) { sA[0][r] = EXP2(sA[0][r]); sA[1][r] = EXP2(sA[1][r]); }
#pragma unroll
    for (int r = 0; r < 8; r += 2) { ls0 += sA[0][r]; lt0 += sA[0][r + 1]; ls1 += sA[1][r]; lt1 += sA[1][r + 1]; }
  }
  unsigned wvo[4];
  const srd_t sW = MKSRD((BB ? p.woB : p.woA) + (size_t)h0 * 131072u + wid * 256u, (BB ? 262144 : 524288) - 768);
  const unsigned wbuf = BB ? hreg + qb * 8192u : ldsb + ESZ + wid * 8192u;
  const unsigned bhi = b >> 2;
#define WSO(i) ((((i) + b) & 3u) * 65536u + ((((i) >> 2) & 1u) ^ bhi) * 128u)
#define WG(i, hi, g) DG(wbuf + (1u + ((i) & 3)) * ESZ + (hi) * 4096u, wvo, sW, WSO(i) + (hi) * 32768u, NT_WO, 4, g)
#define WOP(v, TA, TB_) do { if ((v) < 8) WG(TA, ((v) >> 2) & 1, (v) & 3); else WG(TB_, ((v) >> 2) & 1, (v) & 3); \
    if ((v) == 3) WG(TA, 1, -1); if ((v) == 7 && (TB_) >= 0) WG((TB_) & 7, 0, -1); if ((v) == 11) WG((TB_) & 7, 1, -1); } while (0)

#define VRD(db, k4) do { const unsigned a_ = (vb ^ (64u * (db))) + S2 + 4096u * (k4); vlo[db][k4] = lds_tr(a_); vhi[db][k4] = lds_tr(a_ + 2048u); } while (0)
#define GAPA(C, N, s, KB, LAST) do { \
    if ((s) == 0) QKC(N[KB], kf[KB][0], qf[0], negm); else QKA(N[KB], kf[KB][s], qf[s]); \
    if ((s) < 4) { float e0_ = EXP2(C[KB][(8 + 2 * (s)) & 15]), e1_ = EXP2(C[KB][(9 + 2 * (s)) & 15]); PIN(e0_); PIN(e1_); C[KB][(8 + 2 * (s)) & 15] = e0_; C[KB][(9 + 2 * (s)) & 15] = e1_; \
      pw[KB][0][(s) & 3] = cvtpk(C[KB][2 * (s)], C[KB][2 * (s) + 1]); PIN(pw[KB][0]); } \
    else { ls##KB += C[KB][2 * (s)]; lt##KB += C[KB][2 * (s) + 1]; pw[KB][1][(s) & 3] = cvtpk(C[KB][2 * (s)], C[KB][2 * (s) + 1]); PIN(pw[KB][1]); PIN(ls##KB); PIN(lt##KB); } \
    if ((s) < 4) VRD((2 * (s) + KB) & 3, VK4(((s) >> 1) & 1)); else if ((s) < 6) { VRD(2 * KB, VK4(((s) & 1) + 2)); VRD(2 * KB + 1, VK4(((s) & 1) + 2)); } \
    SBAR(); \
    if (LAST) { if (BB && KB == 1) WOP(s, 0, -1); } \
    else if (KB == 1 && (BB || (s) < 4)) { dma1<((s) & 3) * 1024, NTS>(kvo[(s) & 3], sK, RO(tA) + ((s) >> 2) * 4096u); if (BB && (s) == 3) SETM0(dring + S1 + 4096u); } \
    SBAR(); } while (0)
#define VK4(i) ((i) == 0 ? 0 : (i) == 1 ? 2 : (i) == 2 ? 1 : 3)
#define PHASE_A(C, N, ODD, L) do { \
    constexpr unsigned S1 = (ODD ? 3u : 1u) * ESZ, S2 = (ODD ? 0u : 2u) * ESZ; \
    if (!BB && ASLP > 0) __builtin_amdgcn_s_sleep(ASLP); \
    if (L) { if (BB) WG(0, 0, -1); } else SETM0(dring + S1); \
    WAITE(2); SBAR(); \
    GAPA(C, N, 0, 0, L); GAPA(C, N, 0, 1, L); GAPA(C, N, 1, 0, L); GAPA(C, N, 1, 1, L); GAPA(C, N, 2, 0, L); GAPA(C, N, 2, 1, L); GAPA(C, N, 3, 0, L); GAPA(C, N, 3, 1, L); \
    GAPA(C, N, 4, 0, L); GAPA(C, N, 4, 1, L); GAPA(C, N, 5, 0, L); GAPA(C, N, 5, 1, L); GAPA(C, N, 6, 0, L); GAPA(C, N, 6, 1, L); GAPA(C, N, 7, 0, L); GAPA(C, N, 7, 1, L); \
    tA += ESZ; } while (0)
#define PVM(i) o[(i) & 3] = MFMA32(cat8(vlo[(i) & 3][VK4((i) >> 2)], vhi[(i) & 3][VK4((i) >> 2)]), __builtin_bit_cast(bf16x8, pw[((i) >> 2) & 1][(i) >> 3]), o[(i) & 3])
#define DMAB(v, LAST) do { SBAR(); if (LAST) { if (BB && ((v) & 1) == 0) WOP((v) >> 1, 1, -1); } \
    else if (((v) & 1) == 0 && (BB || (v) < 8)) { dma1<(((v) >> 1) & 3) * 1024, NTS>(vvo, sV, RO(tB) + ((v) >> 3) * 4096u); if (BB && (v) == 6) SETM0(dring + S2 + 4096u); } SBAR(); } while (0)
#define GAPE(N, j, LAST) do { PVM(8 + (j)); N[0][j] = EXP2(N[0][j]); N[1][j] = EXP2(N[1][j]); \
    if ((j) > 0) { if ((j) & 1) { ls0 += N[0][((j) + 15) & 15]; ls1 += N[1][((j) + 15) & 15]; } else { lt0 += N[0][((j) + 15) & 15]; lt1 += N[1][((j) + 15) & 15]; } } \
    PIN(N[0]); PIN(N[1]); PIN(ls0); PIN(ls1); PIN(lt0); PIN(lt1); DMAB(8 + (j), LAST); } while (0)
#define PHASE_B(N, ODD, LAST) do { \
    constexpr unsigned S2 = (ODD ? 0u : 2u) * ESZ, S3 = (ODD ? 1u : 3u) * ESZ; \
    if (LAST) { if (BB) WG(1, 0, -1); BARRIER(); asm volatile("s_nop 7"); } else { SETM0(dring + S2); WAITE(2); } SBAR(); \
    const unsigned ka = kb + S3; \
    PVM(0); KRD(0); DMAB(0, LAST); \
    PVM(1); KRD(1); DMAB(1, LAST); \
    PVM(2); KRD(2); float ma0 = max3f(N[0][0], N[0][1], N[0][2]), ma1 = max3f(N[1][0], N[1][1], N[1][2]), mb0 = max3f(N[0][3], N[0][4], N[0][5]), mb1 = max3f(N[1][3], N[1][4], N[1][5]); DMAB(2, LAST); \
    PVM(3); KRD(3); ma0 = max3f(ma0, N[0][6], N[0][7]); ma1 = max3f(ma1, N[1][6], N[1][7]); mb0 = max3f(mb0, N[0][8], N[0][9]); mb1 = max3f(mb1, N[1][8], N[1][9]); DMAB(3, LAST); \
    PVM(4); KRD(4); ma0 = max3f(ma0, N[0][10], N[0][11]); ma1 = max3f(ma1, N[1][10], N[1][11]); mb0 = max3f(mb0, N[0][12], N[0][13]); mb1 = max3f(mb1, N[1][12], N[1][13]); DMAB(4, LAST); \
    PVM(5); ma0 = max3f(ma0, N[0][14], N[0][15]); ma1 = max3f(ma1, N[1][14], N[1][15]); const float mbb = max2f(mb0, mb1); KRD(5); DMAB(5, LAST); \
    PVM(6); const float mall = max3f(ma0, ma1, mbb); KRD(6); const bool grew = __any(mall > THR); DMAB(6, LAST); \
    PVM(7); KRD(7); DMAB(7, LAST); \
    if (__builtin_expect(grew, 0)) {     \
      const float d0 = max2f(swap_max(mall), 0.f); \
      fsc = EXP2(-d0); ls0 *= fsc; lt0 *= fsc; ls1 *= fsc; lt1 *= fsc; \
      sub_tile(N[0], d0); sub_tile(N[1], d0); sub_tile(negm, d0); \
    } \
    SBAR(); \
    GAPE(N, 0, LAST); GAPE(N, 1, LAST); GAPE(N, 2, LAST); GAPE(N, 3, LAST); GAPE(N, 4, LAST); GAPE(N, 5, LAST); GAPE(N, 6, LAST); GAPE(N, 7, LAST); \
    lt0 += N[0][7]; lt1 += N[1][7]; \
    if (__builtin_expect(grew, 0)) { \
      asm volatile("s_nop 7\n\ts_nop 7" ::: "memory"); \
      scale_acc(o[0], fsc); scale_acc(o[1], fsc); scale_acc(o[2], fsc); scale_acc(o[3], fsc); \
    } \
    tB += ESZ; } while (0)

  if (tid == 0u) __hip_atomic_store((GAS u64*)(p.ws + WS_FLAG3) + (size_t)blockIdx.x * 8u, tag, RLX_AGENT);
  for (unsigned t = 0; t < 14u; t += 2u) {
    PHASE_A(sA, sB, false, false); PHASE_B(sB, false, false);
    PHASE_A(sB, sA, true, false);  PHASE_B(sA, true, false);
  }
#pragma unroll
  for (int j = 0; j < 4; ++j) wvo[j] = BB ? (lane >> 3) * 1024u + (((lane & 7u) ^ ((g4 & 1u) << 2)) * 16u) + j * 7168u : g4 * 1024u + ((l15 ^ (g4 << 2)) * 16u) + j * 3072u;
  constexpr unsigned GPITCH16 = 16u * 1024u, GJ2 = 2048u;
  const srd_t gsr = sW; const unsigned (&gvo)[4] = wvo;
  PHASE_A(sA, sB, false, true);
  if (!BB) { GD8(0); GD8(1); GD8(2); }
  PHASE_B(sB, false, true);
  bf16x8 wfr[2][8];
  const unsigned wb = wbuf + (4u * hh + q4) * 128u + (q4 >> 1) * 64u + (g4 & 1u) * 32u + p4 * 8u;
#define WRD(i, x) do { const unsigned a_ = (wb ^ (64u * ((x) & 1))) + (1u + ((i) & 3)) * ESZ + 2048u * ((x) >> 1); wfr[(i) & 1][x] = cat8(lds_tr(a_), lds_tr(a_ + 1024u)); } while (0)
  {
    constexpr unsigned S2 = 0u;
#pragma unroll
    for (int r = 8; r < 16; ++r) { sB[0][r] = EXP2(sB[0][r]); sB[1][r] = EXP2(sB[1][r]); ls0 += sB[0][r]; ls1 += sB[1][r]; }
#pragma unroll
    for (int i = 0; i < 8; ++i) { pw[0][i >> 2][i & 3] = cvtpk(sB[0][2 * i], sB[0][2 * i + 1]); pw[1][i >> 2][i & 3] = cvtpk(sB[1][2 * i], sB[1][2 * i + 1]); }
    PIN(pw[0][0]); PIN(pw[0][1]); PIN(pw[1][0]); PIN(pw[1][1]);
    float inv = __builtin_amdgcn_rcpf(swap_add((ls0 + lt0) + (ls1 + lt1))); PIN(inv);
    SBAR();
    if (BB) WG(2, 0, -1);
    waitv<BB ? 16 : 24>(); BARRIER();
#pragma unroll
    for (int i = 0; i < 16; ++i) VRD(i >> 2, VK4(i & 3));
#define PINA(x) asm volatile("" : "+a"(x))
#define PV1(db, j) do { PVM(4 * (j) + (db)); PINA(o[db]); SBAR(); if (BB) WOP(4 * (db) + (j), 2, 3); SBAR(); } while (0)
#define PV4(db) do { PV1(db, 0); PV1(db, 1); PV1(db, 2); PV1(db, 3); } while (0)
#define OWR(db) do { const f32x16 y_ = o[db] * inv; lds_wru4(wbuf + (2 * (db)) * 1024u + lane * 16u, pack8(y_, 0)); lds_wru4(wbuf + (2 * (db) + 1) * 1024u + lane * 16u, pack8(y_, 1)); } while (0)
    PV4(0); SBAR(); if (BB) { BARRIER(); } PV4(1); SBAR();
    OWR(0); SBAR(); PV4(2); OWR(1); SBAR(); PV4(3);
    if (BB) {
      waitv<24>();
#pragma unroll
      for (int i = 0; i < 8; ++i) WRD(0, i);
    }
    OWR(2); OWR(3);
    BARRIER();
  }
  constexpr unsigned br = BB ? 1u : 0u;
  const srd_t sP = MKSRD(p.ws + WS_PART, 16 << 20);
  GAS u64* flags = (GAS u64*)(p.ws + WS_FLAG);
  const unsigned hp = BB ? (h0 >> 1) : g8;
  const unsigned fgrp = BB ? wid * 16u : (qb * 4u + wid) * 8u;
  if constexpr (!BB) {
    f32x16 acc[4]; bf16x8 gw[2][8], gb[2][2];
    const unsigned ga = gbuf + vl;
#define GB(i) do { const unsigned a_ = ldsb + ESZ + GT(i) * 2048u + lane * 16u; gb[(i) & 1][0] = lds_rd128(a_); gb[(i) & 1][1] = lds_rd128(a_ + 1024u); } while (0)
    waitv<16>();
    GFIRST();
    GTILE(0, 0); GTILE(1, 0); GTILE(2, 0); GTILE(3, 0); GTILE(4, 0); GTILE(5, 0); GTILE(6, 0); GTILE(7, 0);
    GTILE(8, 0); GTILE(9, 0); GTILE(10, 0); GTILE(11, 0); GTILE(12, 0); GTILE(13, 0); GTILE(14, 0); GTILE(15, 0);
    asm volatile("s_nop 7\n\ts_nop 7" : "+v"(acc[0]), "+v"(acc[1]), "+v"(acc[2]), "+v"(acc[3]));
    const unsigned pb = b * 1048576u + (qb * 32u + 8u * wid) * 16384u + hp * 1024u + lane * 16u;
#pragma unroll
    for (int i = 0; i < 8; ++i) __builtin_amdgcn_raw_buffer_store_b128(pack8(acc[i >> 1], i & 1), sP, pb + i * 16384u, 0, 16);
  } else {
    bf16x8 of[4][2][4];
#define ORD(i, x) do { const unsigned r_ = ((i) + b) & 3u; of[i][(x) & 1][(x) >> 1] = lds_rd128(ldsb + (r_ >> 1) * HREG + (r_ & 1u) * 4096u + lane * 16u + ((x) & 1) * 8192u + ((x) >> 1) * 1024u); } while (0)
    ORD(0, 0); ORD(0, 1); ORD(0, 2); ORD(0, 3); ORD(0, 4); ORD(0, 5); ORD(0, 6); ORD(0, 7);
    f32x16 acc[4][2];
    const f32x16 z16 = {};
    const unsigned pb = (br * 8u + b) * 1048576u + 8u * wid * 16384u + hp * 1024u + lane * 16u;
    const unsigned pb0 = pb + bhi * 65536u, pb1 = pb + (bhi ^ 1u) * 65536u;
#define PST(nb, q, s2) __builtin_amdgcn_raw_buffer_store_b128(pack8(acc[nb][q], s2), sP, (((nb) >> 1) ? pb1 : pb0) + ((q) * 32 + ((nb) & 1) * 2 + (s2)) * 16384u, 0, 16)
#define OM(i, x, q) do { acc[2 * ((i) >> 2) + ((x) & 1)][q] = MFMA32(wfr[(i) & 1][x], of[(i) & 3][q][(x) >> 1], (((i) & 3) == 0 && (x) < 2) ? z16 : acc[2 * ((i) >> 2) + ((x) & 1)][q]); \
      PINA(acc[2 * ((i) >> 2) + ((x) & 1)][q]); } while (0)
#define OX(i, x) do { \
      OM(i, x, 0); WRD((i) + 1, x); if ((i) < 3) ORD(((i) + 1) & 3, x); SBAR(); \
      if ((i) < 4) WOP(x, ((i) + 4) & 7, -1); \
      SBAR(); OM(i, x, 1); if ((i) >= 4 && ((x) & 1)) PST((i) & 1, ((x) >> 2) & 1, ((x) >> 1) & 1); SBAR(); } while (0)
#define OT(i, N) do { if ((i) < 4) WG(((i) + 4) & 7, 0, -1); waitv<N>(); if ((i) > 0) { WAITL(); } SBAR(); \
      OX(i, 0); OX(i, 1); OX(i, 2); OX(i, 3); OX(i, 4); OX(i, 5); OX(i, 6); OX(i, 7); } while (0)
    OT(0, 16); OT(1, 16); OT(2, 16); OT(3, 16); OT(4, 16); OT(5, 12);
    waitv<8>(); SBAR();
#pragma unroll
    for (int i = 0; i < 8; ++i) WRD(7, (2 * i + (i >> 2)) & 7);
    SBAR();
#define OF(n, q, pn, pq) do { \
      OM(6, n, q); OM(6, 2 + (n), q); OM(6, 4 + (n), q); SBAR(); OM(6, 6 + (n), q); if ((pn) >= 0) PST(2 + ((pn) & 1), pq, 0); SBAR(); \
      OM(7, n, q); OM(7, 2 + (n), q); SBAR(); OM(7, 4 + (n), q); if ((pn) >= 0) PST(2 + ((pn) & 1), pq, 1); SBAR(); OM(7, 6 + (n), q); SBAR(); } while (0)
    OF(0, 0, -1, 0); OF(0, 1, 0, 0); OF(1, 0, 0, 1); OF(1, 1, 1, 0);
    PST(3, 1, 0); PST(3, 1, 1);
  }
  waitv<0>();
  if (lane == 0) __hip_atomic_store(flags + (size_t)((br * 8u + b) * 64u + fgrp + hp) * 8u, tag, RLX_AGENT);
  {
    constexpr int NP = BB ? 16 : 8;
    const unsigned u = BB ? hp * 4u + wid : qb * 32u + hp * 4u + wid;
    poll_flags<2>(flags + (size_t)((br * 8u + b) * 64u + (BB ? ((u >> 3) & 3u) * 16u + l15 : (u & 56u) + (lane & 7u))) * 8u, tag);
    const unsigned rb = (br * 8u + b) * 1048576u + u * 16384u + lane * 16u;
    u32x4 pv[NP];
#pragma unroll
    for (int j = 0; j < NP; ++j) pv[j] = __builtin_amdgcn_raw_buffer_load_b128(sP, rb + j * 1024u, 0, 16);
    float y[8] = {0.f, 0.f, 0.f, 0.f, 0.f, 0.f, 0.f, 0.f};
#pragma unroll
    for (int j = 0; j < NP; ++j)
#pragma unroll
      for (int e = 0; e < 4; ++e) { y[2 * e] += __uint_as_float(pv[j][e] << 16); y[2 * e + 1] += __uint_as_float(pv[j][e] & 0xffff0000u); }
    const auto r0 = __builtin_amdgcn_permlane32_swap(cvtpk(y[0], y[1]), cvtpk(y[4], y[5]), false, false);
    const auto r1 = __builtin_amdgcn_permlane32_swap(cvtpk(y[2], y[3]), cvtpk(y[6], y[7]), false, false);
    *(u32x4*)(p.out + (size_t)(b * 64u + (u >> 5) * 32u + l31) * 2048u + (br * 512u + (u & 31u) * 16u + 8u * hh) * 2u) = u32x4{r0[0], r1[0], r0[1], r1[1]};
    if (!BB && blockIdx.x == 128u && wid == 0u) {
#pragma unroll 1
      for (unsigned spins = 0; spins < (1u << 21); ++spins) {
        bool ok = true;
#pragma unroll
        for (int k = 0; k < 4; ++k) ok &= (__hip_atomic_load((GAS u64*)(p.ws + WS_FLAG3) + (size_t)(k * 64 + lane) * 8u, RLX_AGENT) == tag);
        if (__all(ok)) break;
        __builtin_amdgcn_s_sleep(8);
      }
      if (lane == 0) __hip_atomic_store((GAS u64*)(p.ws + WS_EPOCH), tag, RLX_AGENT);
    }
  }
}

__global__ __launch_bounds__(256, 1) void k1_attn(P1 p) {
  extern __shared__ __attribute__((aligned(16))) char lds[];
  const unsigned ldsb = (unsigned)(unsigned long)lds;
  if (blockIdx.x < 128u) body<true>(p, ldsb); else body<false>(p, ldsb);
}

extern "C" void kernel_launch(void* const* d_in, const int* in_sizes, int n_in,
                              void* d_out, int out_size, void* d_ws, size_t ws_size,
                              hipStream_t stream) {
  (void)in_sizes; (void)n_in; (void)out_size; (void)ws_size;
  (void)hipFuncSetAttribute((const void*)k1_attn, hipFuncAttributeMaxDynamicSharedMemorySize, K1_LDS);
  P1 p1;
  p1.xA = (const char*)d_in[0]; p1.kA = (const char*)d_in[1]; p1.vA = (const char*)d_in[2]; p1.wqA = (const char*)d_in[3]; p1.woA = (const char*)d_in[4];
  p1.xB = (const char*)d_in[5]; p1.kB = (const char*)d_in[6]; p1.vB = (const char*)d_in[7]; p1.wqB = (const char*)d_in[8]; p1.woB = (const char*)d_in[9];
  p1.ws = (char*)d_ws; p1.out = (char*)d_out;
  hipLaunchKernelGGL(k1_attn, dim3(256), dim3(256), K1_LDS, stream, p1);
}
```

```cpp
#include <hip/hip_runtime.h>
#include <hip/hip_bf16.h>

typedef float    f32x16 __attribute__((ext_vector_type(16)));
typedef float    f32x2  __attribute__((ext_vector_type(2)));
typedef __bf16   bf16x8 __attribute__((ext_vector_type(8)));
typedef __bf16   bf16x2 __attribute__((ext_vector_type(2)));
typedef short    s16x4  __attribute__((ext_vector_type(4)));
typedef unsigned u32x4  __attribute__((ext_vector_type(4)));
typedef unsigned long long u64;
typedef __amdgpu_buffer_rsrc_t srd_t;
#define DI __device__ __forceinline__
#define LAS __attribute__((address_space(3)))
#define GAS __attribute__((address_space(1)))
#define SBAR() __builtin_amdgcn_sched_barrier(0)
#define PIN(x) asm volatile("" : "+v"(x))
#define MFMA32(a, b, c) __builtin_amdgcn_mfma_f32_32x32x16_bf16(a, b, c, 0, 0, 0)
#define RLX_AGENT __ATOMIC_RELAXED, __HIP_MEMORY_SCOPE_AGENT
#define MKSRD(ptr, n) __builtin_amdgcn_make_buffer_rsrc((void*)(ptr), (short)0, (n), 0x00020000)

constexpr int K1_LDS = 163840;
constexpr float C2 = 0.08838834764831845f * 1.4426950408889634f;
constexpr float THR = 8.0f / C2;
constexpr size_t WS_PART = 0;
constexpr size_t WS_FLAG = 16u << 20;
constexpr size_t WS_FLAG3 = WS_FLAG + 65536;
constexpr size_t WS_EPOCH = WS_FLAG3 + 16384;

DI unsigned rfl(unsigned x) { return __builtin_amdgcn_readfirstlane(x); }
DI unsigned cvtpk(float lo, float hi) { f32x2 v = {lo, hi}; bf16x2 b = __builtin_convertvector(v, bf16x2); return __builtin_bit_cast(unsigned, b); }
DI bf16x8 lds_rd128(unsigned a) { return *(const LAS bf16x8*)(unsigned long)a; }
DI void lds_wru4(unsigned a, u32x4 v) { *(LAS u32x4*)(unsigned long)a = v; }
DI s16x4 lds_tr(unsigned a) { return __builtin_amdgcn_ds_read_tr16_b64_v4i16((LAS s16x4*)(unsigned long)a); }
DI bf16x8 cat8(s16x4 lo, s16x4 hi) { return __builtin_bit_cast(bf16x8, __builtin_shufflevector(lo, hi, 0, 1, 2, 3, 4, 5, 6, 7)); }
DI float max3f(float a, float b, float c) { float r; asm("v_max3_f32 %0, %1, %2, %3" : "=v"(r) : "v"(a), "v"(b), "v"(c)); return r; }
DI float max2f(float a, float b) { float r; asm("v_max_f32_e32 %0, %1, %2" : "=v"(r) : "v"(a), "v"(b)); return r; }
DI float swap_max(float m) { auto rr = __builtin_amdgcn_permlane32_swap(__float_as_uint(m), __float_as_uint(m), false, false); return max2f(__uint_as_float(rr[0]), __uint_as_float(rr[1])); }
DI float swap_add(float m) { auto rr = __builtin_amdgcn_permlane32_swap(__float_as_uint(m), __float_as_uint(m), false, false); return __uint_as_float(rr[0]) + __uint_as_float(rr[1]); }
DI float max8lo(const f32x16& p) { float a = max3f(p[0], p[1], p[2]), b = max3f(p[3], p[4], p[5]); return max3f(max3f(a, p[6], p[7]), b, b); }
DI float max8hi(float m, const f32x16& p) { float a = max3f(m, p[8], p[9]), b = max3f(p[10], p[11], p[12]); a = max3f(a, p[13], p[14]); return max3f(a, b, p[15]); }
DI void scale_acc(f32x16& x, float f) {
#pragma unroll
  for (int r = 0; r < 16; ++r) { float e = x[r], t;
    asm volatile("v_accvgpr_read_b32 %1, %0\n\tv_mul_f32 %1, %1, %2\n\tv_accvgpr_write_b32 %0, %1" : "+a"(e), "=&v"(t) : "v"(f));
    x[r] = e; }
}
DI void sub_tile(f32x16& x, float d) {
#pragma unroll
  for (int r = 0; r < 16; ++r) { float e = x[r]; asm volatile("v_sub_f32_e32 %0, %0, %1" : "+v"(e) : "v"(d)); x[r] = e; }
}
DI u64 mix64(u64 z) { z += 0x9E3779B97F4A7C15ull; z = (z ^ (z >> 30)) * 0xBF58476D1CE4E5B9ull; z = (z ^ (z >> 27)) * 0x94D049BB133111EBull; return z ^ (z >> 31); }


#define SETM0(x) asm volatile("s_mov_b32 m0, %0" :: "s"(x) : "memory")
#define SETM0N(x) asm volatile("s_mov_b32 m0, %0\n\ts_nop 0" :: "s"(x) : "memory")
template <int OFF, bool NT> DI void dma2(unsigned vo0, unsigned vo1, srd_t srd, unsigned soff) {
  if constexpr (NT)
    asm volatile("buffer_load_dwordx4 %0, %2, %3 offen offset:%4 nt lds\n\tbuffer_load_dwordx4 %1, %2, %3 offen offset:%5 nt lds"
                 :: "v"(vo0), "v"(vo1), "s"(srd), "s"(soff), "i"(OFF), "i"(OFF + 1024) : "memory");
  else
    asm volatile("buffer_load_dwordx4 %0, %2, %3 offen offset:%4 lds\n\tbuffer_load_dwordx4 %1, %2, %3 offen offset:%5 lds"
                 :: "v"(vo0), "v"(vo1), "s"(srd), "s"(soff), "i"(OFF), "i"(OFF + 1024) : "memory");
}
template <int OFF, bool NT> DI void dma1(unsigned vo, srd_t srd, unsigned soff) {
  if constexpr (NT) asm volatile("buffer_load_dwordx4 %0, %1, %2 offen offset:%3 nt lds" :: "v"(vo), "s"(srd), "s"(soff), "i"(OFF) : "memory");
  else asm volatile("buffer_load_dwordx4 %0, %1, %2 offen offset:%3 lds" :: "v"(vo), "s"(srd), "s"(soff), "i"(OFF) : "memory");
}
template <bool NT> DI void dma4(unsigned m0v, const unsigned (&vo)[4], srd_t srd, unsigned soff) { SETM0N(m0v); dma2<0, NT>(vo[0], vo[1], srd, soff); dma2<2048, NT>(vo[2], vo[3], srd, soff); }
template <int N> DI void waitv() { asm volatile("s_waitcnt vmcnt(%0)" :: "i"(N > 63 ? 63 : N) : "memory"); }
#define WAITL() do { asm volatile("" ::: "memory"); __builtin_amdgcn_s_waitcnt(0xC07F); asm volatile("" ::: "memory"); } while (0)
#define BARRIER() do { WAITL(); asm volatile("s_barrier" ::: "memory"); } while (0)

template <int SLP> DI void poll_flags(GAS u64* f, u64 tag) {
#pragma unroll 1
  for (unsigned spins = 0; spins < (1u << 21); ++spins) {
    const bool ok = (__hip_atomic_load(f, RLX_AGENT) == tag);
    if (__all(ok)) break;
    __builtin_amdgcn_s_sleep(SLP);
  }
  asm volatile("" ::: "memory");
}

constexpr int ADELAY = 40;
constexpr int ASLP = 3;
constexpr int TCH_KV = 2;
constexpr unsigned CPF1 = 10u;
constexpr bool NT_WO = false;

struct P1 {
  const char *xA, *kA, *vA, *wqA, *woA, *xB, *kB, *vB, *wqB, *woB;
  char* ws;
  char* out;
};

DI u32x4 pack8(const f32x16& x, int s) {
  return u32x4{cvtpk(x[8 * s], x[8 * s + 1]), cvtpk(x[8 * s + 2], x[8 * s + 3]), cvtpk(x[8 * s + 4], x[8 * s + 5]), cvtpk(x[8 * s + 6], x[8 * s + 7])};
}

constexpr unsigned ESZ = 16384u;
constexpr unsigned HREG = 5u * ESZ;

template <bool BB> DI void body(const P1& p, const unsigned ldsb) {
  constexpr int PE = BB ? 8 : 4;
  constexpr bool NTS = true;
  const unsigned tid = threadIdx.x, lane = tid & 63u, wid = rfl(tid >> 6);
  const unsigned g8 = blockIdx.x & 7u, idx = (blockIdx.x >> 3) & 15u;
  const unsigned qb = BB ? (wid & 1u) : (idx & 1u), item = wid >> 1;
  const unsigned h0 = BB ? 4u * g8 + 2u * (idx & 1u) : 4u * g8, b = idx >> 1;
  const unsigned l31 = lane & 31u, hh = lane >> 5, l15 = lane & 15u, g4 = lane >> 4;
  const unsigned hreg = ldsb + item * HREG;
  const unsigned ring = BB ? hreg : ldsb;
  const unsigned dsub = BB ? qb * 8192u : wid * 4096u;

  u64 epoch_v;
  asm volatile("global_load_dwordx2 %0, %1, off sc1" : "=v"(epoch_v) : "v"(p.ws + WS_EPOCH) : "memory");
  const size_t kvoff = (BB ? (size_t)(b * 32u + h0 + item) : (size_t)(b * 8u + g8)) * 262144u;
  const srd_t sK = MKSRD((BB ? p.kB : p.kA) + kvoff, 262144), sV = MKSRD((BB ? p.vB : p.vA) + kvoff, 262144);
  unsigned kvo[4];
#pragma unroll
  for (int i = 0; i < 4; ++i) kvo[i] = g4 * 256u + (((l15 ^ g4) ^ (4u * i)) * 16u);
  const unsigned vvo = g4 * 256u + ((l15 ^ (g4 << 2)) * 16u);
  const unsigned vv4[4] = {vvo, vvo, vvo, vvo};
  const unsigned dring = ring + dsub;
  const unsigned rr = ((BB ? (idx * 2u + item) * 3u + g8 : b * 2u + g8 * 5u + 1u) & 15u) * ESZ;
#define RO(x) ((((x) + rr) & 0x3ffffu) | ((x) & 0x40000u))
#define DG(m0v, vo, srd, soff, NT, cnt, g) do { if ((g) < 0) SETM0(m0v); else if ((g) < (cnt)) dma1<((g) & 3) * 1024, NT>(vo[(g) & 3], srd, soff); } while (0)
#define EG(n, j, g) do { if ((n) < 2 || ((n) & 1)) DG(dring + ((n) & 3) * ESZ + (j) * 4096u, kvo, sK, RO(((n) < 2 ? (n) : ((n) + 1) / 2) * ESZ + dsub) + (j) * 4096u, NTS, 4, g); \
                         else DG(dring + ((n) & 3) * ESZ + (j) * 4096u, vv4, sV, RO(((n) - 2) / 2 * ESZ + dsub) + (j) * 4096u, NTS, 4, g); } while (0)
#define EG4(n, j) do { EG(n, j, -1); asm volatile("s_nop 0"); EG(n, j, 0); EG(n, j, 1); EG(n, j, 2); EG(n, j, 3); } while (0)
#define ELEM(n) do { EG4(n, 0); if (BB) EG4(n, 1); } while (0)

  const unsigned gbuf = ldsb + 4u * ESZ + wid * 24576u;
#define GT(i) (((i) + gidx) & 15u)
#define GD(i, hi, g) DG(gbuf + ((i) % 3) * 8192u + (hi) * 4096u, gvo, gsr, (GT(i) * 2u + (hi)) * GPITCH16, false, 4, g)
#define GD8(i) do { GD(i, 0, -1); asm volatile("s_nop 0"); GD(i, 0, 0); GD(i, 0, 1); GD(i, 0, 2); GD(i, 0, 3); GD(i, 1, -1); asm volatile("s_nop 0"); GD(i, 1, 0); GD(i, 1, 1); GD(i, 1, 2); GD(i, 1, 3); } while (0)
#define GW(i, x) gw[(i) & 1][x] = cat8(lds_tr((ga ^ (64u * ((x) & 3))) + ((i) % 3) * 8192u + ((x) >> 2) * 4096u), lds_tr((ga ^ (64u * ((x) & 3))) + ((i) % 3) * 8192u + ((x) >> 2) * 4096u + GJ2))
#define GX(i, x) do { \
      if ((i) == 0 && (x) < 4) asm volatile("v_mfma_f32_32x32x16_bf16 %0, %1, %2, 0" : "=&v"(acc[(x) & 3]) : "v"(gw[(i) & 1][x]), "v"(gb[(i) & 1][(x) >> 2])); \
      else asm volatile("v_mfma_f32_32x32x16_bf16 %0, %1, %2, %0" : "+v"(acc[(x) & 3]) : "v"(gw[(i) & 1][x]), "v"(gb[(i) & 1][(x) >> 2])); \
      if ((i) < 15) { GW((i) + 1, x); if ((x) == 0) GB((i) + 1); } SBAR(); \
      if ((i) < 13) { GD((i) + 3, (x) >> 2, (x) & 3); if ((x) == 3) GD((i) + 3, 1, -1); } SBAR(); } while (0)
#define GTILE(i, XTRA) do { if ((i) < 13) GD((i) + 3, 0, -1); waitv<((i) < 14 ? 8 : 0) + (XTRA)>(); WAITL(); SBAR(); \
      GX(i, 0); GX(i, 1); GX(i, 2); GX(i, 3); GX(i, 4); GX(i, 5); GX(i, 6); GX(i, 7); } while (0)
#define GFIRST() do { GB(0); GW(0, 0); GW(0, 1); GW(0, 2); GW(0, 3); GW(0, 4); GW(0, 5); GW(0, 6); GW(0, 7); } while (0)

  bf16x8 qf[8];
  f32x16 o[4];
  unsigned tch = 0u;
#define TOUCH(srd, soff) asm volatile("buffer_load_dword %0, %1, %2, %3 offen" : "+v"(tch) : "v"(lane * 128u), "s"(srd), "s"(soff) : "memory")
#define QO(g) do { qf[2 * (g)] = __builtin_bit_cast(bf16x8, pack8(acc[g], 0)); qf[2 * (g) + 1] = __builtin_bit_cast(bf16x8, pack8(acc[g], 1)); asm volatile("" : "+a"(qf[2 * (g)]), "+a"(qf[2 * (g) + 1])); } while (0)
  const unsigned p4 = lane & 3u, q4 = (lane >> 2) & 3u;
  const unsigned wl = (8u * hh + q4) * 256u + (q4 << 6) + (g4 & 1u) * 32u + (((p4 & 1u) << 1) | (p4 >> 1)) * 8u;
  if constexpr (!BB) {
    constexpr unsigned GPITCH16 = 16u * 8192u, GJ2 = 1024u;
    const unsigned gidx = idx;
    const srd_t gsr = MKSRD(p.wqA + (h0 + wid) * 256u, 4 << 20), sx = MKSRD(p.xA + (size_t)(b * 64u + qb * 32u) * 1024u, 32768);
    unsigned gvo[4], xvo[4];
#pragma unroll
    for (int i = 0; i < 4; ++i) { gvo[i] = g4 * 8192u + ((l15 ^ (g4 << 2)) * 16u) + i * (32768u - 1024u); xvo[i] = g4 * 1024u + (((l15 ^ g4) ^ (4u * i)) * 16u) + i * 3072u; }
    if (ADELAY > 0) __builtin_amdgcn_s_sleep(ADELAY);
    dma4<false>(ldsb + 2u * ESZ + wid * 8192u, xvo, sx, wid * 256u); dma4<false>(ldsb + 2u * ESZ + wid * 8192u + 4096u, xvo, sx, wid * 256u + 16384u);
    GD8(0); GD8(1);
#pragma unroll
    for (int db = 0; db < 4; ++db) { o[db] = f32x16{}; asm volatile("" : "+a"(o[db])); }
    GD8(2);
#pragma unroll
    for (int i = 0; i < TCH_KV; ++i) TOUCH(item ? sV : sK, RO((4u * i + 2u * qb + (wid & 1u)) * 8192u));
    const unsigned ga = gbuf + wl, xb = ldsb + 2u * ESZ + l31 * 256u + ((hh ^ l15) * 16u);
    f32x16 acc[4]; bf16x8 gw[2][8], gb[2][2];
#define GB(i) do { const unsigned a_ = (xb ^ ((GT(i) & 3u) * 64u)) + (GT(i) >> 2) * 8192u; gb[(i) & 1][0] = lds_rd128(a_); gb[(i) & 1][1] = lds_rd128(a_ ^ 32u); } while (0)
    waitv<16 + TCH_KV>(); BARRIER();
    GFIRST();
    GTILE(0, TCH_KV); GTILE(1, TCH_KV); GTILE(2, 0); GTILE(3, 0); GTILE(4, 0); GTILE(5, 0); GTILE(6, 0); GTILE(7, 0);
    GTILE(8, 0); GTILE(9, 0); GTILE(10, 0); GTILE(11, 0); GTILE(12, 0);
    ELEM(0); ELEM(1);
    GTILE(13, 8); GTILE(14, 8);
    BARRIER();
    ELEM(2);
    GTILE(15, 12);
#undef GB
    asm volatile("s_nop 7\n\ts_nop 7" : "+v"(acc[0]), "+v"(acc[1]), "+v"(acc[2]), "+v"(acc[3]));
    QO(0); QO(1); QO(2); QO(3); SBAR();
  } else {
    constexpr int QE = 10;
    const unsigned rot = b + 4u * (idx & 1u);
    const srd_t sw = MKSRD(p.wqB + h0 * 256u, 4 << 20), sx = MKSRD(p.xB + (size_t)b * 65536u, 65536);
    unsigned wvo[4], xvo[4];
#pragma unroll
    for (int i = 0; i < 4; ++i) {
      wvo[i] = g4 * 8192u + ((l15 ^ (g4 << 2)) * 16u) + i * (32768u - 1024u);
      xvo[i] = (lane >> 3) * 1024u + (((lane & 7u) ^ ((4u * i + g4) & 7u)) * 16u) + i * (8192u - 1024u);
    }
#define WOFF(s) (((s) & 3) * ESZ)
#define XOFF(s) ((((s) >> 1) & 1) * HREG + 4u * ESZ + ((s) & 1) * 8192u)
    const unsigned wdst = ldsb + wid * 4096u, xdst = ldsb + wid * 2048u;
#define QG(s, j, g) do { const unsigned cv_ = ((s) + rot) & 7u; \
      if ((j) == 0) DG(wdst + WOFF(s), wvo, sw, (cv_ * 64u + wid * 16u) * 8192u, false, 4, g); \
      if ((j) == 1) DG(wdst + WOFF(s) + HREG, wvo, sw, (cv_ * 64u + wid * 16u) * 8192u + 256u, false, 4, g); \
      if ((j) == 2) DG(xdst + XOFF(s), xvo, sx, wid * 16384u + cv_ * 128u, false, 2, g); } while (0)
#define QG4(s, j) do { QG(s, j, -1); asm volatile("s_nop 0"); QG(s, j, 0); QG(s, j, 1); QG(s, j, 2); QG(s, j, 3); } while (0)
#define QLD(s) do { QG4(s, 0); QG4(s, 1); QG4(s, 2); } while (0)
    const unsigned wa = hreg + wl;
    const unsigned xa = ldsb + qb * 4096u + l31 * 128u + ((hh ^ ((l31 >> 1) & 7u)) * 16u);
    f32x16 acc[4];
    bf16x8 xf[2][4], wf[2][4][4];
#define QM1(s, ks, g) do { if ((s) == 0 && (ks) == 0) asm volatile("v_mfma_f32_32x32x16_bf16 %0, %1, %2, 0" : "=&v"(acc[g]) : "v"(wf[(s) & 1][g][ks]), "v"(xf[(s) & 1][ks])); \
      else asm volatile("v_mfma_f32_32x32x16_bf16 %0, %1, %2, %0" : "+v"(acc[g]) : "v"(wf[(s) & 1][g][ks]), "v"(xf[(s) & 1][ks])); } while (0)
#define QOP(HOOK, NOP, G, v) do { if ((v) < (NOP) && ((v) * 16) / (NOP) == (G)) { HOOK((v) >> 2, (v) & 3); if (((v) & 3) == 3 && (v) + 1 < (NOP)) { HOOK(((v) >> 2) + 1, -1); } } } while (0)
#define QGAP(s, ks, g, HOOK, NOP) do { if ((s) > 0) QM1((s) - 1, ks, g); else asm volatile("s_nop 0"); \
        if ((g) == 0) xf[(s) & 1][ks] = lds_rd128((xa ^ (32u * (ks))) + XOFF(s)); \
        wf[(s) & 1][g][ks] = cat8(lds_tr((wa ^ (64u * (g))) + WOFF(s) + (ks) * 4096u), lds_tr((wa ^ (64u * (g))) + WOFF(s) + (ks) * 4096u + 1024u)); \
        SBAR(); \
        QOP(HOOK, NOP, 4 * (ks) + (g), 0); QOP(HOOK, NOP, 4 * (ks) + (g), 1); QOP(HOOK, NOP, 4 * (ks) + (g), 2); QOP(HOOK, NOP, 4 * (ks) + (g), 3); \
        QOP(HOOK, NOP, 4 * (ks) + (g), 4); QOP(HOOK, NOP, 4 * (ks) + (g), 5); QOP(HOOK, NOP, 4 * (ks) + (g), 6); QOP(HOOK, NOP, 4 * (ks) + (g), 7); \
        QOP(HOOK, NOP, 4 * (ks) + (g), 8); QOP(HOOK, NOP, 4 * (ks) + (g), 9); \
        SBAR(); } while (0)
#define QKS(s, ks, HOOK, NOP) do { QGAP(s, ks, 0, HOOK, NOP); QGAP(s, ks, 1, HOOK, NOP); QGAP(s, ks, 2, HOOK, NOP); QGAP(s, ks, 3, HOOK, NOP); } while (0)
#define QCMP(s, HOOK, NOP) do { HOOK(0, -1); SBAR(); QKS(s, 0, HOOK, NOP); QKS(s, 1, HOOK, NOP); QKS(s, 2, HOOK, NOP); QKS(s, 3, HOOK, NOP); } while (0)
#define HQ0(j, g) QG(3, j, g)
#define HQ1(j, g) QG(4, j, g)
#define HQ2(j, g) QG(5, j, g)
#define HQ3(j, g) QG(6, j, g)
#define HQ4(j, g) QG(7, j, g)
#define HQ5(j, g) EG(0, j, g)
#define HQ6(j, g) EG(1, j, g)
#define HQ7(j, g) EG(2, j, g)
    QLD(0); QLD(1);
#pragma unroll
    for (int db = 0; db < 4; ++db) { o[db] = f32x16{}; asm volatile("" : "+a"(o[db])); }
    QLD(2);
    waitv<2 * QE>(); BARRIER(); QCMP(0, HQ0, QE);
    waitv<2 * QE>(); BARRIER(); QCMP(1, HQ1, QE);
    waitv<2 * QE>(); BARRIER(); QCMP(2, HQ2, QE);
    waitv<2 * QE>(); BARRIER(); QCMP(3, HQ3, QE);
    waitv<2 * QE>(); BARRIER(); QCMP(4, HQ4, QE);
    waitv<2 * QE>(); BARRIER(); QCMP(5, HQ5, PE);
    waitv<QE + PE>(); BARRIER(); QCMP(6, HQ6, PE);
    waitv<2 * PE>(); BARRIER(); QCMP(7, HQ7, PE);
    QM1(7, 0, 0); QM1(7, 1, 0); QM1(7, 2, 0); QM1(7, 3, 0); SBAR();
    QM1(7, 0, 1); QM1(7, 1, 1); QM1(7, 2, 1); QM1(7, 3, 1); asm volatile("s_nop 7" : "+v"(acc[0])); SBAR();
    QM1(7, 0, 2); QM1(7, 1, 2); QO(0); SBAR(); QM1(7, 2, 2); QM1(7, 3, 2); PIN(acc[1]); SBAR();
    QM1(7, 0, 3); QM1(7, 1, 3); QO(1); SBAR(); QM1(7, 2, 3); QM1(7, 3, 3); PIN(acc[2]); SBAR();
    QO(2); SBAR(); asm volatile("s_nop 3" : "+v"(acc[3])); QO(3); SBAR();
  }
  PIN(epoch_v);
  const u64 tag = mix64(((u64)rfl((unsigned)(epoch_v >> 32)) << 32) | rfl((unsigned)epoch_v));

  unsigned tA = 3u * ESZ + dsub, tB = 2u * ESZ + dsub;
  const unsigned kb = ring + l31 * 256u + ((hh ^ l15) * 16u);
  const unsigned vl = (4u * hh + ((lane >> 2) & 3u)) * 256u + (((lane >> 2) & 3u) << 6) + (g4 & 1u) * 32u + (lane & 3u) * 8u;
  const unsigned vb = ring + vl;
  f32x16 sA[2], sB[2], negm;
  bf16x8 kf[2][8]; s16x4 vlo[4][4], vhi[4][4]; u32x4 pw[2][2];
  float fsc = 1.f, ls0 = 0.f, lt0 = 0.f, ls1 = 0.f, lt1 = 0.f;
#define QK0(D, a, bq) asm volatile("v_mfma_f32_32x32x16_bf16 %0, %1, %2, 0" : "=&v"(D) : "v"(a), "a"(bq))
#define QKC(D, a, bq, C) asm volatile("v_mfma_f32_32x32x16_bf16 %0, %1, %2, %3" : "=&v"(D) : "v"(a), "a"(bq), "v"(C))
#define QKA(D, a, bq) asm volatile("v_mfma_f32_32x32x16_bf16 %0, %1, %2, %0" : "+v"(D) : "v"(a), "a"(bq))
#define WAITE(k) do { waitv<PE * (k)>(); BARRIER(); } while (0)
#define EXP2(x) __builtin_amdgcn_exp2f((x) * C2)
#define KRD(s) do { const unsigned a_ = ka ^ (32u * (s)); kf[0][s] = lds_rd128(a_); kf[1][s] = lds_rd128(a_ + 8192u); } while (0)

  WAITE(2);
  {
    const unsigned ka = kb;
#pragma unroll
    for (int s = 0; s < 8; ++s) KRD(s);
  }
  QK0(sA[0], kf[0][0], qf[0]); QK0(sA[1], kf[1][0], qf[0]);
#pragma unroll
  for (int s = 1; s < 8; ++s) { QKA(sA[0], kf[0][s], qf[s]); QKA(sA[1], kf[1][s], qf[s]); if (s == 2) ELEM(3); }
  WAITE(2);
  ELEM(4);
  {
    const unsigned ka = kb + ESZ;
#pragma unroll
    for (int s = 0; s < 8; ++s) KRD(s);
  }
  asm volatile("s_nop 7\n\ts_nop 7" : "+v"(sA[0]), "+v"(sA[1]));
  {
    const float m0 = swap_max(max2f(max8hi(max8lo(sA[0]), sA[0]), max8hi(max8lo(sA[1]), sA[1])));
#pragma unroll
    for (int r = 0; r < 16; ++r) { sA[0][r] -= m0; sA[1][r] -= m0; negm[r] = -m0; }
    PIN(negm);
#pragma unroll
    for (int r = 0; r < 8; ++r) { sA[0][r] = EXP2(sA[0][r]); sA[1][r] = EXP2(sA[1][r]); }
#pragma unroll
    for (int r = 0; r < 8; r += 2) { ls0 += sA[0][r]; lt0 += sA[0][r + 1]; ls1 += sA[1][r]; lt1 += sA[1][r + 1]; }
  }
  unsigned wvo[4];
  const srd_t sW = MKSRD((BB ? p.woB : p.woA) + (size_t)h0 * 131072u + wid * 256u, (BB ? 262144 : 524288) - 768);
  const unsigned wbuf = BB ? hreg + qb * 8192u : ldsb + ESZ + wid * 8192u;
  const unsigned bhi = b >> 2;
#define WSO(i) ((((i) + b) & 3u) * 65536u + ((((i) >> 2) & 1u) ^ bhi) * 128u)
#define WG(i, hi, g) DG(wbuf + (1u + ((i) & 3)) * ESZ + (hi) * 4096u, wvo, sW, WSO(i) + (hi) * 32768u, NT_WO, 4, g)
#define WOP(v, TA, TB_) do { if ((v) < 8) WG(TA, ((v) >> 2) & 1, (v) & 3); else WG(TB_, ((v) >> 2) & 1, (v) & 3); \
    if ((v) == 3) WG(TA, 1, -1); if ((v) == 7 && (TB_) >= 0) WG((TB_) & 7, 0, -1); if ((v) == 11) WG((TB_) & 7, 1, -1); } while (0)

#define VRD(db, k4) do { const unsigned a_ = (vb ^ (64u * (db))) + S2 + 4096u * (k4); vlo[db][k4] = lds_tr(a_); vhi[db][k4] = lds_tr(a_ + 2048u); } while (0)
  s16x4 ulo[4][4], uhi[4][4];
#define URD(db, k4) do { const unsigned a_ = (vb ^ (64u * (db))) + 4096u * (k4); ulo[db][k4] = lds_tr(a_); uhi[db][k4] = lds_tr(a_ + 2048u); } while (0)
#define GAPA(C, N, s, KB, LAST) do { \
    if ((s) == 0) QKC(N[KB], kf[KB][0], qf[0], negm); else QKA(N[KB], kf[KB][s], qf[s]); \
    if ((s) < 4) { float e0_ = EXP2(C[KB][(8 + 2 * (s)) & 15]), e1_ = EXP2(C[KB][(9 + 2 * (s)) & 15]); PIN(e0_); PIN(e1_); C[KB][(8 + 2 * (s)) & 15] = e0_; C[KB][(9 + 2 * (s)) & 15] = e1_; \
      pw[KB][0][(s) & 3] = cvtpk(C[KB][2 * (s)], C[KB][2 * (s) + 1]); PIN(pw[KB][0]); } \
    else { ls##KB += C[KB][2 * (s)]; lt##KB += C[KB][2 * (s) + 1]; pw[KB][1][(s) & 3] = cvtpk(C[KB][2 * (s)], C[KB][2 * (s) + 1]); PIN(pw[KB][1]); PIN(ls##KB); PIN(lt##KB); } \
    if ((s) < 4) VRD((2 * (s) + KB) & 3, VK4(((s) >> 1) & 1)); else if ((s) < 6) { VRD(2 * KB, VK4(((s) & 1) + 2)); VRD(2 * KB + 1, VK4(((s) & 1) + 2)); } \
    SBAR(); \
    if (LAST) { if (BB && KB == 1) WOP(s, 0, -1); } \
    else if (KB == 1 && (BB || (s) < 4)) { dma1<((s) & 3) * 1024, NTS>(kvo[(s) & 3], sK, RO(tA) + ((s) >> 2) * 4096u); if (BB && (s) == 3) SETM0(dring + S1 + 4096u); } \
    SBAR(); } while (0)
#define VK4(i) ((i) == 0 ? 0 : (i) == 1 ? 2 : (i) == 2 ? 1 : 3)
#define PHASE_A(C, N, ODD, L) do { \
    constexpr unsigned S1 = (ODD ? 3u : 1u) * ESZ, S2 = (ODD ? 0u : 2u) * ESZ; \
    if (!BB && ASLP > 0) __builtin_amdgcn_s_sleep(ASLP); \
    if (L) { if (BB) WG(0, 0, -1); } else SETM0(dring + S1); \
    WAITE(2); SBAR(); \
    GAPA(C, N, 0, 0, L); GAPA(C, N, 0, 1, L); GAPA(C, N, 1, 0, L); GAPA(C, N, 1, 1, L); GAPA(C, N, 2, 0, L); GAPA(C, N, 2, 1, L); GAPA(C, N, 3, 0, L); GAPA(C, N, 3, 1, L); \
    GAPA(C, N, 4, 0, L); GAPA(C, N, 4, 1, L); GAPA(C, N, 5, 0, L); GAPA(C, N, 5, 1, L); GAPA(C, N, 6, 0, L); GAPA(C, N, 6, 1, L); GAPA(C, N, 7, 0, L); GAPA(C, N, 7, 1, L); \
    tA += ESZ; } while (0)
#define PVM(i) o[(i) & 3] = MFMA32(cat8(vlo[(i) & 3][VK4((i) >> 2)], vhi[(i) & 3][VK4((i) >> 2)]), __builtin_bit_cast(bf16x8, pw[((i) >> 2) & 1][(i) >> 3]), o[(i) & 3])
#define DMAB(v, LAST) do { SBAR(); if (LAST) { if (BB && (v) < 8) WOP((v) & 7, 1, -1); } \
    else if (((v) & 1) == 0 && (BB || (v) < 8)) { dma1<(((v) >> 1) & 3) * 1024, NTS>(vvo, sV, RO(tB) + ((v) >> 3) * 4096u); if (BB && (v) == 6) SETM0(dring + S2 + 4096u); } SBAR(); } while (0)
#define GAPE(N, j, LAST) do { PVM(8 + (j)); N[0][j] = EXP2(N[0][j]); N[1][j] = EXP2(N[1][j]); \
    if ((j) > 0) { if ((j) & 1) { ls0 += N[0][((j) + 15) & 15]; ls1 += N[1][((j) + 15) & 15]; } else { lt0 += N[0][((j) + 15) & 15]; lt1 += N[1][((j) + 15) & 15]; } } \
    if (LAST) { URD((2 * (j)) & 3, VK4((j) >> 1)); URD((2 * (j) + 1) & 3, VK4((j) >> 1)); } \
    PIN(N[0]); PIN(N[1]); PIN(ls0); PIN(ls1); PIN(lt0); PIN(lt1); DMAB(8 + (j), LAST); } while (0)
#define PHASE_B(N, ODD, LAST) do { \
    constexpr unsigned S2 = (ODD ? 0u : 2u) * ESZ, S3 = (ODD ? 1u : 3u) * ESZ; \
    if (LAST) { if (BB) WG(1, 0, -1); BARRIER(); asm volatile("s_nop 7"); } else { SETM0(dring + S2); WAITE(2); } SBAR(); \
    const unsigned ka = kb + S3; \
    PVM(0); KRD(0); DMAB(0, LAST); \
    PVM(1); KRD(1); DMAB(1, LAST); \
    PVM(2); KRD(2); float ma0 = max3f(N[0][0], N[0][1], N[0][2]), ma1 = max3f(N[1][0], N[1][1], N[1][2]), mb0 = max3f(N[0][3], N[0][4], N[0][5]), mb1 = max3f(N[1][3], N[1][4], N[1][5]); DMAB(2, LAST); \
    PVM(3); KRD(3); ma0 = max3f(ma0, N[0][6], N[0][7]); ma1 = max3f(ma1, N[1][6], N[1][7]); mb0 = max3f(mb0, N[0][8], N[0][9]); mb1 = max3f(mb1, N[1][8], N[1][9]); DMAB(3, LAST); \
    PVM(4); KRD(4); ma0 = max3f(ma0, N[0][10], N[0][11]); ma1 = max3f(ma1, N[1][10], N[1][11]); mb0 = max3f(mb0, N[0][12], N[0][13]); mb1 = max3f(mb1, N[1][12], N[1][13]); DMAB(4, LAST); \
    PVM(5); ma0 = max3f(ma0, N[0][14], N[0][15]); ma1 = max3f(ma1, N[1][14], N[1][15]); const float mbb = max2f(mb0, mb1); KRD(5); DMAB(5, LAST); \
    PVM(6); const float mall = max3f(ma0, ma1, mbb); KRD(6); const bool grew = __any(mall > THR); DMAB(6, LAST); \
    PVM(7); KRD(7); DMAB(7, LAST); \
    if (__builtin_expect(grew, 0)) {     \
      const float d0 = max2f(swap_max(mall), 0.f); \
      fsc = EXP2(-d0); ls0 *= fsc; lt0 *= fsc; ls1 *= fsc; lt1 *= fsc; \
      sub_tile(N[0], d0); sub_tile(N[1], d0); sub_tile(negm, d0); \
    } \
    if (LAST) { waitv<BB ? 16 : 24>(); asm volatile("s_barrier" ::: "memory"); }     \
    SBAR(); \
    GAPE(N, 0, LAST); GAPE(N, 1, LAST); GAPE(N, 2, LAST); GAPE(N, 3, LAST); GAPE(N, 4, LAST); GAPE(N, 5, LAST); GAPE(N, 6, LAST); GAPE(N, 7, LAST); \
    lt0 += N[0][7]; lt1 += N[1][7]; \
    if (__builtin_expect(grew, 0)) { \
      asm volatile("s_nop 7\n\ts_nop 7" ::: "memory"); \
      scale_acc(o[0], fsc); scale_acc(o[1], fsc); scale_acc(o[2], fsc); scale_acc(o[3], fsc); \
    } \
    tB += ESZ; } while (0)

  if (tid == 0u) __hip_atomic_store((GAS u64*)(p.ws + WS_FLAG3) + (size_t)blockIdx.x * 8u, tag, RLX_AGENT);
  for (unsigned t = 0; t < 14u; t += 2u) {
    if (t == CPF1 && idx == 0u) {
      u64 pc0; unsigned clen;
      if constexpr (BB) asm volatile("s_getpc_b64 %0\n.Lk1_b0:\n\ts_mov_b32 %1, .Lk1_b1-.Lk1_b0" : "=s"(pc0), "=s"(clen));
      else              asm volatile("s_getpc_b64 %0\n.Lk1_a0:\n\ts_mov_b32 %1, .Lk1_a1-.Lk1_a0" : "=s"(pc0), "=s"(clen));
      const srd_t sC = MKSRD(pc0, (int)(clen <= 65536u ? (clen & ~3u) : 0u));
      TOUCH(sC, wid * 8192u);
    }
    PHASE_A(sA, sB, false, false); PHASE_B(sB, false, false);
    PHASE_A(sB, sA, true, false);  PHASE_B(sA, true, false);
  }
#pragma unroll
  for (int j = 0; j < 4; ++j) wvo[j] = BB ? (lane >> 3) * 1024u + (((lane & 7u) ^ ((g4 & 1u) << 2)) * 16u) + j * 7168u : g4 * 1024u + ((l15 ^ (g4 << 2)) * 16u) + j * 3072u;
  constexpr unsigned GPITCH16 = 16u * 1024u, GJ2 = 2048u;
  unsigned gidx = idx; asm volatile("" : "+s"(gidx));
  const srd_t gsr = sW; const unsigned (&gvo)[4] = wvo;
  PHASE_A(sA, sB, false, true);
  if (!BB) { GD8(0); GD8(1); GD8(2); }
  PHASE_B(sB, false, true);
  bf16x8 wfr[2][8];
  const unsigned wb = wbuf + (4u * hh + q4) * 128u + (q4 >> 1) * 64u + (g4 & 1u) * 32u + p4 * 8u;
#define WRD(i, x) do { const unsigned a_ = (wb ^ (64u * ((x) & 1))) + (1u + ((i) & 3)) * ESZ + 2048u * ((x) >> 1); wfr[(i) & 1][x] = cat8(lds_tr(a_), lds_tr(a_ + 1024u)); } while (0)
  {
    PIN(tch);
#define PINA(x) asm volatile("" : "+a"(x))
#define UM(db, kb_, ks, v) do { o[db] = MFMA32(cat8(ulo[db][2 * (kb_) + (ks)], uhi[db][2 * (kb_) + (ks)]), __builtin_bit_cast(bf16x8, pw[kb_][ks]), o[db]); PINA(o[db]); SBAR(); \
      if (BB) WOP(v, 2, 3); SBAR(); } while (0)
#pragma unroll
    for (int i = 0; i < 4; ++i) { pw[0][0][i] = cvtpk(sB[0][2 * i], sB[0][2 * i + 1]); pw[1][0][i] = cvtpk(sB[1][2 * i], sB[1][2 * i + 1]); }
    if (BB) { WG(2, 0, -1); BARRIER(); }
    SBAR();
#define UG(x) do { UM((x) & 3, (x) >> 2, 0, x); float e0_ = EXP2(sB[(x) >> 2][8 + 2 * ((x) & 3)]), e1_ = EXP2(sB[(x) >> 2][9 + 2 * ((x) & 3)]); ls0 += e0_; ls1 += e1_; \
      pw[(x) >> 2][1][(x) & 3] = cvtpk(e0_, e1_); PIN(pw[(x) >> 2][1]); PIN(ls0); PIN(ls1); SBAR(); } while (0)
    UG(0); UG(1); UG(2); UG(3); UG(4); UG(5); UG(6); UG(7);
    float inv = __builtin_amdgcn_rcpf(swap_add((ls0 + lt0) + (ls1 + lt1))); PIN(inv);
#define OWR(db) do { const f32x16 y_ = o[db] * inv; lds_wru4(wbuf + (2 * (db)) * 1024u + lane * 16u, pack8(y_, 0)); lds_wru4(wbuf + (2 * (db) + 1) * 1024u + lane * 16u, pack8(y_, 1)); } while (0)
    UM(0, 0, 1, 8); UM(0, 1, 1, 9); UM(1, 0, 1, 10); UM(1, 1, 1, 11); UM(2, 0, 1, 12); UM(2, 1, 1, 13);
    OWR(0); SBAR();
    UM(3, 0, 1, 14); UM(3, 1, 1, 15);
    if (BB) {
      waitv<24>();
#pragma unroll
      for (int i = 0; i < 8; ++i) WRD(0, i);
    }
    OWR(1); OWR(2); OWR(3);
    BARRIER();
  }
  constexpr unsigned br = BB ? 1u : 0u;
  const srd_t sP = MKSRD(p.ws + WS_PART, 16 << 20);
  GAS u64* flags = (GAS u64*)(p.ws + WS_FLAG);
  const unsigned hp = BB ? (h0 >> 1) : g8;
  const unsigned fgrp = BB ? wid * 16u : (qb * 4u + wid) * 8u;
  if constexpr (!BB) {
    f32x16 acc[4]; bf16x8 gw[2][8], gb[2][2];
    const unsigned ga = gbuf + vl;
#define GB(i) do { const unsigned a_ = ldsb + ESZ + GT(i) * 2048u + lane * 16u; gb[(i) & 1][0] = lds_rd128(a_); gb[(i) & 1][1] = lds_rd128(a_ + 1024u); } while (0)
    waitv<16>();
    GFIRST();
    GTILE(0, 0); GTILE(1, 0); GTILE(2, 0); GTILE(3, 0); GTILE(4, 0); GTILE(5, 0); GTILE(6, 0); GTILE(7, 0);
    GTILE(8, 0); GTILE(9, 0); GTILE(10, 0); GTILE(11, 0); GTILE(12, 0); GTILE(13, 0); GTILE(14, 0); GTILE(15, 0);
    asm volatile("s_nop 7\n\ts_nop 7" : "+v"(acc[0]), "+v"(acc[1]), "+v"(acc[2]), "+v"(acc[3]));
    const unsigned pb = b * 1048576u + (qb * 32u + 8u * wid) * 16384u + hp * 1024u + lane * 16u;
#pragma unroll
    for (int i = 0; i < 8; ++i) __builtin_amdgcn_raw_buffer_store_b128(pack8(acc[i >> 1], i & 1), sP, pb + i * 16384u, 0, 16);
  } else {
    bf16x8 of[4][2][4];
#define ORD(i, x) do { const unsigned r_ = ((i) + b) & 3u; of[i][(x) & 1][(x) >> 1] = lds_rd128(ldsb + (r_ >> 1) * HREG + (r_ & 1u) * 4096u + lane * 16u + ((x) & 1) * 8192u + ((x) >> 1) * 1024u); } while (0)
    ORD(0, 0); ORD(0, 1); ORD(0, 2); ORD(0, 3); ORD(0, 4); ORD(0, 5); ORD(0, 6); ORD(0, 7);
    f32x16 acc[4][2];
    const f32x16 z16 = {};
    const unsigned pb = (br * 8u + b) * 1048576u + 8u * wid * 16384u + hp * 1024u + lane * 16u;
    const unsigned pb0 = pb + bhi * 65536u, pb1 = pb + (bhi ^ 1u) * 65536u;
#define PST(nb, q, s2) __builtin_amdgcn_raw_buffer_store_b128(pack8(acc[nb][q], s2), sP, (((nb) >> 1) ? pb1 : pb0) + ((q) * 32 + ((nb) & 1) * 2 + (s2)) * 16384u, 0, 16)
#define OM(i, x, q) do { acc[2 * ((i) >> 2) + ((x) & 1)][q] = MFMA32(wfr[(i) & 1][x], of[(i) & 3][q][(x) >> 1], (((i) & 3) == 0 && (x) < 2) ? z16 : acc[2 * ((i) >> 2) + ((x) & 1)][q]); \
      PINA(acc[2 * ((i) >> 2) + ((x) & 1)][q]); } while (0)
#define OX(i, x) do { \
      OM(i, x, 0); WRD((i) + 1, x); if ((i) < 3) ORD(((i) + 1) & 3, x); SBAR(); \
      if ((i) < 4) WOP(x, ((i) + 4) & 7, -1); \
      SBAR(); OM(i, x, 1); if ((i) >= 4 && ((x) & 1)) PST((i) & 1, ((x) >> 2) & 1, ((x) >> 1) & 1); SBAR(); } while (0)
#define OT(i, N) do { if ((i) < 4) WG(((i) + 4) & 7, 0, -1); waitv<N>(); if ((i) > 0) { WAITL(); } SBAR(); \
      OX(i, 0); OX(i, 1); OX(i, 2); OX(i, 3); OX(i, 4); OX(i, 5); OX(i, 6); OX(i, 7); } while (0)
    OT(0, 16); OT(1, 16); OT(2, 16); OT(3, 16); OT(4, 16); OT(5, 12);
    waitv<8>(); SBAR();
#pragma unroll
    for (int i = 0; i < 8; ++i) WRD(7, (2 * i + (i >> 2)) & 7);
    SBAR();
#define OF(n, q, pn, pq) do { \
      OM(6, n, q); OM(6, 2 + (n), q); OM(6, 4 + (n), q); SBAR(); OM(6, 6 + (n), q); if ((pn) >= 0) PST(2 + ((pn) & 1), pq, 0); SBAR(); \
      OM(7, n, q); OM(7, 2 + (n), q); SBAR(); OM(7, 4 + (n), q); if ((pn) >= 0) PST(2 + ((pn) & 1), pq, 1); SBAR(); OM(7, 6 + (n), q); SBAR(); } while (0)
    OF(0, 0, -1, 0); OF(0, 1, 0, 0); OF(1, 0, 0, 1); OF(1, 1, 1, 0);
    PST(3, 1, 0); PST(3, 1, 1);
  }
  waitv<0>();
  if (lane == 0) __hip_atomic_store(flags + (size_t)((br * 8u + b) * 64u + fgrp + hp) * 8u, tag, RLX_AGENT);
  {
    constexpr int NP = BB ? 16 : 8;
    const unsigned u = BB ? hp * 4u + wid : qb * 32u + hp * 4u + wid;
    poll_flags<2>(flags + (size_t)((br * 8u + b) * 64u + (BB ? ((u >> 3) & 3u) * 16u + l15 : (u & 56u) + (lane & 7u))) * 8u, tag);
    const unsigned rb = (br * 8u + b) * 1048576u + u * 16384u + lane * 16u;
    u32x4 pv[NP];
#pragma unroll
    for (int j = 0; j < NP; ++j) pv[j] = __builtin_amdgcn_raw_buffer_load_b128(sP, rb + j * 1024u, 0, 16);
    float y[8] = {0.f, 0.f, 0.f, 0.f, 0.f, 0.f, 0.f, 0.f};
#pragma unroll
    for (int j = 0; j < NP; ++j)
#pragma unroll
      for (int e = 0; e < 4; ++e) { y[2 * e] += __uint_as_float(pv[j][e] << 16); y[2 * e + 1] += __uint_as_float(pv[j][e] & 0xffff0000u); }
    const auto r0 = __builtin_amdgcn_permlane32_swap(cvtpk(y[0], y[1]), cvtpk(y[4], y[5]), false, false);
    const auto r1 = __builtin_amdgcn_permlane32_swap(cvtpk(y[2], y[3]), cvtpk(y[6], y[7]), false, false);
    *(u32x4*)(p.out + (size_t)(b * 64u + (u >> 5) * 32u + l31) * 2048u + (br * 512u + (u & 31u) * 16u + 8u * hh) * 2u) = u32x4{r0[0], r1[0], r0[1], r1[1]};
    if (!BB && blockIdx.x == 128u && wid == 0u) {
#pragma unroll 1
      for (unsigned spins = 0; spins < (1u << 21); ++spins) {
        bool ok = true;
#pragma unroll
        for (int k = 0; k < 4; ++k) ok &= (__hip_atomic_load((GAS u64*)(p.ws + WS_FLAG3) + (size_t)(k * 64 + lane) * 8u, RLX_AGENT) == tag);
        if (__all(ok)) break;
        __builtin_amdgcn_s_sleep(8);
      }
      if (lane == 0) __hip_atomic_store((GAS u64*)(p.ws + WS_EPOCH), tag, RLX_AGENT);
    }
  }
  if constexpr (BB) asm volatile(".Lk1_b1:"); else asm volatile(".Lk1_a1:");
}

__global__ __launch_bounds__(256, 1) void k1_attn(P1 p) {
  extern __shared__ __attribute__((aligned(16))) char lds[];
  const unsigned ldsb = (unsigned)(unsigned long)lds;
  if (blockIdx.x < 128u) body<true>(p, ldsb); else body<false>(p, ldsb);
}

extern "C" void kernel_launch(void* const* d_in, const int* in_sizes, int n_in,
                              void* d_out, int out_size, void* d_ws, size_t ws_size,
                              hipStream_t stream) {
  (void)in_sizes; (void)n_in; (void)out_size; (void)ws_size;
  (void)hipFuncSetAttribute((const void*)k1_attn, hipFuncAttributeMaxDynamicSharedMemorySize, K1_LDS);
  P1 p1;
  p1.xA = (const char*)d_in[0]; p1.kA = (const char*)d_in[1]; p1.vA = (const char*)d_in[2]; p1.wqA = (const char*)d_in[3]; p1.woA = (const char*)d_in[4];
  p1.xB = (const char*)d_in[5]; p1.kB = (const char*)d_in[6]; p1.vB = (const char*)d_in[7]; p1.wqB = (const char*)d_in[8]; p1.woB = (const char*)d_in[9];
  p1.ws = (char*)d_ws; p1.out = (char*)d_out;
  hipLaunchKernelGGL(k1_attn, dim3(256), dim3(256), K1_LDS, stream, p1);
}
```

```cpp
#include <hip/hip_runtime.h>
#include <hip/hip_bf16.h>

typedef float    f32x16 __attribute__((ext_vector_type(16)));
typedef float    f32x2  __attribute__((ext_vector_type(2)));
typedef __bf16   bf16x8 __attribute__((ext_vector_type(8)));
typedef __bf16   bf16x2 __attribute__((ext_vector_type(2)));
typedef short    s16x4  __attribute__((ext_vector_type(4)));
typedef unsigned u32x4  __attribute__((ext_vector_type(4)));
typedef unsigned long long u64;
typedef __amdgpu_buffer_rsrc_t srd_t;
#define DI __device__ __forceinline__
#define LAS __attribute__((address_space(3)))
#define GAS __attribute__((address_space(1)))
#define SBAR() __builtin_amdgcn_sched_barrier(0)
#define PIN(x) asm volatile("" : "+v"(x))
#define MFMA32(a, b, c) __builtin_amdgcn_mfma_f32_32x32x16_bf16(a, b, c, 0, 0, 0)
#define RLX_AGENT __ATOMIC_RELAXED, __HIP_MEMORY_SCOPE_AGENT
#define MKSRD(ptr, n) __builtin_amdgcn_make_buffer_rsrc((void*)(ptr), (short)0, (n), 0x00020000)

constexpr int K1_LDS = 163840;
constexpr float C2 = 0.08838834764831845f * 1.4426950408889634f;
constexpr float THR = 8.0f / C2;
constexpr size_t WS_PART = 0;
constexpr size_t WS_FLAG = 16u << 20;
constexpr size_t WS_FLAG3 = WS_FLAG + 65536;
constexpr size_t WS_EPOCH = WS_FLAG3 + 16384;

DI unsigned rfl(unsigned x) { return __builtin_amdgcn_readfirstlane(x); }
DI unsigned cvtpk(float lo, float hi) { f32x2 v = {lo, hi}; bf16x2 b = __builtin_convertvector(v, bf16x2); return __builtin_bit_cast(unsigned, b); }
DI bf16x8 lds_rd128(unsigned a) { return *(const LAS bf16x8*)(unsigned long)a; }
DI void lds_wru4(unsigned a, u32x4 v) { *(LAS u32x4*)(unsigned long)a = v; }
DI s16x4 lds_tr(unsigned a) { return __builtin_amdgcn_ds_read_tr16_b64_v4i16((LAS s16x4*)(unsigned long)a); }
DI bf16x8 cat8(s16x4 lo, s16x4 hi) { return __builtin_bit_cast(bf16x8, __builtin_shufflevector(lo, hi, 0, 1, 2, 3, 4, 5, 6, 7)); }
DI float max3f(float a, float b, float c) { float r; asm("v_max3_f32 %0, %1, %2, %3" : "=v"(r) : "v"(a), "v"(b), "v"(c)); return r; }
DI float max2f(float a, float b) { float r; asm("v_max_f32_e32 %0, %1, %2" : "=v"(r) : "v"(a), "v"(b)); return r; }
DI float swap_max(float m) { auto rr = __builtin_amdgcn_permlane32_swap(__float_as_uint(m), __float_as_uint(m), false, false); return max2f(__uint_as_float(rr[0]), __uint_as_float(rr[1])); }
DI float swap_add(float m) { auto rr = __builtin_amdgcn_permlane32_swap(__float_as_uint(m), __float_as_uint(m), false, false); return __uint_as_float(rr[0]) + __uint_as_float(rr[1]); }
DI float max8lo(const f32x16& p) { float a = max3f(p[0], p[1], p[2]), b = max3f(p[3], p[4], p[5]); return max3f(max3f(a, p[6], p[7]), b, b); }
DI float max8hi(float m, const f32x16& p) { float a = max3f(m, p[8], p[9]), b = max3f(p[10], p[11], p[12]); a = max3f(a, p[13], p[14]); return max3f(a, b, p[15]); }
DI void scale_acc(f32x16& x, float f) {
#pragma unroll
  for (int r = 0; r < 16; ++r) { float e = x[r], t;
    asm volatile("v_accvgpr_read_b32 %1, %0\n\tv_mul_f32 %1, %1, %2\n\tv_accvgpr_write_b32 %0, %1" : "+a"(e), "=&v"(t) : "v"(f));
    x[r] = e; }
}
DI void sub_tile(f32x16& x, float d) {
#pragma unroll
  for (int r = 0; r < 16; ++r) { float e = x[r]; asm volatile("v_sub_f32_e32 %0, %0, %1" : "+v"(e) : "v"(d)); x[r] = e; }
}
DI u64 mix64(u64 z) { z += 0x9E3779B97F4A7C15ull; z = (z ^ (z >> 30)) * 0xBF58476D1CE4E5B9ull; z = (z ^ (z >> 27)) * 0x94D049BB133111EBull; return z ^ (z >> 31); }


#define SETM0(x) asm volatile("s_mov_b32 m0, %0" :: "s"(x) : "memory")
#define SETM0N(x) asm volatile("s_mov_b32 m0, %0\n\ts_nop 0" :: "s"(x) : "memory")
template <int OFF, bool NT> DI void dma2(unsigned vo0, unsigned vo1, srd_t srd, unsigned soff) {
  if constexpr (NT)
    asm volatile("buffer_load_dwordx4 %0, %2, %3 offen offset:%4 nt lds\n\tbuffer_load_dwordx4 %1, %2, %3 offen offset:%5 nt lds"
                 :: "v"(vo0), "v"(vo1), "s"(srd), "s"(soff), "i"(OFF), "i"(OFF + 1024) : "memory");
  else
    asm volatile("buffer_load_dwordx4 %0, %2, %3 offen offset:%4 lds\n\tbuffer_load_dwordx4 %1, %2, %3 offen offset:%5 lds"
                 :: "v"(vo0), "v"(vo1), "s"(srd), "s"(soff), "i"(OFF), "i"(OFF + 1024) : "memory");
}
template <int OFF, bool NT> DI void dma1(unsigned vo, srd_t srd, unsigned soff) {
  if constexpr (NT) asm volatile("buffer_load_dwordx4 %0, %1, %2 offen offset:%3 nt lds" :: "v"(vo), "s"(srd), "s"(soff), "i"(OFF) : "memory");
  else asm volatile("buffer_load_dwordx4 %0, %1, %2 offen offset:%3 lds" :: "v"(vo), "s"(srd), "s"(soff), "i"(OFF) : "memory");
}
template <bool NT> DI void dma4(unsigned m0v, const unsigned (&vo)[4], srd_t srd, unsigned soff) { SETM0N(m0v); dma2<0, NT>(vo[0], vo[1], srd, soff); dma2<2048, NT>(vo[2], vo[3], srd, soff); }
template <int N> DI void waitv() { asm volatile("s_waitcnt vmcnt(%0)" :: "i"(N > 63 ? 63 : N) : "memory"); }
#define WAITL() do { asm volatile("" ::: "memory"); __builtin_amdgcn_s_waitcnt(0xC07F); asm volatile("" ::: "memory"); } while (0)
#define BARRIER() do { WAITL(); asm volatile("s_barrier" ::: "memory"); } while (0)

template <int SLP> DI void poll_flags(GAS u64* f, u64 tag) {
#pragma unroll 1
  for (unsigned spins = 0; spins < (1u << 21); ++spins) {
    const bool ok = (__hip_atomic_load(f, RLX_AGENT) == tag);
    if (__all(ok)) break;
    __builtin_amdgcn_s_sleep(SLP);
  }
  asm volatile("" ::: "memory");
}

constexpr int ADELAY = 40;
constexpr int ASLP = 2;
constexpr int TCH_KV = 2;
constexpr unsigned CPF1 = 10u;
constexpr bool NT_WO = false;

struct P1 {
  const char *xA, *kA, *vA, *wqA, *woA, *xB, *kB, *vB, *wqB, *woB;
  char* ws;
  char* out;
};

DI u32x4 pack8(const f32x16& x, int s) {
  return u32x4{cvtpk(x[8 * s], x[8 * s + 1]), cvtpk(x[8 * s + 2], x[8 * s + 3]), cvtpk(x[8 * s + 4], x[8 * s + 5]), cvtpk(x[8 * s + 6], x[8 * s + 7])};
}

constexpr unsigned ESZ = 16384u;
constexpr unsigned HREG = 5u * ESZ;

template <bool BB> DI void body(const P1& p, const unsigned ldsb) {
  constexpr int PE = BB ? 8 : 4;
  constexpr bool NTS = true;
  const unsigned tid = threadIdx.x, lane = tid & 63u, wid = rfl(tid >> 6);
  const unsigned g8 = blockIdx.x & 7u, idx = (blockIdx.x >> 3) & 15u;
  const unsigned qb = BB ? (wid & 1u) : (idx & 1u), item = wid >> 1;
  const unsigned h0 = BB ? 4u * g8 + 2u * (idx & 1u) : 4u * g8, b = idx >> 1;
  const unsigned l31 = lane & 31u, hh = lane >> 5, l15 = lane & 15u, g4 = lane >> 4;
  const unsigned hreg = ldsb + item * HREG;
  const unsigned ring = BB ? hreg : ldsb;
  const unsigned dsub = BB ? qb * 8192u : wid * 4096u;

  u64 epoch_v;
  asm volatile("global_load_dwordx2 %0, %1, off sc1" : "=v"(epoch_v) : "v"(p.ws + WS_EPOCH) : "memory");
  const size_t kvoff = (BB ? (size_t)(b * 32u + h0 + item) : (size_t)(b * 8u + g8)) * 262144u;
  const srd_t sK = MKSRD((BB ? p.kB : p.kA) + kvoff, 262144), sV = MKSRD((BB ? p.vB : p.vA) + kvoff, 262144);
  unsigned kvo[4];
#pragma unroll
  for (int i = 0; i < 4; ++i) kvo[i] = g4 * 256u + (((l15 ^ g4) ^ (4u * i)) * 16u);
  const unsigned vvo = g4 * 256u + ((l15 ^ (g4 << 2)) * 16u);
  const unsigned vv4[4] = {vvo, vvo, vvo, vvo};
  const unsigned dring = ring + dsub;
  const unsigned rr = ((BB ? (idx * 2u + item) * 3u + g8 : b * 2u + g8 * 5u + 1u) & 15u) * ESZ;
#define RO(x) ((((x) + rr) & 0x3ffffu) | ((x) & 0x40000u))
#define DG(m0v, vo, srd, soff, NT, cnt, g) do { if ((g) < 0) SETM0(m0v); else if ((g) < (cnt)) dma1<((g) & 3) * 1024, NT>(vo[(g) & 3], srd, soff); } while (0)
#define EG(n, j, g) do { if ((n) < 2 || ((n) & 1)) DG(dring + ((n) & 3) * ESZ + (j) * 4096u, kvo, sK, RO(((n) < 2 ? (n) : ((n) + 1) / 2) * ESZ + dsub) + (j) * 4096u, NTS, 4, g); \
                         else DG(dring + ((n) & 3) * ESZ + (j) * 4096u, vv4, sV, RO(((n) - 2) / 2 * ESZ + dsub) + (j) * 4096u, NTS, 4, g); } while (0)
#define EG4(n, j) do { EG(n, j, -1); asm volatile("s_nop 0"); EG(n, j, 0); EG(n, j, 1); EG(n, j, 2); EG(n, j, 3); } while (0)
#define ELEM(n) do { EG4(n, 0); if (BB) EG4(n, 1); } while (0)

  const unsigned gbuf = ldsb + 4u * ESZ + wid * 24576u;
#define GT(i) (((i) + gidx) & 15u)
#define GD(i, hi, g) DG(gbuf + ((i) % 3) * 8192u + (hi) * 4096u, gvo, gsr, (GT(i) * 2u + (hi)) * GPITCH16, false, 4, g)
#define GD8(i) do { GD(i, 0, -1); asm volatile("s_nop 0"); GD(i, 0, 0); GD(i, 0, 1); GD(i, 0, 2); GD(i, 0, 3); GD(i, 1, -1); asm volatile("s_nop 0"); GD(i, 1, 0); GD(i, 1, 1); GD(i, 1, 2); GD(i, 1, 3); } while (0)
#define GW(i, x) gw[(i) & 1][x] = cat8(lds_tr((ga ^ (64u * ((x) & 3))) + ((i) % 3) * 8192u + ((x) >> 2) * 4096u), lds_tr((ga ^ (64u * ((x) & 3))) + ((i) % 3) * 8192u + ((x) >> 2) * 4096u + GJ2))
#define GX(i, x) do { \
      if ((i) == 0 && (x) < 4) asm volatile("v_mfma_f32_32x32x16_bf16 %0, %1, %2, 0" : "=&v"(acc[(x) & 3]) : "v"(gw[(i) & 1][x]), "v"(gb[(i) & 1][(x) >> 2])); \
      else asm volatile("v_mfma_f32_32x32x16_bf16 %0, %1, %2, %0" : "+v"(acc[(x) & 3]) : "v"(gw[(i) & 1][x]), "v"(gb[(i) & 1][(x) >> 2])); \
      if ((i) < 15) { GW((i) + 1, x); if ((x) == 0) GB((i) + 1); } SBAR(); \
      if ((i) < 13) { GD((i) + 3, (x) >> 2, (x) & 3); if ((x) == 3) GD((i) + 3, 1, -1); } SBAR(); } while (0)
#define GTILE(i, XTRA) do { if ((i) < 13) GD((i) + 3, 0, -1); waitv<((i) < 14 ? 8 : 0) + (XTRA)>(); WAITL(); SBAR(); \
      GX(i, 0); GX(i, 1); GX(i, 2); GX(i, 3); GX(i, 4); GX(i, 5); GX(i, 6); GX(i, 7); } while (0)
#define GFIRST() do { GB(0); GW(0, 0); GW(0, 1); GW(0, 2); GW(0, 3); GW(0, 4); GW(0, 5); GW(0, 6); GW(0, 7); } while (0)

  bf16x8 qf[8];
  f32x16 o[4];
  unsigned tch = 0u;
#define TOUCH(srd, soff) asm volatile("buffer_load_dword %0, %1, %2, %3 offen" : "+v"(tch) : "v"(lane * 128u), "s"(srd), "s"(soff) : "memory")
#define QO(g) do { qf[2 * (g)] = __builtin_bit_cast(bf16x8, pack8(acc[g], 0)); qf[2 * (g) + 1] = __builtin_bit_cast(bf16x8, pack8(acc[g], 1)); asm volatile("" : "+a"(qf[2 * (g)]), "+a"(qf[2 * (g) + 1])); } while (0)
  const unsigned p4 = lane & 3u, q4 = (lane >> 2) & 3u;
  const unsigned wl = (8u * hh + q4) * 256u + (q4 << 6) + (g4 & 1u) * 32u + (((p4 & 1u) << 1) | (p4 >> 1)) * 8u;
  if constexpr (!BB) {
    constexpr unsigned GPITCH16 = 16u * 8192u, GJ2 = 1024u;
    const unsigned gidx = idx;
    const srd_t gsr = MKSRD(p.wqA + (h0 + wid) * 256u, 4 << 20), sx = MKSRD(p.xA + (size_t)(b * 64u + qb * 32u) * 1024u, 32768);
    unsigned gvo[4], xvo[4];
#pragma unroll
    for (int i = 0; i < 4; ++i) { gvo[i] = g4 * 8192u + ((l15 ^ (g4 << 2)) * 16u) + i * (32768u - 1024u); xvo[i] = g4 * 1024u + (((l15 ^ g4) ^ (4u * i)) * 16u) + i * 3072u; }
    TOUCH(MKSRD(p.xB, 524288), (idx * 4u + wid) * 8192u);
    if (ADELAY > 0) __builtin_amdgcn_s_sleep(ADELAY);
    dma4<false>(ldsb + 2u * ESZ + wid * 8192u, xvo, sx, wid * 256u); dma4<false>(ldsb + 2u * ESZ + wid * 8192u + 4096u, xvo, sx, wid * 256u + 16384u);
    GD8(0); GD8(1);
#pragma unroll
    for (int db = 0; db < 4; ++db) { o[db] = f32x16{}; asm volatile("" : "+a"(o[db])); }
    GD8(2);
#pragma unroll
    for (int i = 0; i < TCH_KV; ++i) TOUCH(item ? sV : sK, RO((4u * i + 2u * qb + (wid & 1u)) * 8192u));
    const unsigned ga = gbuf + wl, xb = ldsb + 2u * ESZ + l31 * 256u + ((hh ^ l15) * 16u);
    f32x16 acc[4]; bf16x8 gw[2][8], gb[2][2];
#define GB(i) do { const unsigned a_ = (xb ^ ((GT(i) & 3u) * 64u)) + (GT(i) >> 2) * 8192u; gb[(i) & 1][0] = lds_rd128(a_); gb[(i) & 1][1] = lds_rd128(a_ ^ 32u); } while (0)
    waitv<16 + TCH_KV>(); BARRIER();
    GFIRST();
    GTILE(0, TCH_KV); GTILE(1, TCH_KV); GTILE(2, 0); GTILE(3, 0); GTILE(4, 0); GTILE(5, 0); GTILE(6, 0); GTILE(7, 0);
    GTILE(8, 0); GTILE(9, 0); GTILE(10, 0); GTILE(11, 0); GTILE(12, 0);
    ELEM(0); ELEM(1);
    GTILE(13, 8); GTILE(14, 8);
    BARRIER();
    ELEM(2);
    GTILE(15, 12);
#undef GB
    asm volatile("s_nop 7\n\ts_nop 7" : "+v"(acc[0]), "+v"(acc[1]), "+v"(acc[2]), "+v"(acc[3]));
    QO(0); QO(1); QO(2); QO(3); SBAR();
  } else {
    constexpr int QE = 10;
    const unsigned rot = b + 4u * (idx & 1u);
    const srd_t sw = MKSRD(p.wqB + h0 * 256u, 4 << 20), sx = MKSRD(p.xB + (size_t)b * 65536u, 65536);
    unsigned wvo[4], xvo[4];
#pragma unroll
    for (int i = 0; i < 4; ++i) {
      wvo[i] = g4 * 8192u + ((l15 ^ (g4 << 2)) * 16u) + i * (32768u - 1024u);
      xvo[i] = (lane >> 3) * 1024u + (((lane & 7u) ^ ((4u * i + g4) & 7u)) * 16u) + i * (8192u - 1024u);
    }
#define WOFF(s) (((s) & 3) * ESZ)
#define XOFF(s) ((((s) >> 1) & 1) * HREG + 4u * ESZ + ((s) & 1) * 8192u)
    const unsigned wdst = ldsb + wid * 4096u, xdst = ldsb + wid * 2048u;
#define QG(s, j, g) do { const unsigned cv_ = ((s) + rot) & 7u; \
      if ((j) == 0) DG(wdst + WOFF(s), wvo, sw, (cv_ * 64u + wid * 16u) * 8192u, false, 4, g); \
      if ((j) == 1) DG(wdst + WOFF(s) + HREG, wvo, sw, (cv_ * 64u + wid * 16u) * 8192u + 256u, false, 4, g); \
      if ((j) == 2) DG(xdst + XOFF(s), xvo, sx, wid * 16384u + cv_ * 128u, false, 2, g); } while (0)
#define QG4(s, j) do { QG(s, j, -1); asm volatile("s_nop 0"); QG(s, j, 0); QG(s, j, 1); QG(s, j, 2); QG(s, j, 3); } while (0)
#define QLD(s) do { QG4(s, 0); QG4(s, 1); QG4(s, 2); } while (0)
    const unsigned wa = hreg + wl;
    const unsigned xa = ldsb + qb * 4096u + l31 * 128u + ((hh ^ ((l31 >> 1) & 7u)) * 16u);
    f32x16 acc[4];
    bf16x8 xf[2][4], wf[2][4][4];
#define QM1(s, ks, g) do { if ((s) == 0 && (ks) == 0) asm volatile("v_mfma_f32_32x32x16_bf16 %0, %1, %2, 0" : "=&v"(acc[g]) : "v"(wf[(s) & 1][g][ks]), "v"(xf[(s) & 1][ks])); \
      else asm volatile("v_mfma_f32_32x32x16_bf16 %0, %1, %2, %0" : "+v"(acc[g]) : "v"(wf[(s) & 1][g][ks]), "v"(xf[(s) & 1][ks])); } while (0)
#define QOP(HOOK, NOP, G, v) do { if ((v) < (NOP) && ((v) * 16) / (NOP) == (G)) { HOOK((v) >> 2, (v) & 3); if (((v) & 3) == 3 && (v) + 1 < (NOP)) { HOOK(((v) >> 2) + 1, -1); } } } while (0)
#define QGAP(s, ks, g, HOOK, NOP) do { if ((s) > 0) QM1((s) - 1, ks, g); else asm volatile("s_nop 0"); \
        if ((g) == 0) xf[(s) & 1][ks] = lds_rd128((xa ^ (32u * (ks))) + XOFF(s)); \
        wf[(s) & 1][g][ks] = cat8(lds_tr((wa ^ (64u * (g))) + WOFF(s) + (ks) * 4096u), lds_tr((wa ^ (64u * (g))) + WOFF(s) + (ks) * 4096u + 1024u)); \
        SBAR(); \
        QOP(HOOK, NOP, 4 * (ks) + (g), 0); QOP(HOOK, NOP, 4 * (ks) + (g), 1); QOP(HOOK, NOP, 4 * (ks) + (g), 2); QOP(HOOK, NOP, 4 * (ks) + (g), 3); \
        QOP(HOOK, NOP, 4 * (ks) + (g), 4); QOP(HOOK, NOP, 4 * (ks) + (g), 5); QOP(HOOK, NOP, 4 * (ks) + (g), 6); QOP(HOOK, NOP, 4 * (ks) + (g), 7); \
        QOP(HOOK, NOP, 4 * (ks) + (g), 8); QOP(HOOK, NOP, 4 * (ks) + (g), 9); \
        SBAR(); } while (0)
#define QKS(s, ks, HOOK, NOP) do { QGAP(s, ks, 0, HOOK, NOP); QGAP(s, ks, 1, HOOK, NOP); QGAP(s, ks, 2, HOOK, NOP); QGAP(s, ks, 3, HOOK, NOP); } while (0)
#define QCMP(s, HOOK, NOP) do { HOOK(0, -1); SBAR(); QKS(s, 0, HOOK, NOP); QKS(s, 1, HOOK, NOP); QKS(s, 2, HOOK, NOP); QKS(s, 3, HOOK, NOP); } while (0)
#define HQ0(j, g) QG(3, j, g)
#define HQ1(j, g) QG(4, j, g)
#define HQ2(j, g) QG(5, j, g)
#define HQ3(j, g) QG(6, j, g)
#define HQ4(j, g) QG(7, j, g)
#define HQ5(j, g) EG(0, j, g)
#define HQ6(j, g) EG(1, j, g)
#define HQ7(j, g) EG(2, j, g)
    QLD(0); QLD(1);
#pragma unroll
    for (int db = 0; db < 4; ++db) { o[db] = f32x16{}; asm volatile("" : "+a"(o[db])); }
    QLD(2);
    waitv<2 * QE>(); BARRIER(); QCMP(0, HQ0, QE);
    waitv<2 * QE>(); BARRIER(); QCMP(1, HQ1, QE);
    waitv<2 * QE>(); BARRIER(); QCMP(2, HQ2, QE);
    waitv<2 * QE>(); BARRIER(); QCMP(3, HQ3, QE);
    waitv<2 * QE>(); BARRIER(); QCMP(4, HQ4, QE);
    waitv<2 * QE>(); BARRIER(); QCMP(5, HQ5, PE);
    waitv<QE + PE>(); BARRIER(); QCMP(6, HQ6, PE);
    waitv<2 * PE>(); BARRIER(); QCMP(7, HQ7, PE);
    QM1(7, 0, 0); QM1(7, 1, 0); QM1(7, 2, 0); QM1(7, 3, 0); SBAR();
    QM1(7, 0, 1); QM1(7, 1, 1); QM1(7, 2, 1); QM1(7, 3, 1); asm volatile("s_nop 7" : "+v"(acc[0])); SBAR();
    QM1(7, 0, 2); QM1(7, 1, 2); QO(0); SBAR(); QM1(7, 2, 2); QM1(7, 3, 2); PIN(acc[1]); SBAR();
    QM1(7, 0, 3); QM1(7, 1, 3); QO(1); SBAR(); QM1(7, 2, 3); QM1(7, 3, 3); PIN(acc[2]); SBAR();
    QO(2); SBAR(); asm volatile("s_nop 3" : "+v"(acc[3])); QO(3); SBAR();
  }
  PIN(epoch_v);
  const u64 tag = mix64(((u64)rfl((unsigned)(epoch_v >> 32)) << 32) | rfl((unsigned)epoch_v));

  unsigned tA = 3u * ESZ + dsub, tB = 2u * ESZ + dsub;
  const unsigned kb = ring + l31 * 256u + ((hh ^ l15) * 16u);
  const unsigned vl = (4u * hh + ((lane >> 2) & 3u)) * 256u + (((lane >> 2) & 3u) << 6) + (g4 & 1u) * 32u + (lane & 3u) * 8u;
  const unsigned vb = ring + vl;
  f32x16 sA[2], sB[2], negm;
  bf16x8 kf[2][8]; s16x4 vlo[4][4], vhi[4][4]; u32x4 pw[2][2];
  float fsc = 1.f, ls0 = 0.f, lt0 = 0.f, ls1 = 0.f, lt1 = 0.f;
#define QK0(D, a, bq) asm volatile("v_mfma_f32_32x32x16_bf16 %0, %1, %2, 0" : "=&v"(D) : "v"(a), "a"(bq))
#define QKC(D, a, bq, C) asm volatile("v_mfma_f32_32x32x16_bf16 %0, %1, %2, %3" : "=&v"(D) : "v"(a), "a"(bq), "v"(C))
#define QKA(D, a, bq) asm volatile("v_mfma_f32_32x32x16_bf16 %0, %1, %2, %0" : "+v"(D) : "v"(a), "a"(bq))
#define WAITE(k) do { waitv<PE * (k)>(); BARRIER(); } while (0)
#define EXP2(x) __builtin_amdgcn_exp2f((x) * C2)
#define KRD(s) do { const unsigned a_ = ka ^ (32u * (s)); kf[0][s] = lds_rd128(a_); kf[1][s] = lds_rd128(a_ + 8192u); } while (0)

  WAITE(2);
  {
    const unsigned ka = kb;
#pragma unroll
    for (int s = 0; s < 8; ++s) KRD(s);
  }
  QK0(sA[0], kf[0][0], qf[0]); QK0(sA[1], kf[1][0], qf[0]);
#pragma unroll
  for (int s = 1; s < 8; ++s) { QKA(sA[0], kf[0][s], qf[s]); QKA(sA[1], kf[1][s], qf[s]); if (s == 2) ELEM(3); }
  WAITE(2);
  ELEM(4);
  {
    const unsigned ka = kb + ESZ;
#pragma unroll
    for (int s = 0; s < 8; ++s) KRD(s);
  }
  asm volatile("s_nop 7\n\ts_nop 7" : "+v"(sA[0]), "+v"(sA[1]));
  {
    const float m0 = swap_max(max2f(max8hi(max8lo(sA[0]), sA[0]), max8hi(max8lo(sA[1]), sA[1])));
#pragma unroll
    for (int r = 0; r < 16; ++r) { sA[0][r] -= m0; sA[1][r] -= m0; negm[r] = -m0; }
    PIN(negm);
#pragma unroll
    for (int r = 0; r < 8; ++r) { sA[0][r] = EXP2(sA[0][r]); sA[1][r] = EXP2(sA[1][r]); }
#pragma unroll
    for (int r = 0; r < 8; r += 2) { ls0 += sA[0][r]; lt0 += sA[0][r + 1]; ls1 += sA[1][r]; lt1 += sA[1][r + 1]; }
  }
  unsigned wvo[4];
  const srd_t sW = MKSRD((BB ? p.woB : p.woA) + (size_t)h0 * 131072u + wid * 256u, (BB ? 262144 : 524288) - 768);
  const unsigned wbuf = BB ? hreg + qb * 8192u : ldsb + ESZ + wid * 8192u;
  const unsigned bhi = b >> 2;
#define WSO(i) ((((i) + b) & 3u) * 65536u + ((((i) >> 2) & 1u) ^ bhi) * 128u)
#define WG(i, hi, g) DG(wbuf + (1u + ((i) & 3)) * ESZ + (hi) * 4096u, wvo, sW, WSO(i) + (hi) * 32768u, NT_WO, 4, g)
#define WOP(v, TA, TB_) do { if ((v) < 8) WG(TA, ((v) >> 2) & 1, (v) & 3); else WG(TB_, ((v) >> 2) & 1, (v) & 3); \
    if ((v) == 3) WG(TA, 1, -1); if ((v) == 7 && (TB_) >= 0) WG((TB_) & 7, 0, -1); if ((v) == 11) WG((TB_) & 7, 1, -1); } while (0)

#define VRD(db, k4) do { const unsigned a_ = (vb ^ (64u * (db))) + S2 + 4096u * (k4); vlo[db][k4] = lds_tr(a_); vhi[db][k4] = lds_tr(a_ + 2048u); } while (0)
  s16x4 ulo[4][4], uhi[4][4];
#define URD(db, k4) do { const unsigned a_ = (vb ^ (64u * (db))) + 4096u * (k4); ulo[db][k4] = lds_tr(a_); uhi[db][k4] = lds_tr(a_ + 2048u); } while (0)
#define GAPA(C, N, s, KB, LAST) do { \
    if ((s) == 0) QKC(N[KB], kf[KB][0], qf[0], negm); else QKA(N[KB], kf[KB][s], qf[s]); \
    if ((s) < 4) { float e0_ = EXP2(C[KB][(8 + 2 * (s)) & 15]), e1_ = EXP2(C[KB][(9 + 2 * (s)) & 15]); PIN(e0_); PIN(e1_); C[KB][(8 + 2 * (s)) & 15] = e0_; C[KB][(9 + 2 * (s)) & 15] = e1_; \
      pw[KB][0][(s) & 3] = cvtpk(C[KB][2 * (s)], C[KB][2 * (s) + 1]); PIN(pw[KB][0]); } \
    else { ls##KB += C[KB][2 * (s)]; lt##KB += C[KB][2 * (s) + 1]; pw[KB][1][(s) & 3] = cvtpk(C[KB][2 * (s)], C[KB][2 * (s) + 1]); PIN(pw[KB][1]); PIN(ls##KB); PIN(lt##KB); } \
    if ((s) < 4) VRD((2 * (s) + KB) & 3, VK4(((s) >> 1) & 1)); else if ((s) < 6) { VRD(2 * KB, VK4(((s) & 1) + 2)); VRD(2 * KB + 1, VK4(((s) & 1) + 2)); } \
    SBAR(); \
    if (LAST) { if (BB && KB == 1) WOP(s, 0, -1); } \
    else if (KB == 1 && (BB || (s) < 4)) { dma1<((s) & 3) * 1024, NTS>(kvo[(s) & 3], sK, RO(tA) + ((s) >> 2) * 4096u); if (BB && (s) == 3) SETM0(dring + S1 + 4096u); } \
    SBAR(); } while (0)
#define VK4(i) ((i) == 0 ? 0 : (i) == 1 ? 2 : (i) == 2 ? 1 : 3)
#define PHASE_A(C, N, ODD, L) do { \
    constexpr unsigned S1 = (ODD ? 3u : 1u) * ESZ, S2 = (ODD ? 0u : 2u) * ESZ; \
    if (!BB && ASLP > 0) __builtin_amdgcn_s_sleep(ASLP); \
    if (L) { if (BB) WG(0, 0, -1); } else SETM0(dring + S1); \
    WAITE(2); SBAR(); \
    GAPA(C, N, 0, 0, L); GAPA(C, N, 0, 1, L); GAPA(C, N, 1, 0, L); GAPA(C, N, 1, 1, L); GAPA(C, N, 2, 0, L); GAPA(C, N, 2, 1, L); GAPA(C, N, 3, 0, L); GAPA(C, N, 3, 1, L); \
    GAPA(C, N, 4, 0, L); GAPA(C, N, 4, 1, L); GAPA(C, N, 5, 0, L); GAPA(C, N, 5, 1, L); GAPA(C, N, 6, 0, L); GAPA(C, N, 6, 1, L); GAPA(C, N, 7, 0, L); GAPA(C, N, 7, 1, L); \
    tA += ESZ; } while (0)
#define PVM(i) o[(i) & 3] = MFMA32(cat8(vlo[(i) & 3][VK4((i) >> 2)], vhi[(i) & 3][VK4((i) >> 2)]), __builtin_bit_cast(bf16x8, pw[((i) >> 2) & 1][(i) >> 3]), o[(i) & 3])
#define DMAB(v, LAST) do { SBAR(); if (LAST) { if (BB && (v) < 8) WOP((v) & 7, 1, -1); } \
    else if (((v) & 1) == 0 && (BB || (v) < 8)) { dma1<(((v) >> 1) & 3) * 1024, NTS>(vvo, sV, RO(tB) + ((v) >> 3) * 4096u); if (BB && (v) == 6) SETM0(dring + S2 + 4096u); } SBAR(); } while (0)
#define GAPE(N, j, LAST) do { PVM(8 + (j)); N[0][j] = EXP2(N[0][j]); N[1][j] = EXP2(N[1][j]); \
    if ((j) > 0) { if ((j) & 1) { ls0 += N[0][((j) + 15) & 15]; ls1 += N[1][((j) + 15) & 15]; } else { lt0 += N[0][((j) + 15) & 15]; lt1 += N[1][((j) + 15) & 15]; } } \
    if (LAST) { URD((2 * (j)) & 3, VK4((j) >> 1)); URD((2 * (j) + 1) & 3, VK4((j) >> 1)); } \
    PIN(N[0]); PIN(N[1]); PIN(ls0); PIN(ls1); PIN(lt0); PIN(lt1); DMAB(8 + (j), LAST); } while (0)
#define PHASE_B(N, ODD, LAST) do { \
    constexpr unsigned S2 = (ODD ? 0u : 2u) * ESZ, S3 = (ODD ? 1u : 3u) * ESZ; \
    if (LAST) { if (BB) WG(1, 0, -1); BARRIER(); asm volatile("s_nop 7"); } else { SETM0(dring + S2); WAITE(2); } SBAR(); \
    const unsigned ka = kb + S3; \
    PVM(0); KRD(0); DMAB(0, LAST); \
    PVM(1); KRD(1); DMAB(1, LAST); \
    PVM(2); KRD(2); float ma0 = max3f(N[0][0], N[0][1], N[0][2]), ma1 = max3f(N[1][0], N[1][1], N[1][2]), mb0 = max3f(N[0][3], N[0][4], N[0][5]), mb1 = max3f(N[1][3], N[1][4], N[1][5]); DMAB(2, LAST); \
    PVM(3); KRD(3); ma0 = max3f(ma0, N[0][6], N[0][7]); ma1 = max3f(ma1, N[1][6], N[1][7]); mb0 = max3f(mb0, N[0][8], N[0][9]); mb1 = max3f(mb1, N[1][8], N[1][9]); DMAB(3, LAST); \
    PVM(4); KRD(4); ma0 = max3f(ma0, N[0][10], N[0][11]); ma1 = max3f(ma1, N[1][10], N[1][11]); mb0 = max3f(mb0, N[0][12], N[0][13]); mb1 = max3f(mb1, N[1][12], N[1][13]); DMAB(4, LAST); \
    PVM(5); ma0 = max3f(ma0, N[0][14], N[0][15]); ma1 = max3f(ma1, N[1][14], N[1][15]); const float mbb = max2f(mb0, mb1); KRD(5); DMAB(5, LAST); \
    PVM(6); const float mall = max3f(ma0, ma1, mbb); KRD(6); const bool grew = __any(mall > THR); DMAB(6, LAST); \
    PVM(7); KRD(7); DMAB(7, LAST); \
    if (__builtin_expect(grew, 0)) {     \
      const float d0 = max2f(swap_max(mall), 0.f); \
      fsc = EXP2(-d0); ls0 *= fsc; lt0 *= fsc; ls1 *= fsc; lt1 *= fsc; \
      sub_tile(N[0], d0); sub_tile(N[1], d0); sub_tile(negm, d0); \
    } \
    if (LAST) { waitv<BB ? 16 : 24>(); asm volatile("s_barrier" ::: "memory"); }     \
    SBAR(); \
    GAPE(N, 0, LAST); GAPE(N, 1, LAST); GAPE(N, 2, LAST); GAPE(N, 3, LAST); GAPE(N, 4, LAST); GAPE(N, 5, LAST); GAPE(N, 6, LAST); GAPE(N, 7, LAST); \
    lt0 += N[0][7]; lt1 += N[1][7]; \
    if (__builtin_expect(grew, 0)) { \
      asm volatile("s_nop 7\n\ts_nop 7" ::: "memory"); \
      scale_acc(o[0], fsc); scale_acc(o[1], fsc); scale_acc(o[2], fsc); scale_acc(o[3], fsc); \
    } \
    tB += ESZ; } while (0)

#define FLAG3() do { if (tid == 0u) __hip_atomic_store((GAS u64*)(p.ws + WS_FLAG3) + (size_t)blockIdx.x * 8u, tag, RLX_AGENT); } while (0)
  if (!BB) FLAG3();
  for (unsigned t = 0; t < 14u; t += 2u) {
    if (t == CPF1 && idx == 0u) {
      u64 pc0; unsigned clen;
      if constexpr (BB) asm volatile("s_getpc_b64 %0\n.Lk1_b0:\n\ts_mov_b32 %1, .Lk1_b1-.Lk1_b0" : "=s"(pc0), "=s"(clen));
      else              asm volatile("s_getpc_b64 %0\n.Lk1_a0:\n\ts_mov_b32 %1, .Lk1_a1-.Lk1_a0" : "=s"(pc0), "=s"(clen));
      const srd_t sC = MKSRD(pc0, (int)(clen <= 65536u ? (clen & ~3u) : 0u));
      TOUCH(sC, wid * 8192u);
    }
    PHASE_A(sA, sB, false, false); PHASE_B(sB, false, false);
    PHASE_A(sB, sA, true, false);  PHASE_B(sA, true, false);
  }
#pragma unroll
  for (int j = 0; j < 4; ++j) wvo[j] = BB ? (lane >> 3) * 1024u + (((lane & 7u) ^ ((g4 & 1u) << 2)) * 16u) + j * 7168u : g4 * 1024u + ((l15 ^ (g4 << 2)) * 16u) + j * 3072u;
  constexpr unsigned GPITCH16 = 16u * 1024u, GJ2 = 2048u;
  unsigned gidx = idx; asm volatile("" : "+s"(gidx));
  const srd_t gsr = sW; const unsigned (&gvo)[4] = wvo;
  if (BB) FLAG3();
  PHASE_A(sA, sB, false, true);
  if (!BB) { GD8(0); GD8(1); GD8(2); }
  PHASE_B(sB, false, true);
  bf16x8 wfr[2][8];
  const unsigned wb = wbuf + (4u * hh + q4) * 128u + (q4 >> 1) * 64u + (g4 & 1u) * 32u + p4 * 8u;
#define WRD(i, x) do { const unsigned a_ = (wb ^ (64u * ((x) & 1))) + (1u + ((i) & 3)) * ESZ + 2048u * ((x) >> 1); wfr[(i) & 1][x] = cat8(lds_tr(a_), lds_tr(a_ + 1024u)); } while (0)
  {
    PIN(tch);
#define PINA(x) asm volatile("" : "+a"(x))
#define UM(db, kb_, ks, v) do { o[db] = MFMA32(cat8(ulo[db][2 * (kb_) + (ks)], uhi[db][2 * (kb_) + (ks)]), __builtin_bit_cast(bf16x8, pw[kb_][ks]), o[db]); PINA(o[db]); SBAR(); \
      if (BB) WOP(v, 2, 3); SBAR(); } while (0)
#pragma unroll
    for (int i = 0; i < 4; ++i) { pw[0][0][i] = cvtpk(sB[0][2 * i], sB[0][2 * i + 1]); pw[1][0][i] = cvtpk(sB[1][2 * i], sB[1][2 * i + 1]); }
    if (BB) { WG(2, 0, -1); BARRIER(); }
    SBAR();
#define UG(x) do { UM((x) & 3, (x) >> 2, 0, x); float e0_ = EXP2(sB[(x) >> 2][8 + 2 * ((x) & 3)]), e1_ = EXP2(sB[(x) >> 2][9 + 2 * ((x) & 3)]); ls0 += e0_; ls1 += e1_; \
      pw[(x) >> 2][1][(x) & 3] = cvtpk(e0_, e1_); PIN(pw[(x) >> 2][1]); PIN(ls0); PIN(ls1); SBAR(); } while (0)
    UG(0); UG(1); UG(2); UG(3); UG(4); UG(5); UG(6); UG(7);
    float inv = __builtin_amdgcn_rcpf(swap_add((ls0 + lt0) + (ls1 + lt1))); PIN(inv);
#define OWR(db) do { const f32x16 y_ = o[db] * inv; lds_wru4(wbuf + (2 * (db)) * 1024u + lane * 16u, pack8(y_, 0)); lds_wru4(wbuf + (2 * (db) + 1) * 1024u + lane * 16u, pack8(y_, 1)); } while (0)
    UM(0, 0, 1, 8); UM(0, 1, 1, 9); UM(1, 0, 1, 10); UM(1, 1, 1, 11); UM(2, 0, 1, 12); UM(2, 1, 1, 13);
    OWR(0); SBAR();
    UM(3, 0, 1, 14); UM(3, 1, 1, 15);
    if (BB) {
      waitv<24>();
#pragma unroll
      for (int i = 0; i < 8; ++i) WRD(0, i);
    }
    OWR(1); OWR(2); OWR(3);
    BARRIER();
  }
  constexpr unsigned br = BB ? 1u : 0u;
  const srd_t sP = MKSRD(p.ws + WS_PART, 16 << 20);
  GAS u64* flags = (GAS u64*)(p.ws + WS_FLAG);
  const unsigned hp = BB ? (h0 >> 1) : g8;
  const unsigned fgrp = BB ? wid * 16u : (qb * 4u + wid) * 8u;
  if constexpr (!BB) {
    f32x16 acc[4]; bf16x8 gw[2][8], gb[2][2];
    const unsigned ga = gbuf + vl;
#define GB(i) do { const unsigned a_ = ldsb + ESZ + GT(i) * 2048u + lane * 16u; gb[(i) & 1][0] = lds_rd128(a_); gb[(i) & 1][1] = lds_rd128(a_ + 1024u); } while (0)
    waitv<16>();
    GFIRST();
    GTILE(0, 0); GTILE(1, 0); GTILE(2, 0); GTILE(3, 0); GTILE(4, 0); GTILE(5, 0); GTILE(6, 0); GTILE(7, 0);
    GTILE(8, 0); GTILE(9, 0); GTILE(10, 0); GTILE(11, 0); GTILE(12, 0); GTILE(13, 0); GTILE(14, 0); GTILE(15, 0);
    asm volatile("s_nop 7\n\ts_nop 7" : "+v"(acc[0]), "+v"(acc[1]), "+v"(acc[2]), "+v"(acc[3]));
    poll_flags<8>((GAS u64*)(p.ws + WS_FLAG3) + (size_t)(blockIdx.x - 128u) * 8u, tag);
    const unsigned pb = b * 1048576u + (qb * 32u + 8u * wid) * 16384u + hp * 1024u + lane * 16u;
#pragma unroll
    for (int i = 0; i < 8; ++i) __builtin_amdgcn_raw_buffer_store_b128(pack8(acc[i >> 1], i & 1), sP, pb + i * 16384u, 0, 16);
  } else {
    bf16x8 of[4][2][4];
#define ORD(i, x) do { const unsigned r_ = ((i) + b) & 3u; of[i][(x) & 1][(x) >> 1] = lds_rd128(ldsb + (r_ >> 1) * HREG + (r_ & 1u) * 4096u + lane * 16u + ((x) & 1) * 8192u + ((x) >> 1) * 1024u); } while (0)
    ORD(0, 0); ORD(0, 1); ORD(0, 2); ORD(0, 3); ORD(0, 4); ORD(0, 5); ORD(0, 6); ORD(0, 7);
    f32x16 acc[4][2];
    const f32x16 z16 = {};
    const unsigned pb = (br * 8u + b) * 1048576u + 8u * wid * 16384u + hp * 1024u + lane * 16u;
    const unsigned pb0 = pb + bhi * 65536u, pb1 = pb + (bhi ^ 1u) * 65536u;
#define PST(nb, q, s2) __builtin_amdgcn_raw_buffer_store_b128(pack8(acc[nb][q], s2), sP, (((nb) >> 1) ? pb1 : pb0) + ((q) * 32 + ((nb) & 1) * 2 + (s2)) * 16384u, 0, 16)
#define OM(i, x, q) do { acc[2 * ((i) >> 2) + ((x) & 1)][q] = MFMA32(wfr[(i) & 1][x], of[(i) & 3][q][(x) >> 1], (((i) & 3) == 0 && (x) < 2) ? z16 : acc[2 * ((i) >> 2) + ((x) & 1)][q]); \
      PINA(acc[2 * ((i) >> 2) + ((x) & 1)][q]); } while (0)
#define OX(i, x) do { \
      OM(i, x, 0); WRD((i) + 1, x); if ((i) < 3) ORD(((i) + 1) & 3, x); SBAR(); \
      if ((i) < 4) WOP(x, ((i) + 4) & 7, -1); \
      SBAR(); OM(i, x, 1); if ((i) >= 4 && ((x) & 1)) PST((i) & 1, ((x) >> 2) & 1, ((x) >> 1) & 1); SBAR(); } while (0)
#define OT(i, N) do { if ((i) < 4) WG(((i) + 4) & 7, 0, -1); waitv<N>(); if ((i) > 0) { WAITL(); } SBAR(); \
      OX(i, 0); OX(i, 1); OX(i, 2); OX(i, 3); OX(i, 4); OX(i, 5); OX(i, 6); OX(i, 7); } while (0)
    OT(0, 16); OT(1, 16); OT(2, 16); OT(3, 16); OT(4, 16); OT(5, 12);
    waitv<8>(); SBAR();
#pragma unroll
    for (int i = 0; i < 8; ++i) WRD(7, (2 * i + (i >> 2)) & 7);
    SBAR();
#define OF(n, q, pn, pq) do { \
      OM(6, n, q); OM(6, 2 + (n), q); OM(6, 4 + (n), q); SBAR(); OM(6, 6 + (n), q); if ((pn) >= 0) PST(2 + ((pn) & 1), pq, 0); SBAR(); \
      OM(7, n, q); OM(7, 2 + (n), q); SBAR(); OM(7, 4 + (n), q); if ((pn) >= 0) PST(2 + ((pn) & 1), pq, 1); SBAR(); OM(7, 6 + (n), q); SBAR(); } while (0)
    OF(0, 0, -1, 0); OF(0, 1, 0, 0); OF(1, 0, 0, 1); OF(1, 1, 1, 0);
    PST(3, 1, 0); PST(3, 1, 1);
  }
  waitv<0>();
  if (lane == 0) __hip_atomic_store(flags + (size_t)((br * 8u + b) * 64u + fgrp + hp) * 8u, tag, RLX_AGENT);
  {
    constexpr int NP = BB ? 16 : 8;
    const unsigned u = BB ? hp * 4u + wid : qb * 32u + hp * 4u + wid;
    poll_flags<2>(flags + (size_t)((br * 8u + b) * 64u + (BB ? ((u >> 3) & 3u) * 16u + l15 : (u & 56u) + (lane & 7u))) * 8u, tag);
    const unsigned rb = (br * 8u + b) * 1048576u + u * 16384u + lane * 16u;
    u32x4 pv[NP];
#pragma unroll
    for (int j = 0; j < NP; ++j) pv[j] = __builtin_amdgcn_raw_buffer_load_b128(sP, rb + j * 1024u, 0, 16);
    float y[8] = {0.f, 0.f, 0.f, 0.f, 0.f, 0.f, 0.f, 0.f};
#pragma unroll
    for (int j = 0; j < NP; ++j)
#pragma unroll
      for (int e = 0; e < 4; ++e) { y[2 * e] += __uint_as_float(pv[j][e] << 16); y[2 * e + 1] += __uint_as_float(pv[j][e] & 0xffff0000u); }
    const auto r0 = __builtin_amdgcn_permlane32_swap(cvtpk(y[0], y[1]), cvtpk(y[4], y[5]), false, false);
    const auto r1 = __builtin_amdgcn_permlane32_swap(cvtpk(y[2], y[3]), cvtpk(y[6], y[7]), false, false);
    *(u32x4*)(p.out + (size_t)(b * 64u + (u >> 5) * 32u + l31) * 2048u + (br * 512u + (u & 31u) * 16u + 8u * hh) * 2u) = u32x4{r0[0], r1[0], r0[1], r1[1]};
    if (!BB && blockIdx.x == 128u && wid == 0u) {
#pragma unroll 1
      for (unsigned spins = 0; spins < (1u << 21); ++spins) {
        bool ok = true;
#pragma unroll
        for (int k = 0; k < 4; ++k) ok &= (__hip_atomic_load((GAS u64*)(p.ws + WS_FLAG3) + (size_t)(k * 64 + lane) * 8u, RLX_AGENT) == tag);
        if (__all(ok)) break;
        __builtin_amdgcn_s_sleep(8);
      }
      if (lane == 0) __hip_atomic_store((GAS u64*)(p.ws + WS_EPOCH), tag, RLX_AGENT);
    }
  }
  if constexpr (BB) asm volatile(".Lk1_b1:"); else asm volatile(".Lk1_a1:");
}

__global__ __launch_bounds__(256, 1) void k1_attn(P1 p) {
  extern __shared__ __attribute__((aligned(16))) char lds[];
  const unsigned ldsb = (unsigned)(unsigned long)lds;
  if (blockIdx.x < 128u) body<true>(p, ldsb); else body<false>(p, ldsb);
}

extern "C" void kernel_launch(void* const* d_in, const int* in_sizes, int n_in,
                              void* d_out, int out_size, void* d_ws, size_t ws_size,
                              hipStream_t stream) {
  (void)in_sizes; (void)n_in; (void)out_size; (void)ws_size;
  (void)hipFuncSetAttribute((const void*)k1_attn, hipFuncAttributeMaxDynamicSharedMemorySize, K1_LDS);
  P1 p1;
  p1.xA = (const char*)d_in[0]; p1.kA = (const char*)d_in[1]; p1.vA = (const char*)d_in[2]; p1.wqA = (const char*)d_in[3]; p1.woA = (const char*)d_in[4];
  p1.xB = (const char*)d_in[5]; p1.kB = (const char*)d_in[6]; p1.vB = (const char*)d_in[7]; p1.wqB = (const char*)d_in[8]; p1.woB = (const char*)d_in[9];
  p1.ws = (char*)d_ws; p1.out = (char*)d_out;
  hipLaunchKernelGGL(k1_attn, dim3(256), dim3(256), K1_LDS, stream, p1);
}
```

```cpp
#include <hip/hip_runtime.h>
#include <hip/hip_bf16.h>

typedef float    f32x16 __attribute__((ext_vector_type(16)));
typedef float    f32x2  __attribute__((ext_vector_type(2)));
typedef __bf16   bf16x8 __attribute__((ext_vector_type(8)));
typedef __bf16   bf16x2 __attribute__((ext_vector_type(2)));
typedef short    s16x4  __attribute__((ext_vector_type(4)));
typedef unsigned u32x4  __attribute__((ext_vector_type(4)));
typedef unsigned long long u64;
typedef __amdgpu_buffer_rsrc_t srd_t;
#define DI __device__ __forceinline__
#define LAS __attribute__((address_space(3)))
#define GAS __attribute__((address_space(1)))
#define SBAR() __builtin_amdgcn_sched_barrier(0)
#define PIN(x) asm volatile("" : "+v"(x))
#define MFMA32(a, b, c) __builtin_amdgcn_mfma_f32_32x32x16_bf16(a, b, c, 0, 0, 0)
#define RLX_AGENT __ATOMIC_RELAXED, __HIP_MEMORY_SCOPE_AGENT
#define MKSRD(ptr, n) __builtin_amdgcn_make_buffer_rsrc((void*)(ptr), (short)0, (n), 0x00020000)

constexpr int K1_LDS = 163840;
constexpr float C2 = 0.08838834764831845f * 1.4426950408889634f;
constexpr float THR = 8.0f / C2;
constexpr size_t WS_PART = 0;
constexpr size_t WS_FLAG = 16u << 20;
constexpr size_t WS_FLAG3 = WS_FLAG + 65536;
constexpr size_t WS_EPOCH = WS_FLAG3 + 16384;

DI unsigned rfl(unsigned x) { return __builtin_amdgcn_readfirstlane(x); }
DI unsigned cvtpk(float lo, float hi) { f32x2 v = {lo, hi}; bf16x2 b = __builtin_convertvector(v, bf16x2); return __builtin_bit_cast(unsigned, b); }
DI bf16x8 lds_rd128(unsigned a) { return *(const LAS bf16x8*)(unsigned long)a; }
DI void lds_wru4(unsigned a, u32x4 v) { *(LAS u32x4*)(unsigned long)a = v; }
DI s16x4 lds_tr(unsigned a) { return __builtin_amdgcn_ds_read_tr16_b64_v4i16((LAS s16x4*)(unsigned long)a); }
DI bf16x8 cat8(s16x4 lo, s16x4 hi) { return __builtin_bit_cast(bf16x8, __builtin_shufflevector(lo, hi, 0, 1, 2, 3, 4, 5, 6, 7)); }
DI float max3f(float a, float b, float c) { float r; asm("v_max3_f32 %0, %1, %2, %3" : "=v"(r) : "v"(a), "v"(b), "v"(c)); return r; }
DI float max2f(float a, float b) { float r; asm("v_max_f32_e32 %0, %1, %2" : "=v"(r) : "v"(a), "v"(b)); return r; }
DI float swap_max(float m) { auto rr = __builtin_amdgcn_permlane32_swap(__float_as_uint(m), __float_as_uint(m), false, false); return max2f(__uint_as_float(rr[0]), __uint_as_float(rr[1])); }
DI float swap_add(float m) { auto rr = __builtin_amdgcn_permlane32_swap(__float_as_uint(m), __float_as_uint(m), false, false); return __uint_as_float(rr[0]) + __uint_as_float(rr[1]); }
DI float max8lo(const f32x16& p) { float a = max3f(p[0], p[1], p[2]), b = max3f(p[3], p[4], p[5]); return max3f(max3f(a, p[6], p[7]), b, b); }
DI float max8hi(float m, const f32x16& p) { float a = max3f(m, p[8], p[9]), b = max3f(p[10], p[11], p[12]); a = max3f(a, p[13], p[14]); return max3f(a, b, p[15]); }
DI void scale_acc(f32x16& x, float f) {
#pragma unroll
  for (int r = 0; r < 16; ++r) { float e = x[r], t;
    asm volatile("v_accvgpr_read_b32 %1, %0\n\tv_mul_f32 %1, %1, %2\n\tv_accvgpr_write_b32 %0, %1" : "+a"(e), "=&v"(t) : "v"(f));
    x[r] = e; }
}
DI void sub_tile(f32x16& x, float d) {
#pragma unroll
  for (int r = 0; r < 16; ++r) { float e = x[r]; asm volatile("v_sub_f32_e32 %0, %0, %1" : "+v"(e) : "v"(d)); x[r] = e; }
}
DI u64 mix64(u64 z) { z += 0x9E3779B97F4A7C15ull; z = (z ^ (z >> 30)) * 0xBF58476D1CE4E5B9ull; z = (z ^ (z >> 27)) * 0x94D049BB133111EBull; return z ^ (z >> 31); }


#define SETM0(x) asm volatile("s_mov_b32 m0, %0" :: "s"(x) : "memory")
#define SETM0N(x) asm volatile("s_mov_b32 m0, %0\n\ts_nop 0" :: "s"(x) : "memory")
template <int OFF, bool NT> DI void dma2(unsigned vo0, unsigned vo1, srd_t srd, unsigned soff) {
  if constexpr (NT)
    asm volatile("buffer_load_dwordx4 %0, %2, %3 offen offset:%4 nt lds\n\tbuffer_load_dwordx4 %1, %2, %3 offen offset:%5 nt lds"
                 :: "v"(vo0), "v"(vo1), "s"(srd), "s"(soff), "i"(OFF), "i"(OFF + 1024) : "memory");
  else
    asm volatile("buffer_load_dwordx4 %0, %2, %3 offen offset:%4 lds\n\tbuffer_load_dwordx4 %1, %2, %3 offen offset:%5 lds"
                 :: "v"(vo0), "v"(vo1), "s"(srd), "s"(soff), "i"(OFF), "i"(OFF + 1024) : "memory");
}
template <int OFF, bool NT> DI void dma1(unsigned vo, srd_t srd, unsigned soff) {
  if constexpr (NT) asm volatile("buffer_load_dwordx4 %0, %1, %2 offen offset:%3 nt lds" :: "v"(vo), "s"(srd), "s"(soff), "i"(OFF) : "memory");
  else asm volatile("buffer_load_dwordx4 %0, %1, %2 offen offset:%3 lds" :: "v"(vo), "s"(srd), "s"(soff), "i"(OFF) : "memory");
}
template <bool NT> DI void dma4(unsigned m0v, const unsigned (&vo)[4], srd_t srd, unsigned soff) { SETM0N(m0v); dma2<0, NT>(vo[0], vo[1], srd, soff); dma2<2048, NT>(vo[2], vo[3], srd, soff); }
template <int N> DI void waitv() { asm volatile("s_waitcnt vmcnt(%0)" :: "i"(N > 63 ? 63 : N) : "memory"); }
#define WAITL() do { asm volatile("" ::: "memory"); __builtin_amdgcn_s_waitcnt(0xC07F); asm volatile("" ::: "memory"); } while (0)
#define BARRIER() do { WAITL(); asm volatile("s_barrier" ::: "memory"); } while (0)

template <int SLP> DI void poll_flags(GAS u64* f, u64 tag) {
#pragma unroll 1
  for (unsigned spins = 0; spins < (1u << 21); ++spins) {
    const bool ok = (__hip_atomic_load(f, RLX_AGENT) == tag);
    if (__all(ok)) break;
    __builtin_amdgcn_s_sleep(SLP);
  }
  asm volatile("" ::: "memory");
}

constexpr int ADELAY = 40;
constexpr int ASLP = 3;
constexpr int TCH_KV = 2;
constexpr unsigned CPF1 = 10u;
constexpr bool NT_WO = false;

struct P1 {
  const char *xA, *kA, *vA, *wqA, *woA, *xB, *kB, *vB, *wqB, *woB;
  char* ws;
  char* out;
};

DI u32x4 pack8(const f32x16& x, int s) {
  return u32x4{cvtpk(x[8 * s], x[8 * s + 1]), cvtpk(x[8 * s + 2], x[8 * s + 3]), cvtpk(x[8 * s + 4], x[8 * s + 5]), cvtpk(x[8 * s + 6], x[8 * s + 7])};
}

constexpr unsigned ESZ = 16384u;
constexpr unsigned HREG = 5u * ESZ;

template <bool BB> DI void body(const P1& p, const unsigned ldsb) {
  constexpr int PE = BB ? 8 : 4;
  constexpr bool NTS = true;
  const unsigned tid = threadIdx.x, lane = tid & 63u, wid = rfl(tid >> 6);
  const unsigned g8 = blockIdx.x & 7u, idx = (blockIdx.x >> 3) & 15u;
  const unsigned qb = BB ? (wid & 1u) : (idx & 1u), item = wid >> 1;
  const unsigned h0 = BB ? 4u * g8 + 2u * (idx & 1u) : 4u * g8, b = idx >> 1;
  const unsigned l31 = lane & 31u, hh = lane >> 5, l15 = lane & 15u, g4 = lane >> 4;
  const unsigned hreg = ldsb + item * HREG;
  const unsigned ring = BB ? hreg : ldsb;
  const unsigned dsub = BB ? qb * 8192u : wid * 4096u;

  u64 epoch_v;
  asm volatile("global_load_dwordx2 %0, %1, off sc1" : "=v"(epoch_v) : "v"(p.ws + WS_EPOCH) : "memory");
  const size_t kvoff = (BB ? (size_t)(b * 32u + h0 + item) : (size_t)(b * 8u + g8)) * 262144u;
  const srd_t sK = MKSRD((BB ? p.kB : p.kA) + kvoff, 262144), sV = MKSRD((BB ? p.vB : p.vA) + kvoff, 262144);
  unsigned kvo[4];
#pragma unroll
  for (int i = 0; i < 4; ++i) kvo[i] = g4 * 256u + (((l15 ^ g4) ^ (4u * i)) * 16u);
  const unsigned vvo = g4 * 256u + ((l15 ^ (g4 << 2)) * 16u);
  const unsigned vv4[4] = {vvo, vvo, vvo, vvo};
  const unsigned dring = ring + dsub;
  const unsigned rr = ((BB ? (idx * 2u + item) * 3u + g8 : b * 2u + g8 * 5u + 1u) & 15u) * ESZ;
#define RO(x) ((((x) + rr) & 0x3ffffu) | ((x) & 0x40000u))
#define DG(m0v, vo, srd, soff, NT, cnt, g) do { if ((g) < 0) SETM0(m0v); else if ((g) < (cnt)) dma1<((g) & 3) * 1024, NT>(vo[(g) & 3], srd, soff); } while (0)
#define EG(n, j, g) do { if ((n) < 2 || ((n) & 1)) DG(dring + ((n) & 3) * ESZ + (j) * 4096u, kvo, sK, RO(((n) < 2 ? (n) : ((n) + 1) / 2) * ESZ + dsub) + (j) * 4096u, NTS, 4, g); \
                         else DG(dring + ((n) & 3) * ESZ + (j) * 4096u, vv4, sV, RO(((n) - 2) / 2 * ESZ + dsub) + (j) * 4096u, NTS, 4, g); } while (0)
#define EG4(n, j) do { EG(n, j, -1); asm volatile("s_nop 0"); EG(n, j, 0); EG(n, j, 1); EG(n, j, 2); EG(n, j, 3); } while (0)
#define ELEM(n) do { EG4(n, 0); if (BB) EG4(n, 1); } while (0)

  const unsigned gbuf = ldsb + 4u * ESZ + wid * 24576u;
#define GT(i) (((i) + gidx) & 15u)
#define GD(i, hi, g) DG(gbuf + ((i) % 3) * 8192u + (hi) * 4096u, gvo, gsr, (GT(i) * 2u + (hi)) * GPITCH16, false, 4, g)
#define GD8(i) do { GD(i, 0, -1); asm volatile("s_nop 0"); GD(i, 0, 0); GD(i, 0, 1); GD(i, 0, 2); GD(i, 0, 3); GD(i, 1, -1); asm volatile("s_nop 0"); GD(i, 1, 0); GD(i, 1, 1); GD(i, 1, 2); GD(i, 1, 3); } while (0)
#define GW(i, x) gw[(i) & 1][x] = cat8(lds_tr((ga ^ (64u * ((x) & 3))) + ((i) % 3) * 8192u + ((x) >> 2) * 4096u), lds_tr((ga ^ (64u * ((x) & 3))) + ((i) % 3) * 8192u + ((x) >> 2) * 4096u + GJ2))
#define GX(i, x) do { \
      if ((i) == 0 && (x) < 4) asm volatile("v_mfma_f32_32x32x16_bf16 %0, %1, %2, 0" : "=&v"(acc[(x) & 3]) : "v"(gw[(i) & 1][x]), "v"(gb[(i) & 1][(x) >> 2])); \
      else asm volatile("v_mfma_f32_32x32x16_bf16 %0, %1, %2, %0" : "+v"(acc[(x) & 3]) : "v"(gw[(i) & 1][x]), "v"(gb[(i) & 1][(x) >> 2])); \
      if ((i) < 15) { GW((i) + 1, x); if ((x) == 0) GB((i) + 1); } SBAR(); \
      if ((i) < 13) { GD((i) + 3, (x) >> 2, (x) & 3); if ((x) == 3) GD((i) + 3, 1, -1); } SBAR(); } while (0)
#define GTILE(i, XTRA) do { if ((i) < 13) GD((i) + 3, 0, -1); waitv<((i) < 14 ? 8 : 0) + (XTRA)>(); WAITL(); SBAR(); \
      GX(i, 0); GX(i, 1); GX(i, 2); GX(i, 3); GX(i, 4); GX(i, 5); GX(i, 6); GX(i, 7); } while (0)
#define GFIRST() do { GB(0); GW(0, 0); GW(0, 1); GW(0, 2); GW(0, 3); GW(0, 4); GW(0, 5); GW(0, 6); GW(0, 7); } while (0)

  bf16x8 qf[8];
  f32x16 o[4];
  unsigned tch = 0u;
#define TOUCH(srd, soff) asm volatile("buffer_load_dword %0, %1, %2, %3 offen" : "+v"(tch) : "v"(lane * 128u), "s"(srd), "s"(soff) : "memory")
#define QO(g) do { qf[2 * (g)] = __builtin_bit_cast(bf16x8, pack8(acc[g], 0)); qf[2 * (g) + 1] = __builtin_bit_cast(bf16x8, pack8(acc[g], 1)); asm volatile("" : "+a"(qf[2 * (g)]), "+a"(qf[2 * (g) + 1])); } while (0)
  const unsigned p4 = lane & 3u, q4 = (lane >> 2) & 3u;
  const unsigned wl = (8u * hh + q4) * 256u + (q4 << 6) + (g4 & 1u) * 32u + (((p4 & 1u) << 1) | (p4 >> 1)) * 8u;
  if constexpr (!BB) {
    constexpr unsigned GPITCH16 = 16u * 8192u, GJ2 = 1024u;
    const unsigned gidx = idx;
    const srd_t gsr = MKSRD(p.wqA + (h0 + wid) * 256u, 4 << 20), sx = MKSRD(p.xA + (size_t)(b * 64u + qb * 32u) * 1024u, 32768);
    unsigned gvo[4], xvo[4];
#pragma unroll
    for (int i = 0; i < 4; ++i) { gvo[i] = g4 * 8192u + ((l15 ^ (g4 << 2)) * 16u) + i * (32768u - 1024u); xvo[i] = g4 * 1024u + (((l15 ^ g4) ^ (4u * i)) * 16u) + i * 3072u; }
    TOUCH(MKSRD(p.xB, 524288), (idx * 4u + wid) * 8192u);
    if (ADELAY > 0) __builtin_amdgcn_s_sleep(ADELAY);
    dma4<false>(ldsb + 2u * ESZ + wid * 8192u, xvo, sx, wid * 256u); dma4<false>(ldsb + 2u * ESZ + wid * 8192u + 4096u, xvo, sx, wid * 256u + 16384u);
    GD8(0); GD8(1);
#pragma unroll
    for (int db = 0; db < 4; ++db) { o[db] = f32x16{}; asm volatile("" : "+a"(o[db])); }
    GD8(2);
#pragma unroll
    for (int i = 0; i < TCH_KV; ++i) TOUCH(item ? sV : sK, RO((4u * i + 2u * qb + (wid & 1u)) * 8192u));
    const unsigned ga = gbuf + wl, xb = ldsb + 2u * ESZ + l31 * 256u + ((hh ^ l15) * 16u);
    f32x16 acc[4]; bf16x8 gw[2][8], gb[2][2];
#define GB(i) do { const unsigned a_ = (xb ^ ((GT(i) & 3u) * 64u)) + (GT(i) >> 2) * 8192u; gb[(i) & 1][0] = lds_rd128(a_); gb[(i) & 1][1] = lds_rd128(a_ ^ 32u); } while (0)
    waitv<16 + TCH_KV>(); BARRIER();
    GFIRST();
    GTILE(0, TCH_KV); GTILE(1, TCH_KV); GTILE(2, 0); GTILE(3, 0); GTILE(4, 0); GTILE(5, 0); GTILE(6, 0); GTILE(7, 0);
    GTILE(8, 0); GTILE(9, 0); GTILE(10, 0); GTILE(11, 0); GTILE(12, 0);
    ELEM(0); ELEM(1);
    GTILE(13, 8); GTILE(14, 8);
    BARRIER();
    ELEM(2);
    GTILE(15, 12);
#undef GB
    asm volatile("s_nop 7\n\ts_nop 7" : "+v"(acc[0]), "+v"(acc[1]), "+v"(acc[2]), "+v"(acc[3]));
    QO(0); QO(1); QO(2); QO(3); SBAR();
  } else {
    constexpr int QE = 10;
    const unsigned rot = b + 4u * (idx & 1u);
    const srd_t sw = MKSRD(p.wqB + h0 * 256u, 4 << 20), sx = MKSRD(p.xB + (size_t)b * 65536u, 65536);
    unsigned wvo[4], xvo[4];
#pragma unroll
    for (int i = 0; i < 4; ++i) {
      wvo[i] = g4 * 8192u + ((l15 ^ (g4 << 2)) * 16u) + i * (32768u - 1024u);
      xvo[i] = (lane >> 3) * 1024u + (((lane & 7u) ^ ((4u * i + g4) & 7u)) * 16u) + i * (8192u - 1024u);
    }
#define WOFF(s) (((s) & 3) * ESZ)
#define XOFF(s) ((((s) >> 1) & 1) * HREG + 4u * ESZ + ((s) & 1) * 8192u)
    const unsigned wdst = ldsb + wid * 4096u, xdst = ldsb + wid * 2048u;
#define QG(s, j, g) do { const unsigned cv_ = ((s) + rot) & 7u; \
      if ((j) == 0) DG(wdst + WOFF(s), wvo, sw, (cv_ * 64u + wid * 16u) * 8192u, false, 4, g); \
      if ((j) == 1) DG(wdst + WOFF(s) + HREG, wvo, sw, (cv_ * 64u + wid * 16u) * 8192u + 256u, false, 4, g); \
      if ((j) == 2) DG(xdst + XOFF(s), xvo, sx, wid * 16384u + cv_ * 128u, false, 2, g); } while (0)
#define QG4(s, j) do { QG(s, j, -1); asm volatile("s_nop 0"); QG(s, j, 0); QG(s, j, 1); QG(s, j, 2); QG(s, j, 3); } while (0)
#define QLD(s) do { QG4(s, 0); QG4(s, 1); QG4(s, 2); } while (0)
    const unsigned wa = hreg + wl;
    const unsigned xa = ldsb + qb * 4096u + l31 * 128u + ((hh ^ ((l31 >> 1) & 7u)) * 16u);
    f32x16 acc[4];
    bf16x8 xf[2][4], wf[2][4][4];
#define QM1(s, ks, g) do { if ((s) == 0 && (ks) == 0) asm volatile("v_mfma_f32_32x32x16_bf16 %0, %1, %2, 0" : "=&v"(acc[g]) : "v"(wf[(s) & 1][g][ks]), "v"(xf[(s) & 1][ks])); \
      else asm volatile("v_mfma_f32_32x32x16_bf16 %0, %1, %2, %0" : "+v"(acc[g]) : "v"(wf[(s) & 1][g][ks]), "v"(xf[(s) & 1][ks])); } while (0)
#define QOP(HOOK, NOP, G, v) do { if ((v) < (NOP) && ((v) * 16) / (NOP) == (G)) { HOOK((v) >> 2, (v) & 3); if (((v) & 3) == 3 && (v) + 1 < (NOP)) { HOOK(((v) >> 2) + 1, -1); } } } while (0)
#define QGAP(s, ks, g, HOOK, NOP) do { if ((s) > 0) QM1((s) - 1, ks, g); else asm volatile("s_nop 0"); \
        if ((g) == 0) xf[(s) & 1][ks] = lds_rd128((xa ^ (32u * (ks))) + XOFF(s)); \
        wf[(s) & 1][g][ks] = cat8(lds_tr((wa ^ (64u * (g))) + WOFF(s) + (ks) * 4096u), lds_tr((wa ^ (64u * (g))) + WOFF(s) + (ks) * 4096u + 1024u)); \
        SBAR(); \
        QOP(HOOK, NOP, 4 * (ks) + (g), 0); QOP(HOOK, NOP, 4 * (ks) + (g), 1); QOP(HOOK, NOP, 4 * (ks) + (g), 2); QOP(HOOK, NOP, 4 * (ks) + (g), 3); \
        QOP(HOOK, NOP, 4 * (ks) + (g), 4); QOP(HOOK, NOP, 4 * (ks) + (g), 5); QOP(HOOK, NOP, 4 * (ks) + (g), 6); QOP(HOOK, NOP, 4 * (ks) + (g), 7); \
        QOP(HOOK, NOP, 4 * (ks) + (g), 8); QOP(HOOK, NOP, 4 * (ks) + (g), 9); \
        SBAR(); } while (0)
#define QKS(s, ks, HOOK, NOP) do { QGAP(s, ks, 0, HOOK, NOP); QGAP(s, ks, 1, HOOK, NOP); QGAP(s, ks, 2, HOOK, NOP); QGAP(s, ks, 3, HOOK, NOP); } while (0)
#define QCMP(s, HOOK, NOP) do { HOOK(0, -1); SBAR(); QKS(s, 0, HOOK, NOP); QKS(s, 1, HOOK, NOP); QKS(s, 2, HOOK, NOP); QKS(s, 3, HOOK, NOP); } while (0)
#define HQ0(j, g) QG(3, j, g)
#define HQ1(j, g) QG(4, j, g)
#define HQ2(j, g) QG(5, j, g)
#define HQ3(j, g) QG(6, j, g)
#define HQ4(j, g) QG(7, j, g)
#define HQ5(j, g) EG(0, j, g)
#define HQ6(j, g) EG(1, j, g)
#define HQ7(j, g) EG(2, j, g)
    QLD(0); QLD(1);
#pragma unroll
    for (int db = 0; db < 4; ++db) { o[db] = f32x16{}; asm volatile("" : "+a"(o[db])); }
    QLD(2);
    waitv<2 * QE>(); BARRIER(); QCMP(0, HQ0, QE);
    waitv<2 * QE>(); BARRIER(); QCMP(1, HQ1, QE);
    waitv<2 * QE>(); BARRIER(); QCMP(2, HQ2, QE);
    waitv<2 * QE>(); BARRIER(); QCMP(3, HQ3, QE);
    waitv<2 * QE>(); BARRIER(); QCMP(4, HQ4, QE);
    waitv<2 * QE>(); BARRIER(); QCMP(5, HQ5, PE);
    waitv<QE + PE>(); BARRIER(); QCMP(6, HQ6, PE);
    waitv<2 * PE>(); BARRIER(); QCMP(7, HQ7, PE);
    QM1(7, 0, 0); QM1(7, 1, 0); QM1(7, 2, 0); QM1(7, 3, 0); SBAR();
    QM1(7, 0, 1); QM1(7, 1, 1); QM1(7, 2, 1); QM1(7, 3, 1); asm volatile("s_nop 7" : "+v"(acc[0])); SBAR();
    QM1(7, 0, 2); QM1(7, 1, 2); QO(0); SBAR(); QM1(7, 2, 2); QM1(7, 3, 2); PIN(acc[1]); SBAR();
    QM1(7, 0, 3); QM1(7, 1, 3); QO(1); SBAR(); QM1(7, 2, 3); QM1(7, 3, 3); PIN(acc[2]); SBAR();
    QO(2); SBAR(); asm volatile("s_nop 3" : "+v"(acc[3])); QO(3); SBAR();
  }
  PIN(epoch_v);
  const u64 tag = mix64(((u64)rfl((unsigned)(epoch_v >> 32)) << 32) | rfl((unsigned)epoch_v));

  unsigned tA = 3u * ESZ + dsub, tB = 2u * ESZ + dsub;
  const unsigned kb = ring + l31 * 256u + ((hh ^ l15) * 16u);
  const unsigned vl = (4u * hh + ((lane >> 2) & 3u)) * 256u + (((lane >> 2) & 3u) << 6) + (g4 & 1u) * 32u + (lane & 3u) * 8u;
  const unsigned vb = ring + vl;
  f32x16 sA[2], sB[2], negm;
  bf16x8 kf[2][8]; s16x4 vlo[4][4], vhi[4][4]; u32x4 pw[2][2];
  float fsc = 1.f, ls0 = 0.f, lt0 = 0.f, ls1 = 0.f, lt1 = 0.f;
#define QK0(D, a, bq) asm volatile("v_mfma_f32_32x32x16_bf16 %0, %1, %2, 0" : "=&v"(D) : "v"(a), "a"(bq))
#define QKC(D, a, bq, C) asm volatile("v_mfma_f32_32x32x16_bf16 %0, %1, %2, %3" : "=&v"(D) : "v"(a), "a"(bq), "v"(C))
#define QKA(D, a, bq) asm volatile("v_mfma_f32_32x32x16_bf16 %0, %1, %2, %0" : "+v"(D) : "v"(a), "a"(bq))
#define WAITE(k) do { waitv<PE * (k)>(); BARRIER(); } while (0)
#define EXP2(x) __builtin_amdgcn_exp2f((x) * C2)
#define KRD(s) do { const unsigned a_ = ka ^ (32u * (s)); kf[0][s] = lds_rd128(a_); kf[1][s] = lds_rd128(a_ + 8192u); } while (0)

  WAITE(2);
  {
    const unsigned ka = kb;
#pragma unroll
    for (int s = 0; s < 8; ++s) KRD(s);
  }
  QK0(sA[0], kf[0][0], qf[0]); QK0(sA[1], kf[1][0], qf[0]);
#pragma unroll
  for (int s = 1; s < 8; ++s) { QKA(sA[0], kf[0][s], qf[s]); QKA(sA[1], kf[1][s], qf[s]); if (s == 2) ELEM(3); }
  WAITE(2);
  ELEM(4);
  {
    const unsigned ka = kb + ESZ;
#pragma unroll
    for (int s = 0; s < 8; ++s) KRD(s);
  }
  asm volatile("s_nop 7\n\ts_nop 7" : "+v"(sA[0]), "+v"(sA[1]));
  {
    const float m0 = swap_max(max2f(max8hi(max8lo(sA[0]), sA[0]), max8hi(max8lo(sA[1]), sA[1])));
#pragma unroll
    for (int r = 0; r < 16; ++r) { sA[0][r] -= m0; sA[1][r] -= m0; negm[r] = -m0; }
    PIN(negm);
#pragma unroll
    for (int r = 0; r < 8; ++r) { sA[0][r] = EXP2(sA[0][r]); sA[1][r] = EXP2(sA[1][r]); }
#pragma unroll
    for (int r = 0; r < 8; r += 2) { ls0 += sA[0][r]; lt0 += sA[0][r + 1]; ls1 += sA[1][r]; lt1 += sA[1][r + 1]; }
  }
  unsigned wvo[4];
  const srd_t sW = MKSRD((BB ? p.woB : p.woA) + (size_t)h0 * 131072u + wid * 256u, (BB ? 262144 : 524288) - 768);
  const unsigned wbuf = BB ? hreg + qb * 8192u : ldsb + ESZ + wid * 8192u;
  const unsigned bhi = b >> 2;
#define WSO(i) ((((i) + b) & 3u) * 65536u + ((((i) >> 2) & 1u) ^ bhi) * 128u)
#define WG(i, hi, g) DG(wbuf + (1u + ((i) & 3)) * ESZ + (hi) * 4096u, wvo, sW, WSO(i) + (hi) * 32768u, NT_WO, 4, g)
#define WOP(v, TA, TB_) do { if ((v) < 8) WG(TA, ((v) >> 2) & 1, (v) & 3); else WG(TB_, ((v) >> 2) & 1, (v) & 3); \
    if ((v) == 3) WG(TA, 1, -1); if ((v) == 7 && (TB_) >= 0) WG((TB_) & 7, 0, -1); if ((v) == 11) WG((TB_) & 7, 1, -1); } while (0)

#define VRD(db, k4) do { const unsigned a_ = (vb ^ (64u * (db))) + S2 + 4096u * (k4); vlo[db][k4] = lds_tr(a_); vhi[db][k4] = lds_tr(a_ + 2048u); } while (0)
  s16x4 ulo[4][4], uhi[4][4];
#define URD(db, k4) do { const unsigned a_ = (vb ^ (64u * (db))) + 4096u * (k4); ulo[db][k4] = lds_tr(a_); uhi[db][k4] = lds_tr(a_ + 2048u); } while (0)
#define GAPA(C, N, s, KB, LAST) do { \
    if ((s) == 0) QKC(N[KB], kf[KB][0], qf[0], negm); else QKA(N[KB], kf[KB][s], qf[s]); \
    if ((s) < 4) { float e0_ = EXP2(C[KB][(8 + 2 * (s)) & 15]), e1_ = EXP2(C[KB][(9 + 2 * (s)) & 15]); PIN(e0_); PIN(e1_); C[KB][(8 + 2 * (s)) & 15] = e0_; C[KB][(9 + 2 * (s)) & 15] = e1_; \
      pw[KB][0][(s) & 3] = cvtpk(C[KB][2 * (s)], C[KB][2 * (s) + 1]); PIN(pw[KB][0]); } \
    else { ls##KB += C[KB][2 * (s)]; lt##KB += C[KB][2 * (s) + 1]; pw[KB][1][(s) & 3] = cvtpk(C[KB][2 * (s)], C[KB][2 * (s) + 1]); PIN(pw[KB][1]); PIN(ls##KB); PIN(lt##KB); } \
    if ((s) < 4) VRD((2 * (s) + KB) & 3, VK4(((s) >> 1) & 1)); else if ((s) < 6) { VRD(2 * KB, VK4(((s) & 1) + 2)); VRD(2 * KB + 1, VK4(((s) & 1) + 2)); } \
    SBAR(); \
    if (LAST) { if (BB && KB == 1) WOP(s, 0, -1); } \
    else if (KB == 1 && (BB || (s) < 4)) { dma1<((s) & 3) * 1024, NTS>(kvo[(s) & 3], sK, RO(tA) + ((s) >> 2) * 4096u); if (BB && (s) == 3) SETM0(dring + S1 + 4096u); } \
    SBAR(); } while (0)
#define VK4(i) ((i) == 0 ? 0 : (i) == 1 ? 2 : (i) == 2 ? 1 : 3)
#define PHASE_A(C, N, ODD, L) do { \
    constexpr unsigned S1 = (ODD ? 3u : 1u) * ESZ, S2 = (ODD ? 0u : 2u) * ESZ; \
    if (!BB && ASLP > 0) __builtin_amdgcn_s_sleep(ASLP); \
    if (L) { if (BB) WG(0, 0, -1); } else SETM0(dring + S1); \
    WAITE(2); SBAR(); \
    GAPA(C, N, 0, 0, L); GAPA(C, N, 0, 1, L); GAPA(C, N, 1, 0, L); GAPA(C, N, 1, 1, L); GAPA(C, N, 2, 0, L); GAPA(C, N, 2, 1, L); GAPA(C, N, 3, 0, L); GAPA(C, N, 3, 1, L); \
    GAPA(C, N, 4, 0, L); GAPA(C, N, 4, 1, L); GAPA(C, N, 5, 0, L); GAPA(C, N, 5, 1, L); GAPA(C, N, 6, 0, L); GAPA(C, N, 6, 1, L); GAPA(C, N, 7, 0, L); GAPA(C, N, 7, 1, L); \
    tA += ESZ; } while (0)
#define PVM(i) o[(i) & 3] = MFMA32(cat8(vlo[(i) & 3][VK4((i) >> 2)], vhi[(i) & 3][VK4((i) >> 2)]), __builtin_bit_cast(bf16x8, pw[((i) >> 2) & 1][(i) >> 3]), o[(i) & 3])
#define DMAB(v, LAST) do { SBAR(); if (LAST) { if (BB && (v) < 8) WOP((v) & 7, 1, -1); } \
    else if (((v) & 1) == 0 && (BB || (v) < 8)) { dma1<(((v) >> 1) & 3) * 1024, NTS>(vvo, sV, RO(tB) + ((v) >> 3) * 4096u); if (BB && (v) == 6) SETM0(dring + S2 + 4096u); } SBAR(); } while (0)
#define GAPE(N, j, LAST) do { PVM(8 + (j)); N[0][j] = EXP2(N[0][j]); N[1][j] = EXP2(N[1][j]); \
    if ((j) > 0) { if ((j) & 1) { ls0 += N[0][((j) + 15) & 15]; ls1 += N[1][((j) + 15) & 15]; } else { lt0 += N[0][((j) + 15) & 15]; lt1 += N[1][((j) + 15) & 15]; } } \
    if (LAST) { URD((2 * (j)) & 3, VK4((j) >> 1)); URD((2 * (j) + 1) & 3, VK4((j) >> 1)); } \
    PIN(N[0]); PIN(N[1]); PIN(ls0); PIN(ls1); PIN(lt0); PIN(lt1); DMAB(8 + (j), LAST); } while (0)
#define PHASE_B(N, ODD, LAST) do { \
    constexpr unsigned S2 = (ODD ? 0u : 2u) * ESZ, S3 = (ODD ? 1u : 3u) * ESZ; \
    if (LAST) { if (BB) WG(1, 0, -1); BARRIER(); asm volatile("s_nop 7"); } else { SETM0(dring + S2); WAITE(2); } SBAR(); \
    const unsigned ka = kb + S3; \
    PVM(0); KRD(0); DMAB(0, LAST); \
    PVM(1); KRD(1); DMAB(1, LAST); \
    PVM(2); KRD(2); float ma0 = max3f(N[0][0], N[0][1], N[0][2]), ma1 = max3f(N[1][0], N[1][1], N[1][2]), mb0 = max3f(N[0][3], N[0][4], N[0][5]), mb1 = max3f(N[1][3], N[1][4], N[1][5]); DMAB(2, LAST); \
    PVM(3); KRD(3); ma0 = max3f(ma0, N[0][6], N[0][7]); ma1 = max3f(ma1, N[1][6], N[1][7]); mb0 = max3f(mb0, N[0][8], N[0][9]); mb1 = max3f(mb1, N[1][8], N[1][9]); DMAB(3, LAST); \
    PVM(4); KRD(4); ma0 = max3f(ma0, N[0][10], N[0][11]); ma1 = max3f(ma1, N[1][10], N[1][11]); mb0 = max3f(mb0, N[0][12], N[0][13]); mb1 = max3f(mb1, N[1][12], N[1][13]); DMAB(4, LAST); \
    PVM(5); ma0 = max3f(ma0, N[0][14], N[0][15]); ma1 = max3f(ma1, N[1][14], N[1][15]); const float mbb = max2f(mb0, mb1); KRD(5); DMAB(5, LAST); \
    PVM(6); const float mall = max3f(ma0, ma1, mbb); KRD(6); const bool grew = __any(mall > THR); DMAB(6, LAST); \
    PVM(7); KRD(7); DMAB(7, LAST); \
    if (__builtin_expect(grew, 0)) {     \
      const float d0 = max2f(swap_max(mall), 0.f); \
      fsc = EXP2(-d0); ls0 *= fsc; lt0 *= fsc; ls1 *= fsc; lt1 *= fsc; \
      sub_tile(N[0], d0); sub_tile(N[1], d0); sub_tile(negm, d0); \
    } \
    if (LAST) { waitv<BB ? 16 : 24>(); asm volatile("s_barrier" ::: "memory"); }     \
    SBAR(); \
    GAPE(N, 0, LAST); GAPE(N, 1, LAST); GAPE(N, 2, LAST); GAPE(N, 3, LAST); GAPE(N, 4, LAST); GAPE(N, 5, LAST); GAPE(N, 6, LAST); GAPE(N, 7, LAST); \
    lt0 += N[0][7]; lt1 += N[1][7]; \
    if (__builtin_expect(grew, 0)) { \
      asm volatile("s_nop 7\n\ts_nop 7" ::: "memory"); \
      scale_acc(o[0], fsc); scale_acc(o[1], fsc); scale_acc(o[2], fsc); scale_acc(o[3], fsc); \
    } \
    tB += ESZ; } while (0)

#define FLAG3() do { if (tid == 0u) __hip_atomic_store((GAS u64*)(p.ws + WS_FLAG3) + (size_t)blockIdx.x * 8u, tag, RLX_AGENT); } while (0)
  if (!BB) FLAG3();
  for (unsigned t = 0; t < 14u; t += 2u) {
    if (t == CPF1 && idx == 0u) {
      u64 pc0; unsigned clen;
      if constexpr (BB) asm volatile("s_getpc_b64 %0\n.Lk1_b0:\n\ts_mov_b32 %1, .Lk1_b1-.Lk1_b0" : "=s"(pc0), "=s"(clen));
      else              asm volatile("s_getpc_b64 %0\n.Lk1_a0:\n\ts_mov_b32 %1, .Lk1_a1-.Lk1_a0" : "=s"(pc0), "=s"(clen));
      const srd_t sC = MKSRD(pc0, (int)(clen <= 65536u ? (clen & ~3u) : 0u));
      TOUCH(sC, wid * 8192u);
    }
    PHASE_A(sA, sB, false, false); PHASE_B(sB, false, false);
    PHASE_A(sB, sA, true, false);  PHASE_B(sA, true, false);
  }
#pragma unroll
  for (int j = 0; j < 4; ++j) wvo[j] = BB ? (lane >> 3) * 1024u + (((lane & 7u) ^ ((g4 & 1u) << 2)) * 16u) + j * 7168u : g4 * 1024u + ((l15 ^ (g4 << 2)) * 16u) + j * 3072u;
  constexpr unsigned GPITCH16 = 16u * 1024u, GJ2 = 2048u;
  unsigned gidx = idx; asm volatile("" : "+s"(gidx));
  const srd_t gsr = sW; const unsigned (&gvo)[4] = wvo;
  if (BB) FLAG3();
  PHASE_A(sA, sB, false, true);
  if (!BB) { GD8(0); GD8(1); GD8(2); }
  PHASE_B(sB, false, true);
  bf16x8 wfr[2][8];
  const unsigned wb = wbuf + (4u * hh + q4) * 128u + (q4 >> 1) * 64u + (g4 & 1u) * 32u + p4 * 8u;
#define WRD(i, x) do { const unsigned a_ = (wb ^ (64u * ((x) & 1))) + (1u + ((i) & 3)) * ESZ + 2048u * ((x) >> 1); wfr[(i) & 1][x] = cat8(lds_tr(a_), lds_tr(a_ + 1024u)); } while (0)
  {
    PIN(tch);
#define PINA(x) asm volatile("" : "+a"(x))
#define UM(db, kb_, ks, v) do { o[db] = MFMA32(cat8(ulo[db][2 * (kb_) + (ks)], uhi[db][2 * (kb_) + (ks)]), __builtin_bit_cast(bf16x8, pw[kb_][ks]), o[db]); PINA(o[db]); SBAR(); \
      if (BB) WOP(v, 2, 3); SBAR(); } while (0)
#pragma unroll
    for (int i = 0; i < 4; ++i) { pw[0][0][i] = cvtpk(sB[0][2 * i], sB[0][2 * i + 1]); pw[1][0][i] = cvtpk(sB[1][2 * i], sB[1][2 * i + 1]); }
    if (BB) { WG(2, 0, -1); BARRIER(); }
    SBAR();
#define UG(x) do { UM((x) & 3, (x) >> 2, 0, x); float e0_ = EXP2(sB[(x) >> 2][8 + 2 * ((x) & 3)]), e1_ = EXP2(sB[(x) >> 2][9 + 2 * ((x) & 3)]); ls0 += e0_; ls1 += e1_; \
      pw[(x) >> 2][1][(x) & 3] = cvtpk(e0_, e1_); PIN(pw[(x) >> 2][1]); PIN(ls0); PIN(ls1); SBAR(); } while (0)
    UG(0); UG(1); UG(2); UG(3); UG(4); UG(5); UG(6); UG(7);
    float inv = __builtin_amdgcn_rcpf(swap_add((ls0 + lt0) + (ls1 + lt1))); PIN(inv);
#define OWR(db) do { const f32x16 y_ = o[db] * inv; lds_wru4(wbuf + (2 * (db)) * 1024u + lane * 16u, pack8(y_, 0)); lds_wru4(wbuf + (2 * (db) + 1) * 1024u + lane * 16u, pack8(y_, 1)); } while (0)
    UM(0, 0, 1, 8); UM(0, 1, 1, 9); UM(1, 0, 1, 10); UM(1, 1, 1, 11); UM(2, 0, 1, 12); UM(2, 1, 1, 13);
    OWR(0); SBAR();
    UM(3, 0, 1, 14); UM(3, 1, 1, 15);
    if (BB) {
      waitv<24>();
#pragma unroll
      for (int i = 0; i < 8; ++i) WRD(0, i);
    }
    OWR(1); OWR(2); OWR(3);
    BARRIER();
  }
  constexpr unsigned br = BB ? 1u : 0u;
  const srd_t sP = MKSRD(p.ws + WS_PART, 16 << 20);
  GAS u64* flags = (GAS u64*)(p.ws + WS_FLAG);
  const unsigned hp = BB ? (h0 >> 1) : g8;
  const unsigned fgrp = BB ? wid * 16u : (qb * 4u + wid) * 8u;
  if constexpr (!BB) {
    f32x16 acc[4]; bf16x8 gw[2][8], gb[2][2];
    const unsigned ga = gbuf + vl;
#define GB(i) do { const unsigned a_ = ldsb + ESZ + GT(i) * 2048u + lane * 16u; gb[(i) & 1][0] = lds_rd128(a_); gb[(i) & 1][1] = lds_rd128(a_ + 1024u); } while (0)
    waitv<16>();
    GFIRST();
    GTILE(0, 0); GTILE(1, 0); GTILE(2, 0); GTILE(3, 0); GTILE(4, 0); GTILE(5, 0); GTILE(6, 0); GTILE(7, 0);
    GTILE(8, 0); GTILE(9, 0); GTILE(10, 0); GTILE(11, 0); GTILE(12, 0); GTILE(13, 0); GTILE(14, 0); GTILE(15, 0);
    asm volatile("s_nop 7\n\ts_nop 7" : "+v"(acc[0]), "+v"(acc[1]), "+v"(acc[2]), "+v"(acc[3]));
    poll_flags<8>((GAS u64*)(p.ws + WS_FLAG3) + (size_t)(blockIdx.x - 128u) * 8u, tag);
    const unsigned pb = b * 1048576u + (qb * 32u + 8u * wid) * 16384u + hp * 1024u + lane * 16u;
#pragma unroll
    for (int i = 0; i < 8; ++i) __builtin_amdgcn_raw_buffer_store_b128(pack8(acc[i >> 1], i & 1), sP, pb + i * 16384u, 0, 16);
  } else {
    bf16x8 of[4][2][4];
#define ORD(i, x) do { const unsigned r_ = ((i) + b) & 3u; of[i][(x) & 1][(x) >> 1] = lds_rd128(ldsb + (r_ >> 1) * HREG + (r_ & 1u) * 4096u + lane * 16u + ((x) & 1) * 8192u + ((x) >> 1) * 1024u); } while (0)
    ORD(0, 0); ORD(0, 1); ORD(0, 2); ORD(0, 3); ORD(0, 4); ORD(0, 5); ORD(0, 6); ORD(0, 7);
    f32x16 acc[4][2];
    const f32x16 z16 = {};
    const unsigned pb = (br * 8u + b) * 1048576u + 8u * wid * 16384u + hp * 1024u + lane * 16u;
    const unsigned pb0 = pb + bhi * 65536u, pb1 = pb + (bhi ^ 1u) * 65536u;
#define PST(nb, q, s2) __builtin_amdgcn_raw_buffer_store_b128(pack8(acc[nb][q], s2), sP, (((nb) >> 1) ? pb1 : pb0) + ((q) * 32 + ((nb) & 1) * 2 + (s2)) * 16384u, 0, 16)
#define OM(i, x, q) do { acc[2 * ((i) >> 2) + ((x) & 1)][q] = MFMA32(wfr[(i) & 1][x], of[(i) & 3][q][(x) >> 1], (((i) & 3) == 0 && (x) < 2) ? z16 : acc[2 * ((i) >> 2) + ((x) & 1)][q]); \
      PINA(acc[2 * ((i) >> 2) + ((x) & 1)][q]); } while (0)
#define OX(i, x) do { \
      OM(i, x, 0); WRD((i) + 1, x); if ((i) < 3) ORD(((i) + 1) & 3, x); SBAR(); \
      if ((i) < 4) WOP(x, ((i) + 4) & 7, -1); \
      SBAR(); OM(i, x, 1); if ((i) == 5) PST((x) >> 2, ((x) >> 1) & 1, (x) & 1); SBAR(); } while (0)
#define OT(i, N) do { if ((i) < 4) WG(((i) + 4) & 7, 0, -1); waitv<N>(); if ((i) > 0) { WAITL(); } SBAR(); \
      OX(i, 0); OX(i, 1); OX(i, 2); OX(i, 3); OX(i, 4); OX(i, 5); OX(i, 6); OX(i, 7); } while (0)
    OT(0, 16); OT(1, 16); OT(2, 16); OT(3, 16); OT(4, 16); OT(5, 0);
#pragma unroll
    for (int i = 0; i < 8; ++i) WRD(7, (2 * i + (i >> 2)) & 7);
    SBAR();
#define OF(n, q, pn, pq) do { \
      OM(6, n, q); OM(6, 2 + (n), q); OM(6, 4 + (n), q); SBAR(); OM(6, 6 + (n), q); if ((pn) >= 0) PST(2 + ((pn) & 1), pq, 0); SBAR(); \
      OM(7, n, q); OM(7, 2 + (n), q); SBAR(); OM(7, 4 + (n), q); if ((pn) >= 0) PST(2 + ((pn) & 1), pq, 1); SBAR(); OM(7, 6 + (n), q); SBAR(); } while (0)
    OF(0, 0, -1, 0); OF(0, 1, 0, 0); OF(1, 0, 0, 1); OF(1, 1, 1, 0);
    PST(3, 1, 0); PST(3, 1, 1);
  }
  waitv<0>();
  if (lane == 0) __hip_atomic_store(flags + (size_t)((br * 8u + b) * 64u + fgrp + hp) * 8u, tag, RLX_AGENT);
  {
    constexpr int NP = BB ? 16 : 8;
    const unsigned u = BB ? hp * 4u + wid : qb * 32u + hp * 4u + wid;
    poll_flags<2>(flags + (size_t)((br * 8u + b) * 64u + (BB ? ((u >> 3) & 3u) * 16u + l15 : (u & 56u) + (lane & 7u))) * 8u, tag);
    const unsigned rb = (br * 8u + b) * 1048576u + u * 16384u + lane * 16u;
    u32x4 pv[NP];
#pragma unroll
    for (int j = 0; j < NP; ++j) pv[j] = __builtin_amdgcn_raw_buffer_load_b128(sP, rb + j * 1024u, 0, 16);
    float y[8] = {0.f, 0.f, 0.f, 0.f, 0.f, 0.f, 0.f, 0.f};
#pragma unroll
    for (int j = 0; j < NP; ++j)
#pragma unroll
      for (int e = 0; e < 4; ++e) { y[2 * e] += __uint_as_float(pv[j][e] << 16); y[2 * e + 1] += __uint_as_float(pv[j][e] & 0xffff0000u); }
    const auto r0 = __builtin_amdgcn_permlane32_swap(cvtpk(y[0], y[1]), cvtpk(y[4], y[5]), false, false);
    const auto r1 = __builtin_amdgcn_permlane32_swap(cvtpk(y[2], y[3]), cvtpk(y[6], y[7]), false, false);
    *(u32x4*)(p.out + (size_t)(b * 64u + (u >> 5) * 32u + l31) * 2048u + (br * 512u + (u & 31u) * 16u + 8u * hh) * 2u) = u32x4{r0[0], r1[0], r0[1], r1[1]};
    if (!BB && blockIdx.x == 128u && wid == 0u) {
#pragma unroll 1
      for (unsigned spins = 0; spins < (1u << 21); ++spins) {
        bool ok = true;
#pragma unroll
        for (int k = 0; k < 4; ++k) ok &= (__hip_atomic_load((GAS u64*)(p.ws + WS_FLAG3) + (size_t)(k * 64 + lane) * 8u, RLX_AGENT) == tag);
        if (__all(ok)) break;
        __builtin_amdgcn_s_sleep(8);
      }
      if (lane == 0) __hip_atomic_store((GAS u64*)(p.ws + WS_EPOCH), tag, RLX_AGENT);
    }
  }
  if constexpr (BB) asm volatile(".Lk1_b1:"); else asm volatile(".Lk1_a1:");
}

__global__ __launch_bounds__(256, 1) void k1_attn(P1 p) {
  extern __shared__ __attribute__((aligned(16))) char lds[];
  const unsigned ldsb = (unsigned)(unsigned long)lds;
  if (blockIdx.x < 128u) body<true>(p, ldsb); else body<false>(p, ldsb);
}

extern "C" void kernel_launch(void* const* d_in, const int* in_sizes, int n_in,
                              void* d_out, int out_size, void* d_ws, size_t ws_size,
                              hipStream_t stream) {
  (void)in_sizes; (void)n_in; (void)out_size; (void)ws_size;
  (void)hipFuncSetAttribute((const void*)k1_attn, hipFuncAttributeMaxDynamicSharedMemorySize, K1_LDS);
  P1 p1;
  p1.xA = (const char*)d_in[0]; p1.kA = (const char*)d_in[1]; p1.vA = (const char*)d_in[2]; p1.wqA = (const char*)d_in[3]; p1.woA = (const char*)d_in[4];
  p1.xB = (const char*)d_in[5]; p1.kB = (const char*)d_in[6]; p1.vB = (const char*)d_in[7]; p1.wqB = (const char*)d_in[8]; p1.woB = (const char*)d_in[9];
  p1.ws = (char*)d_ws; p1.out = (char*)d_out;
  hipLaunchKernelGGL(k1_attn, dim3(256), dim3(256), K1_LDS, stream, p1);
}
```
